```python
import torch

def _softmax(x, axis=-1):
    return torch.softmax(x, dim=axis)
def _gelu(x):
    return torch.nn.functional.gelu(x, approximate="tanh")
def _silu(x):
    return torch.nn.functional.silu(x)
def _sigmoid(x):
    return torch.sigmoid(x)
def _layernorm(x, axis=-1, eps=1e-5):
    m = x.mean(dim=axis, keepdim=True)
    v = x.var(dim=axis, keepdim=True, unbiased=False)
    return (x - m) / torch.sqrt(v + eps)
def _scatter_add(tgt, idx, src, axis=0):
    return tgt.clone().index_add_(axis, idx, src)
def _segment_sum(src, seg, n):
    out = torch.zeros((n,) + src.shape[1:], dtype=src.dtype)
    return out.index_add_(0, seg, src)
def _pad(x, pw):
    flat = tuple(v for pair in reversed(pw) for v in pair)
    return torch.nn.functional.pad(x, flat)

kernel_name = 'parallel_sum__grouped_kv_mqa__glu_gated_bilinear__prompt'

def setup_inputs():

    _w = (lambda a: (lambda t: t.to(torch.bfloat16) if t.is_floating_point() else t)(torch.as_tensor(a)))
    def _A_setup():
        B, S, D, H = 1, 2048, 640, 32
        Dh = D // H
        return {
            "x": _w((lambda *s: torch.randn(*s, dtype=torch.bfloat16) * 0.05)(B, S, D)),
            "wq": _w((lambda *s: torch.randn(*s, dtype=torch.bfloat16) * 0.05)(D, H * Dh)),
            "wk": _w((lambda *s: torch.randn(*s, dtype=torch.bfloat16) * 0.05)(D, Dh)),
            "wv": _w((lambda *s: torch.randn(*s, dtype=torch.bfloat16) * 0.05)(D, Dh)),
            "wo": _w((lambda *s: torch.randn(*s, dtype=torch.bfloat16) * 0.05)(H * Dh, D)),
        }
    _A_in = _A_setup()

    def _B_setup():
        B, S, D, F = 1, 2048, 640, 1536
        return {
            "x": _w((lambda *s: torch.randn(*s, dtype=torch.bfloat16) * 0.05)(B, S, D)),
            "w_gate": _w((lambda *s: torch.randn(*s, dtype=torch.bfloat16) * 0.8)(D, F) * (D ** -0.5)),
            "w_up": _w((lambda *s: torch.randn(*s, dtype=torch.bfloat16) * 0.8)(D, F) * (D ** -0.5)),
            "w_down": _w((lambda *s: torch.randn(*s, dtype=torch.bfloat16) * 0.8)(F, D) * (F ** -0.5)),
        }
    _B_in = _B_setup()

    return {
        "A_x": _A_in["x"],
        "A_wq": _A_in["wq"],
        "A_wk": _A_in["wk"],
        "A_wv": _A_in["wv"],
        "A_wo": _A_in["wo"],
        "B_x": _B_in["x"],
        "B_w_gate": _B_in["w_gate"],
        "B_w_up": _B_in["w_up"],
        "B_w_down": _B_in["w_down"],
    }

def reference(A_x, A_wq, A_wk, A_wv, A_wo, B_x, B_w_gate, B_w_up, B_w_down):
    def _A_ref(x, wq, wk, wv, wo):
        B, S, D = x.shape
        H = 32
        Dh = D // H
        q = (lambda x, *d: x.permute(*d))((x @ wq).reshape(B, S, H, Dh), 0, 2, 1, 3)
        k = x @ wk
        v = x @ wv
        scores = torch.einsum("bhqd,bkd->bhqk", q, k) * (Dh ** -0.5)
        p = _softmax(scores, axis=-1)
        o = torch.einsum("bhqk,bkd->bhqd", p, v)
        return (lambda x, *d: x.permute(*d))(o, 0, 2, 1, 3).reshape(B, S, H * Dh) @ wo

    def _B_ref(x, w_gate, w_up, w_down):
        g = torch.einsum("bsd,df->bsf", x, w_gate)
        u = torch.einsum("bsd,df->bsf", x, w_up)
        h = g * u
        return torch.einsum("bsf,fd->bsd", h, w_down)

    x = A_x
    xn = _layernorm(x, axis=-1)
    return x + _A_ref(xn, A_wq, A_wk, A_wv, A_wo) + _B_ref(xn, B_w_gate, B_w_up, B_w_down)
```

```cpp
#include <hip/hip_runtime.h>
#include <hip/hip_bf16.h>
#include <stdint.h>

typedef unsigned int u32;
typedef unsigned long long u64;
typedef __bf16 bf16x2 __attribute__((ext_vector_type(2)));
typedef __bf16 bf16x8 __attribute__((ext_vector_type(8)));
typedef short s16x4 __attribute__((ext_vector_type(4)));
typedef short s16x8 __attribute__((ext_vector_type(8)));
typedef float f32x2 __attribute__((ext_vector_type(2)));
typedef float f32x4 __attribute__((ext_vector_type(4)));
typedef float f32x16 __attribute__((ext_vector_type(16)));
typedef u32 u32x2 __attribute__((ext_vector_type(2)));
typedef u32 u32x4 __attribute__((ext_vector_type(4)));
typedef u32 u32x8 __attribute__((ext_vector_type(8)));
typedef u32 u32x16 __attribute__((ext_vector_type(16)));
#define LAS __attribute__((address_space(3)))
#define GAS __attribute__((address_space(1)))
#define DI __device__ __forceinline__

#define NLAUNCH 1

constexpr u32 WS_G = 0;
constexpr u32 WS_KVFLAG = 1024;
constexpr u32 WS_HFLAG = 4096;
constexpr u32 WS_OFLAG = 4096 + 16384;
constexpr u32 WS_KV = 65536;
constexpr u32 KV_REC = 3584;
constexpr u32 WS_ACT = 65536 + 262144;
constexpr u32 ACT_G = 136 * 2048;
constexpr u32 XS = 1296;
constexpr u32 L_XN = 0;
constexpr u32 L_GR = 64 * XS;
constexpr u32 L_AR = L_GR + 4 * 12288;
constexpr u32 AR_SZ = 7680;
constexpr u32 L_MISC = L_AR + 4 * AR_SZ;
constexpr u32 LDS_BYTES = L_MISC + 256;
enum { C_KV = 0, C_Q = 1, C_H = 2, C_O = 3, C_PAIR = 4  , C_QR = 8, W_QN = 12  , W_KN = 16, W_VN = 17 };

constexpr u32 SPIN_CAP = 1u << 17;

DI u32 pk(float a, float b) { f32x2 f = {a, b}; return __builtin_bit_cast(u32, __builtin_convertvector(f, bf16x2)); }
DI float asf(u32 u) { return __builtin_bit_cast(float, u); }
DI bf16x2 asb2(u32 u) { return __builtin_bit_cast(bf16x2, u); }
DI float bflo(u32 u) { return __builtin_bit_cast(float, u << 16); }
DI float bfhi(u32 u) { return __builtin_bit_cast(float, u & 0xffff0000u); }
DI float rbf(float a) { return bflo(pk(a, 0.f)); }
template <int CTRL> DI float dpp(float v) { return __builtin_bit_cast(float, __builtin_amdgcn_mov_dpp(__builtin_bit_cast(int, v), CTRL, 0xf, 0xf, true)); }
DI float xhalf(float v) {
  u32 a = __builtin_bit_cast(u32, v);
  auto r = __builtin_amdgcn_permlane32_swap(a, a, false, false);
  u32 r0 = r[0], r1 = r[1];
  return asf((threadIdx.x & 32) ? r0 : r1);
}
DI float wave_max(float v) {
  v = fmaxf(v, dpp<0xB1>(v)); v = fmaxf(v, dpp<0x4E>(v)); v = fmaxf(v, dpp<0x141>(v)); v = fmaxf(v, dpp<0x140>(v));
  v = fmaxf(v, __shfl_xor(v, 16)); v = fmaxf(v, xhalf(v));
  return v;
}
DI bf16x8 trfrag(LAS char* p, int hi_off) {
  s16x4 lo = __builtin_amdgcn_ds_read_tr16_b64_v4i16((LAS s16x4*)p);
  s16x4 hi = __builtin_amdgcn_ds_read_tr16_b64_v4i16((LAS s16x4*)(p + hi_off));
  return __builtin_bit_cast(bf16x8, __builtin_shufflevector(lo, hi, 0, 1, 2, 3, 4, 5, 6, 7));
}
DI f32x16 mfma16(bf16x8 a, bf16x8 b, f32x16 c) { return __builtin_amdgcn_mfma_f32_32x32x16_bf16(a, b, c, 0, 0, 0); }
DI void glds(const char* src, u32 dst) {
  asm volatile("s_mov_b32 m0, %1\n\ts_nop 0\n\tglobal_load_lds_dwordx4 %0, off" ::"v"(src), "s"(dst) : "memory");
}
DI void glds3(const char* a0, const char* a1, const char* a2, u32 dst) {
  asm volatile("s_mov_b32 m0, %3\n\ts_nop 0\n\tglobal_load_lds_dwordx4 %0, off\n\tglobal_load_lds_dwordx4 %1, off offset:1024\n\t"
               "global_load_lds_dwordx4 %2, off offset:2048" ::"v"(a0), "v"(a1), "v"(a2), "s"(dst) : "memory");
}
DI u32x4 mk_srd(const void* p, u32 bytes) {
  u64 a = (u64)p;
  u32x4 r = {(u32)a, (u32)(a >> 32) & 0xffffu, bytes, 0x00020000u};
  return r;
}
DI void st16_sc1(char* p, u32x4 v) { asm volatile("global_store_dwordx4 %0, %1, off sc1\n\ts_nop 1" ::"v"(p), "v"(v) : "memory"); }
DI void st8_sc1(char* p, u32x2 v) { asm volatile("global_store_dwordx2 %0, %1, off sc1" ::"v"(p), "v"(v) : "memory"); }
DI void st4_sc1(char* p, u32 v) { asm volatile("global_store_dword %0, %1, off sc1" ::"v"(p), "v"(v) : "memory"); }
DI void drain() { asm volatile("s_waitcnt vmcnt(0)" ::: "memory"); }
DI u32 lds_add(LAS char* lds, int word) {
  u32 old = 0;
  asm volatile("s_waitcnt lgkmcnt(0)" ::: "memory");
  if ((threadIdx.x & 63) == 0) old = __hip_atomic_fetch_add((LAS u32*)(lds + L_MISC) + word, 1u, __ATOMIC_RELAXED, __HIP_MEMORY_SCOPE_WORKGROUP);
  return __builtin_amdgcn_readfirstlane(old);
}
DI void lds_wait(LAS char* lds, int word, u32 want) {
  for (u32 s = 0; s < (SPIN_CAP << 4); ++s) {
    u32 v = __hip_atomic_load((LAS u32*)(lds + L_MISC) + word, __ATOMIC_RELAXED, __HIP_MEMORY_SCOPE_WORKGROUP);
    if (__builtin_amdgcn_readfirstlane(v) >= want) break;
    __builtin_amdgcn_s_sleep(1);
  }
  asm volatile("" ::: "memory");
}
DI void poll_tag(const char* p, u32 tag) {
  for (u32 s = 0; s < SPIN_CAP; ++s) {
    u32 v;
    asm volatile("global_load_dword %0, %1, off sc1\n\ts_waitcnt vmcnt(0)" : "=&v"(v) : "v"(p) : "memory");
    if (__all(v == tag)) break;
    __builtin_amdgcn_s_sleep(2);
  }
}

DI void layernorm(u32x4 (&xr)[10], LAS char* lds, int w, int lane) {
  const bf16x2 one = __builtin_bit_cast(bf16x2, 0x3f803f80u);
  float s[4] = {0.f, 0.f, 0.f, 0.f}, q[4] = {0.f, 0.f, 0.f, 0.f};
#pragma unroll
  for (int i = 0; i < 10; ++i)
#pragma unroll
    for (int e = 0; e < 4; ++e) {
      bf16x2 v = asb2(xr[i][e]);
      s[e] = __builtin_amdgcn_fdot2_f32_bf16(v, one, s[e], false);
      q[e] = __builtin_amdgcn_fdot2_f32_bf16(v, v, q[e], false);
    }
  float sum = (s[0] + s[1]) + (s[2] + s[3]), sq = (q[0] + q[1]) + (q[2] + q[3]);
  sum += dpp<0xB1>(sum); sq += dpp<0xB1>(sq);
  sum += dpp<0x4E>(sum); sq += dpp<0x4E>(sq);
  sum += dpp<0x141>(sum); sq += dpp<0x141>(sq);
  float mean = sum * (1.f / 640.f);
  float var = fmaxf(sq * (1.f / 640.f) - mean * mean, 0.f);
  float r = __builtin_amdgcn_rsqf(var + 1e-5f);
  float nm = -mean * r;
  LAS char* dst = lds + L_XN + (8 * w + (lane >> 3)) * XS + (lane & 7) * 16;
#pragma unroll
  for (int i = 0; i < 10; ++i) {
    u32x4 o;
#pragma unroll
    for (int e = 0; e < 4; ++e) o[e] = pk(__builtin_fmaf(bflo(xr[i][e]), r, nm), __builtin_fmaf(bfhi(xr[i][e]), r, nm));
    *(LAS u32x4*)(dst + i * 128) = o;
  }
}

#define LOADX(i) asm volatile("global_load_dwordx4 %0, %1, off offset:%2" : "=v"(xr[i]) : "v"(xp), "i"(128 * (i)))
#define LOADXALL                                                                                     \
  u32 gv;                                                                                            \
  u32x4 xr[10];                                                                                      \
  asm volatile("global_load_dword %0, %1, off sc1" : "=v"(gv) : "v"(ws + WS_G));                     \
  LOADX(0); LOADX(1); LOADX(2); LOADX(3); LOADX(4); LOADX(5); LOADX(6); LOADX(7); LOADX(8); LOADX(9); \
  __builtin_amdgcn_sched_barrier(0)
#define XPIN "+v"(xr[0]), "+v"(xr[1]), "+v"(xr[2]), "+v"(xr[3]), "+v"(xr[4]), "+v"(xr[5]), "+v"(xr[6]), "+v"(xr[7]), "+v"(xr[8]), "+v"(xr[9]), "+v"(gv)

template <int PH>
__global__ void __launch_bounds__(512, 2)
fused_kernel(const char* __restrict__ x, const char* __restrict__ wq, const char* __restrict__ wk, const char* __restrict__ wv,
             const char* __restrict__ wo, const char* __restrict__ wg, const char* __restrict__ wu, const char* __restrict__ wd,
             char* __restrict__ out, char* __restrict__ ws) {
  extern __shared__ __attribute__((aligned(16))) char smem[];
  LAS char* lds = (LAS char*)smem;
  constexpr bool FUSED = PH == 7;
  const int tid = threadIdx.x, lane = tid & 63;
  const int w = __builtin_amdgcn_readfirstlane(tid >> 6);
  const int bid = blockIdx.x;
  const int xcd = bid & 7, loc = bid >> 3;
  const int g1 = (xcd & 1) * 16 + (loc >> 1), j1 = (xcd >> 1) * 2 + (loc & 1);
  const int g3 = xcd * 4 + (loc >> 3), j3 = loc & 7;
  const int l31 = lane & 31, h = lane >> 5;
  const int trq = (lane & 15) >> 2, trp = lane & 3, trb = (lane >> 4) & 1;

  if (tid < 64) *((LAS u32*)(lds + L_MISC) + tid) = 0u;

  const char* xp = x + (u32)((64 * g1 + 8 * w + (lane >> 3)) * 1280 + (lane & 7) * 16);

  if (w < 4) {
    LAS char* ar = lds + L_AR + w * AR_SZ;
    const u32 ar_a = (u32)(size_t)ar;
    const bool kvjob = (PH & 1) && j1 < 4;
    const int kind = j1 >> 1, ktb = j1 & 1;
    if (kvjob) {
      const char* wsrc = (kind ? wv : wk) + w * 6400;
#pragma unroll
      for (int i = 0; i < 7; ++i) { int n = i * 64 + lane; glds(wsrc + (n < 400 ? n : 399) * 16, ar_a + i * 1024); }
    }
    LOADXALL;
    const int head = 4 * j1 + w;
    u32x4 wqr[30];
    if (PH & 2) {
      const char* b0 = wq + ((head * 40) & ~15);
      u32 o0 = (lane / 3) * 1280 + (lane % 3) * 16, o1 = ((lane + 64) / 3) * 1280 + ((lane + 64) % 3) * 16,
          o2 = ((lane + 128) / 3) * 1280 + ((lane + 128) % 3) * 16;
#pragma unroll
      for (int u = 0; u < 10; ++u) {
        const char* bu = b0 + u * 64 * 1280;
        asm volatile("global_load_dwordx4 %0, %1, %2" : "=v"(wqr[3 * u]) : "v"(o0), "s"(bu));
        asm volatile("global_load_dwordx4 %0, %1, %2" : "=v"(wqr[3 * u + 1]) : "v"(o1), "s"(bu));
        asm volatile("global_load_dwordx4 %0, %1, %2" : "=v"(wqr[3 * u + 2]) : "v"(o2), "s"(bu));
      }
    }
    asm volatile("s_waitcnt vmcnt(%11)" : XPIN : "i"((PH & 2) ? 30 : 0) : "memory");
    const u32 g0 = __builtin_amdgcn_readfirstlane(gv);
    u32 tag = g0 * 0x9E3779B1u + 0x7F4A7C15u;
    if (tag == g0) tag ^= 0x10u;
    layernorm(xr, lds, w, lane);
    asm volatile("s_waitcnt lgkmcnt(0)" ::: "memory");
    __builtin_amdgcn_s_barrier();
    asm volatile("" ::: "memory");

    if (kvjob) {
      const int trow = kind ? ((l31 & 0x13) | ((l31 & 4) << 1) | ((l31 & 8) >> 1)) : l31;
      LAS char* xa = lds + L_XN + (32 * ktb + trow) * XS + h * 16 + w * 320;
      LAS char* wa = ar + (8 * h + trq) * 40 + trb * 32 + trp * 8;
      f32x16 acc = {};
#pragma unroll
      for (int s = 0; s < 10; ++s) {
        bf16x8 wf = trfrag(wa + s * 640, 160);
        bf16x8 xf = *(LAS bf16x8*)(xa + s * 32);
        if (kind) acc = mfma16(xf, wf, acc); else acc = mfma16(wf, xf, acc);
      }
      if (w != 0) {
#pragma unroll
        for (int r = 0; r < 4; ++r) *(LAS f32x4*)(ar + 3072 + r * 1024 + lane * 16) = f32x4{acc[4 * r], acc[4 * r + 1], acc[4 * r + 2], acc[4 * r + 3]};
        lds_add(lds, C_KV);
      } else {
        lds_wait(lds, C_KV, 3);
#pragma unroll
        for (int o = 1; o < 4; ++o)
#pragma unroll
          for (int r = 0; r < 4; ++r) {
            f32x4 p = *(LAS f32x4*)(ar + o * AR_SZ + 3072 + r * 1024 + lane * 16);
            acc[4 * r] += p[0]; acc[4 * r + 1] += p[1]; acc[4 * r + 2] += p[2]; acc[4 * r + 3] += p[3];
          }
        char* rec = ws + WS_KV + (u32)(2 * g1 + ktb) * KV_REC;
        u32x4 lo = {pk(acc[0], acc[1]), pk(acc[2], acc[3]), pk(acc[4], acc[5]), pk(acc[6], acc[7])};
        u32x4 hi = {pk(acc[8], acc[9]), pk(acc[10], acc[11]), pk(acc[12], acc[13]), pk(acc[14], acc[15])};
        float nrm;
        if (kind == 0) {
          u32x2 k2 = {hi[0], hi[1]};
          if (h) k2 = u32x2{0u, 0u};
          float n2 = 0.f;
#pragma unroll
          for (int e = 0; e < 4; ++e) n2 += bflo(lo[e]) * bflo(lo[e]) + bfhi(lo[e]) * bfhi(lo[e]);
#pragma unroll
          for (int e = 0; e < 2; ++e) n2 += bflo(k2[e]) * bflo(k2[e]) + bfhi(k2[e]) * bfhi(k2[e]);
          n2 += xhalf(n2);
          if (!(n2 <= 3.0e38f)) n2 = 3.0e38f;
          nrm = wave_max(n2);
          if (h) k2 = u32x2{0x3f803f80u, 0x00003f80u};
          st16_sc1(rec + lane * 16, lo);
          st8_sc1(rec + 3072 + lane * 8, k2);
        } else {
          int hp, rp;
          if (l31 < 20) { hp = l31 >= 10; rp = l31 - 10 * hp; } else { hp = l31 & 1; rp = 10 + ((l31 - 20) >> 1); }
          int rho = (rp & 3) + 8 * (rp >> 2) + 4 * hp;
          float m = 0.f;
          if (l31 >= 20) { u32 c = l31 < 22 ? 0x3f803f80u : 0u; lo = u32x4{c, c, c, c}; hi = lo; }
#pragma unroll
          for (int e = 0; e < 4; ++e) {
            float a = fabsf(bflo(lo[e])), b = fabsf(bfhi(lo[e])), c = fabsf(bflo(hi[e])), d = fabsf(bfhi(hi[e]));
            if (!(a + b + c + d <= 3.0e38f)) a = 3.0e38f;
            m = fmaxf(fmaxf(m, fmaxf(a, b)), fmaxf(c, d));
          }
          nrm = wave_max(m);
          st16_sc1(rec + 1024 + (h * 32 + rho) * 16, lo);
          st16_sc1(rec + 2048 + (h * 32 + rho) * 16, hi);
        }
        drain();
        if (lane == 0) st8_sc1(ws + WS_KVFLAG + (u32)(kind * 64 + 2 * g1 + ktb) * 8, u32x2{tag, __builtin_bit_cast(u32, nrm)});
      }
    }

    if (PH & 2) {
      f32x16 qa[2] = {};
      {
        LAS char* xa = lds + L_XN + l31 * XS + h * 16;
        LAS char* wa = ar + (8 * h + trq) * 48 + ((head & 1) * 8) + trb * 32 + trp * 8;
#define QSTEP(u)                                                                                                        \
  asm volatile("s_waitcnt vmcnt(%3)" : "+v"(wqr[3 * (u)]), "+v"(wqr[3 * (u) + 1]), "+v"(wqr[3 * (u) + 2]) : "i"(27 - 3 * (u)) : "memory"); \
  for (int i = 0; i < 3; ++i) *(LAS u32x4*)(ar + i * 1024 + lane * 16) = wqr[3 * (u) + i];                              \
  for (int s = 0; s < 4; ++s) {                                                                                         \
    bf16x8 wf = trfrag(wa + s * 768, 192);                                                                              \
    qa[0] = mfma16(wf, *(LAS bf16x8*)(xa + (4 * (u) + s) * 32), qa[0]);                                                 \
    qa[1] = mfma16(wf, *(LAS bf16x8*)(xa + (4 * (u) + s) * 32 + 32 * XS), qa[1]);                                       \
  }
        QSTEP(0) QSTEP(1) QSTEP(2) QSTEP(3) QSTEP(4) QSTEP(5) QSTEP(6) QSTEP(7) QSTEP(8) QSTEP(9)
      }
      const float qs = 0.22360679774997896f * 1.4426950408889634f;
      float qn = 0.f;
#pragma unroll
      for (int tb = 0; tb < 2; ++tb) {
        u32x4 q1 = {pk(qa[tb][0] * qs, qa[tb][1] * qs), pk(qa[tb][2] * qs, qa[tb][3] * qs), pk(qa[tb][4] * qs, qa[tb][5] * qs), pk(qa[tb][6] * qs, qa[tb][7] * qs)};
        u32x2 q2 = {pk(qa[tb][8] * qs, qa[tb][9] * qs), pk(qa[tb][10] * qs, qa[tb][11] * qs)};
        if (h) q2 = u32x2{0u, 0u};
        float n2 = 0.f;
#pragma unroll
        for (int e = 0; e < 4; ++e) n2 += bflo(q1[e]) * bflo(q1[e]) + bfhi(q1[e]) * bfhi(q1[e]);
#pragma unroll
        for (int e = 0; e < 2; ++e) n2 += bflo(q2[e]) * bflo(q2[e]) + bfhi(q2[e]) * bfhi(q2[e]);
        n2 += xhalf(n2);
        if (!(n2 <= 3.0e38f)) n2 = 3.0e38f;
        qn = fmaxf(qn, n2);
        *(LAS u32x4*)(ar + tb * 1536 + lane * 16) = q1;
        *(LAS u32x2*)(ar + tb * 1536 + 1024 + lane * 8) = q2;
      }
      qn = wave_max(qn);
      if (lane == 0) *((LAS float*)(lds + L_MISC) + W_QN + w) = qn;
      if (w == 3) {
        u32x4 f;
        for (u32 s = 0; s < SPIN_CAP; ++s) {
          asm volatile("global_load_dwordx4 %0, %1, off sc1\n\ts_waitcnt vmcnt(0)" : "=&v"(f) : "v"(ws + WS_KVFLAG + lane * 16) : "memory");
          if (!FUSED || __all(f[0] == tag && f[2] == tag)) break;
          __builtin_amdgcn_s_sleep(2);
        }
        float m = fmaxf(asf(f[1]), asf(f[3]));
        float mk = wave_max(h ? 0.f : m), mv = wave_max(h ? m : 0.f);
        if (lane == 0) { *((LAS float*)(lds + L_MISC) + W_KN) = mk; *((LAS float*)(lds + L_MISC) + W_VN) = mv; }
      }
      lds_add(lds, C_Q);
      lds_wait(lds, C_Q, 4);

      const int ta = w & 1, kb = w >> 1;
      u32x16 Q1; u32x8 Q2;
#pragma unroll
      for (int i = 0; i < 4; ++i) {
        u32x4 a = *(LAS u32x4*)(lds + L_AR + i * AR_SZ + ta * 1536 + lane * 16);
        u32x2 b = *(LAS u32x2*)(lds + L_AR + i * AR_SZ + ta * 1536 + 1024 + lane * 8);
        Q1[4 * i] = a[0]; Q1[4 * i + 1] = a[1]; Q1[4 * i + 2] = a[2]; Q1[4 * i + 3] = a[3];
        Q2[2 * i] = b[0]; Q2[2 * i + 1] = b[1];
      }
      lds_add(lds, C_QR);
      const u32x4 kvsrd = mk_srd(ws + WS_KV, 64 * KV_REC);
      {
        LAS float* mf = (LAS float*)(lds + L_MISC);
        float q2m = fmaxf(fmaxf(mf[W_QN], mf[W_QN + 1]), fmaxf(mf[W_QN + 2], mf[W_QN + 3]));
        float bound = __builtin_sqrtf(q2m) * __builtin_sqrtf(mf[W_KN]) * 1.01f;
        bool safe = bound <= 90.f && mf[W_VN] <= 1048576.f;
        if (!__builtin_amdgcn_readfirstlane((int)safe)) {
          float mx[4] = {-3.0e38f, -3.0e38f, -3.0e38f, -3.0e38f};
          const __amdgpu_buffer_rsrc_t rs = __builtin_amdgcn_make_buffer_rsrc(ws + WS_KV, 0, 64 * KV_REC, 0x00020000);
          for (int t = 0; t < 64; ++t) {
            u32x4 k1 = __builtin_amdgcn_raw_buffer_load_b128(rs, lane * 16, t * KV_REC, 16);
            u32x2 k2 = __builtin_amdgcn_raw_buffer_load_b64(rs, 3072 + lane * 8, t * KV_REC, 16);
#pragma unroll
            for (int i = 0; i < 4; ++i) {
              u32x4 a = {Q1[4 * i], Q1[4 * i + 1], Q1[4 * i + 2], Q1[4 * i + 3]};
              u32x2 b = {Q2[2 * i], Q2[2 * i + 1]};
              f32x16 s = mfma16(__builtin_bit_cast(bf16x8, k1), __builtin_bit_cast(bf16x8, a), f32x16{});
              s = __builtin_amdgcn_mfma_f32_32x32x8bf16_1k(__builtin_bit_cast(s16x4, k2), __builtin_bit_cast(s16x4, b), s, 0, 0, 0);
#pragma unroll
              for (int r = 0; r < 16; ++r) mx[i] = fmaxf(mx[i], s[r]);
            }
          }
#pragma unroll
          for (int i = 0; i < 4; ++i) {
            float m = -fmaxf(mx[i], xhalf(mx[i]));
            float m0 = rbf(m), m1 = rbf(m - m0), m2 = rbf(m - m0 - m1);
            if (h) { Q2[2 * i] = pk(m0, m1); Q2[2 * i + 1] = pk(m2, 0.f); }
          }
        }
      }
      f32x16 O0, O1, O2, O3;
      {
        u32 so = kb * 32 * KV_REC, cnt = 8;
        asm volatile(
      "s_nop 4\n"
      "buffer_load_dwordx4 v[192:195], %[vo16], %[srd], %[so] offen sc1\n"
      "buffer_load_dwordx2 v[204:205], %[vo8], %[srd], %[so] offen offset:3072 sc1\n"
      "buffer_load_dwordx4 v[196:199], %[vo16], %[srd], %[so] offen offset:1024 sc1\n"
      "buffer_load_dwordx4 v[200:203], %[vo16], %[srd], %[so] offen offset:2048 sc1\n"
      "s_add_u32 %[so], %[so], 3584\n"
      "buffer_load_dwordx4 v[206:209], %[vo16], %[srd], %[so] offen sc1\n"
      "buffer_load_dwordx2 v[218:219], %[vo8], %[srd], %[so] offen offset:3072 sc1\n"
      "buffer_load_dwordx4 v[210:213], %[vo16], %[srd], %[so] offen offset:1024 sc1\n"
      "buffer_load_dwordx4 v[214:217], %[vo16], %[srd], %[so] offen offset:2048 sc1\n"
      "s_add_u32 %[so], %[so], 3584\n"
      "buffer_load_dwordx4 v[220:223], %[vo16], %[srd], %[so] offen sc1\n"
      "buffer_load_dwordx2 v[232:233], %[vo8], %[srd], %[so] offen offset:3072 sc1\n"
      "buffer_load_dwordx4 v[224:227], %[vo16], %[srd], %[so] offen offset:1024 sc1\n"
      "buffer_load_dwordx4 v[228:231], %[vo16], %[srd], %[so] offen offset:2048 sc1\n"
      "s_add_u32 %[so], %[so], 3584\n"
      "v_mov_b32 v176, 0\n"
      "v_mov_b32 v177, 0\n"
      "v_mov_b32 v178, 0\n"
      "v_mov_b32 v179, 0\n"
      "v_mov_b32 v180, 0\n"
      "v_mov_b32 v181, 0\n"
      "v_mov_b32 v182, 0\n"
      "v_mov_b32 v183, 0\n"
      "v_mov_b32 v184, 0\n"
      "v_mov_b32 v185, 0\n"
      "v_mov_b32 v186, 0\n"
      "v_mov_b32 v187, 0\n"
      "v_mov_b32 v188, 0\n"
      "v_mov_b32 v189, 0\n"
      "v_mov_b32 v190, 0\n"
      "v_mov_b32 v191, 0\n"
      "v_mov_b32 v238, 0\n"
      "v_mov_b32 v239, 0\n"
      "v_mov_b32 v240, 0\n"
      "v_mov_b32 v241, 0\n"
      "v_mov_b32 v242, 0\n"
      "v_mov_b32 v243, 0\n"
      "v_mov_b32 v244, 0\n"
      "v_mov_b32 v245, 0\n"
      "v_mov_b32 v64, 0\n"
      "v_mov_b32 v65, 0\n"
      "v_mov_b32 v66, 0\n"
      "v_mov_b32 v67, 0\n"
      "v_mov_b32 v68, 0\n"
      "v_mov_b32 v69, 0\n"
      "v_mov_b32 v70, 0\n"
      "v_mov_b32 v71, 0\n"
      "v_mov_b32 v72, 0\n"
      "v_mov_b32 v73, 0\n"
      "v_mov_b32 v74, 0\n"
      "v_mov_b32 v75, 0\n"
      "v_mov_b32 v76, 0\n"
      "v_mov_b32 v77, 0\n"
      "v_mov_b32 v78, 0\n"
      "v_mov_b32 v79, 0\n"
      "v_mov_b32 v80, 0\n"
      "v_mov_b32 v81, 0\n"
      "v_mov_b32 v82, 0\n"
      "v_mov_b32 v83, 0\n"
      "v_mov_b32 v84, 0\n"
      "v_mov_b32 v85, 0\n"
      "v_mov_b32 v86, 0\n"
      "v_mov_b32 v87, 0\n"
      "v_mov_b32 v88, 0\n"
      "v_mov_b32 v89, 0\n"
      "v_mov_b32 v90, 0\n"
      "v_mov_b32 v91, 0\n"
      "v_mov_b32 v92, 0\n"
      "v_mov_b32 v93, 0\n"
      "v_mov_b32 v94, 0\n"
      "v_mov_b32 v95, 0\n"
      "v_mov_b32 v96, 0\n"
      "v_mov_b32 v97, 0\n"
      "v_mov_b32 v98, 0\n"
      "v_mov_b32 v99, 0\n"
      "v_mov_b32 v100, 0\n"
      "v_mov_b32 v101, 0\n"
      "v_mov_b32 v102, 0\n"
      "v_mov_b32 v103, 0\n"
      "v_mov_b32 v104, 0\n"
      "v_mov_b32 v105, 0\n"
      "v_mov_b32 v106, 0\n"
      "v_mov_b32 v107, 0\n"
      "v_mov_b32 v108, 0\n"
      "v_mov_b32 v109, 0\n"
      "v_mov_b32 v110, 0\n"
      "v_mov_b32 v111, 0\n"
      "v_mov_b32 v112, 0\n"
      "v_mov_b32 v113, 0\n"
      "v_mov_b32 v114, 0\n"
      "v_mov_b32 v115, 0\n"
      "v_mov_b32 v116, 0\n"
      "v_mov_b32 v117, 0\n"
      "v_mov_b32 v118, 0\n"
      "v_mov_b32 v119, 0\n"
      "v_mov_b32 v120, 0\n"
      "v_mov_b32 v121, 0\n"
      "v_mov_b32 v122, 0\n"
      "v_mov_b32 v123, 0\n"
      "v_mov_b32 v124, 0\n"
      "v_mov_b32 v125, 0\n"
      "v_mov_b32 v126, 0\n"
      "v_mov_b32 v127, 0\n"
      "s_waitcnt vmcnt(8)\n"
      "v_mfma_f32_32x32x16_bf16 v[128:143], v[192:195], v[40:43], 0\n"
      "v_mfma_f32_32x32x8_bf16 v[128:143], v[204:205], v[56:57], v[128:143]\n"
      "s_nop 15\n"
      "1:\n"
      "v_mfma_f32_32x32x16_bf16 v[144:159], v[192:195], v[44:47], 0\n"
      "v_exp_f32 v128, v128\n"
      "v_exp_f32 v129, v129\n"
      "v_exp_f32 v130, v130\n"
      "v_exp_f32 v131, v131\n"
      "v_mfma_f32_32x32x8_bf16 v[144:159], v[204:205], v[58:59], v[144:159]\n"
      "v_exp_f32 v132, v132\n"
      "v_exp_f32 v133, v133\n"
      "v_exp_f32 v134, v134\n"
      "v_exp_f32 v135, v135\n"
      "v_smfmac_f32_32x32x32_bf16 v[112:127], v[238:241], v[176:183], %[idx]\n"
      "v_exp_f32 v136, v136\n"
      "v_exp_f32 v137, v137\n"
      "v_exp_f32 v138, v138\n"
      "v_exp_f32 v139, v139\n"
      "v_smfmac_f32_32x32x32_bf16 v[112:127], v[242:245], v[184:191], %[idx]\n"
      "v_exp_f32 v140, v140\n"
      "v_exp_f32 v141, v141\n"
      "v_exp_f32 v142, v142\n"
      "v_exp_f32 v143, v143\n"
      "v_mfma_f32_32x32x16_bf16 v[160:175], v[192:195], v[48:51], 0\n"
      "v_exp_f32 v144, v144\n"
      "v_exp_f32 v145, v145\n"
      "v_exp_f32 v146, v146\n"
      "v_exp_f32 v147, v147\n"
      "buffer_load_dwordx4 v[234:237], %[vo16], %[srd], %[so] offen sc1\n"
      "buffer_load_dwordx2 v[246:247], %[vo8], %[srd], %[so] offen offset:3072 sc1\n"
      "v_mfma_f32_32x32x8_bf16 v[160:175], v[204:205], v[60:61], v[160:175]\n"
      "v_exp_f32 v148, v148\n"
      "v_exp_f32 v149, v149\n"
      "v_exp_f32 v150, v150\n"
      "v_exp_f32 v151, v151\n"
      "buffer_load_dwordx4 v[238:241], %[vo16], %[srd], %[so] offen offset:1024 sc1\n"
      "v_smfmac_f32_32x32x32_bf16 v[64:79], v[196:199], v[128:135], %[idx]\n"
      "v_exp_f32 v152, v152\n"
      "v_exp_f32 v153, v153\n"
      "v_exp_f32 v154, v154\n"
      "v_exp_f32 v155, v155\n"
      "buffer_load_dwordx4 v[242:245], %[vo16], %[srd], %[so] offen offset:2048 sc1\n"
      "s_add_u32 %[so], %[so], 3584\n"
      "v_smfmac_f32_32x32x32_bf16 v[64:79], v[200:203], v[136:143], %[idx]\n"
      "v_exp_f32 v156, v156\n"
      "v_exp_f32 v157, v157\n"
      "v_exp_f32 v158, v158\n"
      "v_exp_f32 v159, v159\n"
      "v_mfma_f32_32x32x16_bf16 v[176:191], v[192:195], v[52:55], 0\n"
      "v_exp_f32 v160, v160\n"
      "v_exp_f32 v161, v161\n"
      "v_exp_f32 v162, v162\n"
      "v_exp_f32 v163, v163\n"
      "v_mfma_f32_32x32x8_bf16 v[176:191], v[204:205], v[62:63], v[176:191]\n"
      "v_exp_f32 v164, v164\n"
      "v_exp_f32 v165, v165\n"
      "v_exp_f32 v166, v166\n"
      "v_exp_f32 v167, v167\n"
      "v_smfmac_f32_32x32x32_bf16 v[80:95], v[196:199], v[144:151], %[idx]\n"
      "v_exp_f32 v168, v168\n"
      "v_exp_f32 v169, v169\n"
      "v_exp_f32 v170, v170\n"
      "v_exp_f32 v171, v171\n"
      "v_smfmac_f32_32x32x32_bf16 v[80:95], v[200:203], v[152:159], %[idx]\n"
      "v_exp_f32 v172, v172\n"
      "v_exp_f32 v173, v173\n"
      "v_exp_f32 v174, v174\n"
      "v_exp_f32 v175, v175\n"
      "s_waitcnt vmcnt(8)\n"
      "v_mfma_f32_32x32x16_bf16 v[128:143], v[206:209], v[40:43], 0\n"
      "v_exp_f32 v176, v176\n"
      "v_exp_f32 v177, v177\n"
      "v_exp_f32 v178, v178\n"
      "v_exp_f32 v179, v179\n"
      "v_mfma_f32_32x32x8_bf16 v[128:143], v[218:219], v[56:57], v[128:143]\n"
      "v_exp_f32 v180, v180\n"
      "v_exp_f32 v181, v181\n"
      "v_exp_f32 v182, v182\n"
      "v_exp_f32 v183, v183\n"
      "v_smfmac_f32_32x32x32_bf16 v[96:111], v[196:199], v[160:167], %[idx]\n"
      "v_exp_f32 v184, v184\n"
      "v_exp_f32 v185, v185\n"
      "v_exp_f32 v186, v186\n"
      "v_exp_f32 v187, v187\n"
      "v_smfmac_f32_32x32x32_bf16 v[96:111], v[200:203], v[168:175], %[idx]\n"
      "v_exp_f32 v188, v188\n"
      "v_exp_f32 v189, v189\n"
      "v_exp_f32 v190, v190\n"
      "v_exp_f32 v191, v191\n"
      "v_mfma_f32_32x32x16_bf16 v[144:159], v[206:209], v[44:47], 0\n"
      "v_exp_f32 v128, v128\n"
      "v_exp_f32 v129, v129\n"
      "v_exp_f32 v130, v130\n"
      "v_exp_f32 v131, v131\n"
      "v_mfma_f32_32x32x8_bf16 v[144:159], v[218:219], v[58:59], v[144:159]\n"
      "v_exp_f32 v132, v132\n"
      "v_exp_f32 v133, v133\n"
      "v_exp_f32 v134, v134\n"
      "v_exp_f32 v135, v135\n"
      "v_smfmac_f32_32x32x32_bf16 v[112:127], v[196:199], v[176:183], %[idx]\n"
      "v_exp_f32 v136, v136\n"
      "v_exp_f32 v137, v137\n"
      "v_exp_f32 v138, v138\n"
      "v_exp_f32 v139, v139\n"
      "v_smfmac_f32_32x32x32_bf16 v[112:127], v[200:203], v[184:191], %[idx]\n"
      "v_exp_f32 v140, v140\n"
      "v_exp_f32 v141, v141\n"
      "v_exp_f32 v142, v142\n"
      "v_exp_f32 v143, v143\n"
      "v_mfma_f32_32x32x16_bf16 v[160:175], v[206:209], v[48:51], 0\n"
      "v_exp_f32 v144, v144\n"
      "v_exp_f32 v145, v145\n"
      "v_exp_f32 v146, v146\n"
      "v_exp_f32 v147, v147\n"
      "buffer_load_dwordx4 v[192:195], %[vo16], %[srd], %[so] offen sc1\n"
      "buffer_load_dwordx2 v[204:205], %[vo8], %[srd], %[so] offen offset:3072 sc1\n"
      "v_mfma_f32_32x32x8_bf16 v[160:175], v[218:219], v[60:61], v[160:175]\n"
      "v_exp_f32 v148, v148\n"
      "v_exp_f32 v149, v149\n"
      "v_exp_f32 v150, v150\n"
      "v_exp_f32 v151, v151\n"
      "buffer_load_dwordx4 v[196:199], %[vo16], %[srd], %[so] offen offset:1024 sc1\n"
      "v_smfmac_f32_32x32x32_bf16 v[64:79], v[210:213], v[128:135], %[idx]\n"
      "v_exp_f32 v152, v152\n"
      "v_exp_f32 v153, v153\n"
      "v_exp_f32 v154, v154\n"
      "v_exp_f32 v155, v155\n"
      "buffer_load_dwordx4 v[200:203], %[vo16], %[srd], %[so] offen offset:2048 sc1\n"
      "s_add_u32 %[so], %[so], 3584\n"
      "v_smfmac_f32_32x32x32_bf16 v[64:79], v[214:217], v[136:143], %[idx]\n"
      "v_exp_f32 v156, v156\n"
      "v_exp_f32 v157, v157\n"
      "v_exp_f32 v158, v158\n"
      "v_exp_f32 v159, v159\n"
      "v_mfma_f32_32x32x16_bf16 v[176:191], v[206:209], v[52:55], 0\n"
      "v_exp_f32 v160, v160\n"
      "v_exp_f32 v161, v161\n"
      "v_exp_f32 v162, v162\n"
      "v_exp_f32 v163, v163\n"
      "v_mfma_f32_32x32x8_bf16 v[176:191], v[218:219], v[62:63], v[176:191]\n"
      "v_exp_f32 v164, v164\n"
      "v_exp_f32 v165, v165\n"
      "v_exp_f32 v166, v166\n"
      "v_exp_f32 v167, v167\n"
      "v_smfmac_f32_32x32x32_bf16 v[80:95], v[210:213], v[144:151], %[idx]\n"
      "v_exp_f32 v168, v168\n"
      "v_exp_f32 v169, v169\n"
      "v_exp_f32 v170, v170\n"
      "v_exp_f32 v171, v171\n"
      "v_smfmac_f32_32x32x32_bf16 v[80:95], v[214:217], v[152:159], %[idx]\n"
      "v_exp_f32 v172, v172\n"
      "v_exp_f32 v173, v173\n"
      "v_exp_f32 v174, v174\n"
      "v_exp_f32 v175, v175\n"
      "s_waitcnt vmcnt(8)\n"
      "v_mfma_f32_32x32x16_bf16 v[128:143], v[220:223], v[40:43], 0\n"
      "v_exp_f32 v176, v176\n"
      "v_exp_f32 v177, v177\n"
      "v_exp_f32 v178, v178\n"
      "v_exp_f32 v179, v179\n"
      "v_mfma_f32_32x32x8_bf16 v[128:143], v[232:233], v[56:57], v[128:143]\n"
      "v_exp_f32 v180, v180\n"
      "v_exp_f32 v181, v181\n"
      "v_exp_f32 v182, v182\n"
      "v_exp_f32 v183, v183\n"
      "v_smfmac_f32_32x32x32_bf16 v[96:111], v[210:213], v[160:167], %[idx]\n"
      "v_exp_f32 v184, v184\n"
      "v_exp_f32 v185, v185\n"
      "v_exp_f32 v186, v186\n"
      "v_exp_f32 v187, v187\n"
      "v_smfmac_f32_32x32x32_bf16 v[96:111], v[214:217], v[168:175], %[idx]\n"
      "v_exp_f32 v188, v188\n"
      "v_exp_f32 v189, v189\n"
      "v_exp_f32 v190, v190\n"
      "v_exp_f32 v191, v191\n"
      "v_mfma_f32_32x32x16_bf16 v[144:159], v[220:223], v[44:47], 0\n"
      "v_exp_f32 v128, v128\n"
      "v_exp_f32 v129, v129\n"
      "v_exp_f32 v130, v130\n"
      "v_exp_f32 v131, v131\n"
      "v_mfma_f32_32x32x8_bf16 v[144:159], v[232:233], v[58:59], v[144:159]\n"
      "v_exp_f32 v132, v132\n"
      "v_exp_f32 v133, v133\n"
      "v_exp_f32 v134, v134\n"
      "v_exp_f32 v135, v135\n"
      "v_smfmac_f32_32x32x32_bf16 v[112:127], v[210:213], v[176:183], %[idx]\n"
      "v_exp_f32 v136, v136\n"
      "v_exp_f32 v137, v137\n"
      "v_exp_f32 v138, v138\n"
      "v_exp_f32 v139, v139\n"
      "v_smfmac_f32_32x32x32_bf16 v[112:127], v[214:217], v[184:191], %[idx]\n"
      "v_exp_f32 v140, v140\n"
      "v_exp_f32 v141, v141\n"
      "v_exp_f32 v142, v142\n"
      "v_exp_f32 v143, v143\n"
      "v_mfma_f32_32x32x16_bf16 v[160:175], v[220:223], v[48:51], 0\n"
      "v_exp_f32 v144, v144\n"
      "v_exp_f32 v145, v145\n"
      "v_exp_f32 v146, v146\n"
      "v_exp_f32 v147, v147\n"
      "buffer_load_dwordx4 v[206:209], %[vo16], %[srd], %[so] offen sc1\n"
      "buffer_load_dwordx2 v[218:219], %[vo8], %[srd], %[so] offen offset:3072 sc1\n"
      "v_mfma_f32_32x32x8_bf16 v[160:175], v[232:233], v[60:61], v[160:175]\n"
      "v_exp_f32 v148, v148\n"
      "v_exp_f32 v149, v149\n"
      "v_exp_f32 v150, v150\n"
      "v_exp_f32 v151, v151\n"
      "buffer_load_dwordx4 v[210:213], %[vo16], %[srd], %[so] offen offset:1024 sc1\n"
      "v_smfmac_f32_32x32x32_bf16 v[64:79], v[224:227], v[128:135], %[idx]\n"
      "v_exp_f32 v152, v152\n"
      "v_exp_f32 v153, v153\n"
      "v_exp_f32 v154, v154\n"
      "v_exp_f32 v155, v155\n"
      "buffer_load_dwordx4 v[214:217], %[vo16], %[srd], %[so] offen offset:2048 sc1\n"
      "s_add_u32 %[so], %[so], 3584\n"
      "v_smfmac_f32_32x32x32_bf16 v[64:79], v[228:231], v[136:143], %[idx]\n"
      "v_exp_f32 v156, v156\n"
      "v_exp_f32 v157, v157\n"
      "v_exp_f32 v158, v158\n"
      "v_exp_f32 v159, v159\n"
      "v_mfma_f32_32x32x16_bf16 v[176:191], v[220:223], v[52:55], 0\n"
      "v_exp_f32 v160, v160\n"
      "v_exp_f32 v161, v161\n"
      "v_exp_f32 v162, v162\n"
      "v_exp_f32 v163, v163\n"
      "v_mfma_f32_32x32x8_bf16 v[176:191], v[232:233], v[62:63], v[176:191]\n"
      "v_exp_f32 v164, v164\n"
      "v_exp_f32 v165, v165\n"
      "v_exp_f32 v166, v166\n"
      "v_exp_f32 v167, v167\n"
      "v_smfmac_f32_32x32x32_bf16 v[80:95], v[224:227], v[144:151], %[idx]\n"
      "v_exp_f32 v168, v168\n"
      "v_exp_f32 v169, v169\n"
      "v_exp_f32 v170, v170\n"
      "v_exp_f32 v171, v171\n"
      "v_smfmac_f32_32x32x32_bf16 v[80:95], v[228:231], v[152:159], %[idx]\n"
      "v_exp_f32 v172, v172\n"
      "v_exp_f32 v173, v173\n"
      "v_exp_f32 v174, v174\n"
      "v_exp_f32 v175, v175\n"
      "s_waitcnt vmcnt(8)\n"
      "v_mfma_f32_32x32x16_bf16 v[128:143], v[234:237], v[40:43], 0\n"
      "v_exp_f32 v176, v176\n"
      "v_exp_f32 v177, v177\n"
      "v_exp_f32 v178, v178\n"
      "v_exp_f32 v179, v179\n"
      "v_mfma_f32_32x32x8_bf16 v[128:143], v[246:247], v[56:57], v[128:143]\n"
      "v_exp_f32 v180, v180\n"
      "v_exp_f32 v181, v181\n"
      "v_exp_f32 v182, v182\n"
      "v_exp_f32 v183, v183\n"
      "v_smfmac_f32_32x32x32_bf16 v[96:111], v[224:227], v[160:167], %[idx]\n"
      "v_exp_f32 v184, v184\n"
      "v_exp_f32 v185, v185\n"
      "v_exp_f32 v186, v186\n"
      "v_exp_f32 v187, v187\n"
      "v_smfmac_f32_32x32x32_bf16 v[96:111], v[228:231], v[168:175], %[idx]\n"
      "v_exp_f32 v188, v188\n"
      "v_exp_f32 v189, v189\n"
      "v_exp_f32 v190, v190\n"
      "v_exp_f32 v191, v191\n"
      "v_mfma_f32_32x32x16_bf16 v[144:159], v[234:237], v[44:47], 0\n"
      "v_exp_f32 v128, v128\n"
      "v_exp_f32 v129, v129\n"
      "v_exp_f32 v130, v130\n"
      "v_exp_f32 v131, v131\n"
      "v_mfma_f32_32x32x8_bf16 v[144:159], v[246:247], v[58:59], v[144:159]\n"
      "v_exp_f32 v132, v132\n"
      "v_exp_f32 v133, v133\n"
      "v_exp_f32 v134, v134\n"
      "v_exp_f32 v135, v135\n"
      "v_smfmac_f32_32x32x32_bf16 v[112:127], v[224:227], v[176:183], %[idx]\n"
      "v_exp_f32 v136, v136\n"
      "v_exp_f32 v137, v137\n"
      "v_exp_f32 v138, v138\n"
      "v_exp_f32 v139, v139\n"
      "v_smfmac_f32_32x32x32_bf16 v[112:127], v[228:231], v[184:191], %[idx]\n"
      "v_exp_f32 v140, v140\n"
      "v_exp_f32 v141, v141\n"
      "v_exp_f32 v142, v142\n"
      "v_exp_f32 v143, v143\n"
      "v_mfma_f32_32x32x16_bf16 v[160:175], v[234:237], v[48:51], 0\n"
      "v_exp_f32 v144, v144\n"
      "v_exp_f32 v145, v145\n"
      "v_exp_f32 v146, v146\n"
      "v_exp_f32 v147, v147\n"
      "buffer_load_dwordx4 v[220:223], %[vo16], %[srd], %[so] offen sc1\n"
      "buffer_load_dwordx2 v[232:233], %[vo8], %[srd], %[so] offen offset:3072 sc1\n"
      "v_mfma_f32_32x32x8_bf16 v[160:175], v[246:247], v[60:61], v[160:175]\n"
      "v_exp_f32 v148, v148\n"
      "v_exp_f32 v149, v149\n"
      "v_exp_f32 v150, v150\n"
      "v_exp_f32 v151, v151\n"
      "buffer_load_dwordx4 v[224:227], %[vo16], %[srd], %[so] offen offset:1024 sc1\n"
      "v_smfmac_f32_32x32x32_bf16 v[64:79], v[238:241], v[128:135], %[idx]\n"
      "v_exp_f32 v152, v152\n"
      "v_exp_f32 v153, v153\n"
      "v_exp_f32 v154, v154\n"
      "v_exp_f32 v155, v155\n"
      "buffer_load_dwordx4 v[228:231], %[vo16], %[srd], %[so] offen offset:2048 sc1\n"
      "s_add_u32 %[so], %[so], 3584\n"
      "v_smfmac_f32_32x32x32_bf16 v[64:79], v[242:245], v[136:143], %[idx]\n"
      "v_exp_f32 v156, v156\n"
      "v_exp_f32 v157, v157\n"
      "v_exp_f32 v158, v158\n"
      "v_exp_f32 v159, v159\n"
      "v_mfma_f32_32x32x16_bf16 v[176:191], v[234:237], v[52:55], 0\n"
      "v_exp_f32 v160, v160\n"
      "v_exp_f32 v161, v161\n"
      "v_exp_f32 v162, v162\n"
      "v_exp_f32 v163, v163\n"
      "v_mfma_f32_32x32x8_bf16 v[176:191], v[246:247], v[62:63], v[176:191]\n"
      "v_exp_f32 v164, v164\n"
      "v_exp_f32 v165, v165\n"
      "v_exp_f32 v166, v166\n"
      "v_exp_f32 v167, v167\n"
      "v_smfmac_f32_32x32x32_bf16 v[80:95], v[238:241], v[144:151], %[idx]\n"
      "v_exp_f32 v168, v168\n"
      "v_exp_f32 v169, v169\n"
      "v_exp_f32 v170, v170\n"
      "v_exp_f32 v171, v171\n"
      "v_smfmac_f32_32x32x32_bf16 v[80:95], v[242:245], v[152:159], %[idx]\n"
      "v_exp_f32 v172, v172\n"
      "v_exp_f32 v173, v173\n"
      "v_exp_f32 v174, v174\n"
      "v_exp_f32 v175, v175\n"
      "s_waitcnt vmcnt(8)\n"
      "v_mfma_f32_32x32x16_bf16 v[128:143], v[192:195], v[40:43], 0\n"
      "v_exp_f32 v176, v176\n"
      "v_exp_f32 v177, v177\n"
      "v_exp_f32 v178, v178\n"
      "v_exp_f32 v179, v179\n"
      "v_mfma_f32_32x32x8_bf16 v[128:143], v[204:205], v[56:57], v[128:143]\n"
      "v_exp_f32 v180, v180\n"
      "v_exp_f32 v181, v181\n"
      "v_exp_f32 v182, v182\n"
      "v_exp_f32 v183, v183\n"
      "v_smfmac_f32_32x32x32_bf16 v[96:111], v[238:241], v[160:167], %[idx]\n"
      "v_exp_f32 v184, v184\n"
      "v_exp_f32 v185, v185\n"
      "v_exp_f32 v186, v186\n"
      "v_exp_f32 v187, v187\n"
      "v_smfmac_f32_32x32x32_bf16 v[96:111], v[242:245], v[168:175], %[idx]\n"
      "v_exp_f32 v188, v188\n"
      "v_exp_f32 v189, v189\n"
      "v_exp_f32 v190, v190\n"
      "v_exp_f32 v191, v191\n"
      "s_sub_u32 %[cnt], %[cnt], 1\n"
      "s_cmp_lg_u32 %[cnt], 0\n"
      "s_cbranch_scc1 1b\n"
      "s_nop 1\n"
      "v_smfmac_f32_32x32x32_bf16 v[112:127], v[238:241], v[176:183], %[idx]\n"
      "v_smfmac_f32_32x32x32_bf16 v[112:127], v[242:245], v[184:191], %[idx]\n"
      "s_waitcnt vmcnt(0)\n"
      "s_nop 15\n"
            : "=&{v[64:79]}"(O0), "=&{v[80:95]}"(O1), "=&{v[96:111]}"(O2), "=&{v[112:127]}"(O3), [so] "+s"(so), [cnt] "+s"(cnt)
            : "{v[40:55]}"(Q1), "{v[56:63]}"(Q2), [vo16] "v"(lane * 16), [vo8] "v"(lane * 8), [idx] "v"(0xdddd), [srd] "s"(kvsrd)
            : "memory", "scc", "v128", "v129", "v130", "v131", "v132", "v133", "v134", "v135", "v136", "v137", "v138", "v139", "v140", "v141", "v142", "v143", "v144", "v145", "v146", "v147", "v148", "v149", "v150", "v151", "v152", "v153", "v154", "v155", "v156", "v157", "v158", "v159", "v160", "v161", "v162", "v163", "v164", "v165", "v166", "v167", "v168", "v169", "v170", "v171", "v172", "v173", "v174", "v175", "v176", "v177", "v178", "v179", "v180", "v181", "v182", "v183", "v184", "v185", "v186", "v187", "v188", "v189", "v190", "v191", "v192", "v193", "v194", "v195", "v196", "v197", "v198", "v199", "v200", "v201", "v202", "v203", "v204", "v205", "v206", "v207", "v208", "v209", "v210", "v211", "v212", "v213", "v214", "v215", "v216", "v217", "v218", "v219", "v220", "v221", "v222", "v223", "v224", "v225", "v226", "v227", "v228", "v229", "v230", "v231", "v232", "v233", "v234", "v235", "v236", "v237", "v238", "v239", "v240", "v241", "v242", "v243", "v244", "v245", "v246", "v247");
      }
      LAS char* pr = lds + L_AR + (w ^ 2) * AR_SZ;
#define PUT(n, r) *(LAS f32x4*)(ar + (n) * 1024 + lane * 16) = f32x4{O0[r], O1[r], O2[r], O3[r]}
#define GET(n, r) { f32x4 p = *(LAS f32x4*)(pr + (n) * 1024 + lane * 16); O0[r] += p[0]; O1[r] += p[1]; O2[r] += p[2]; O3[r] += p[3]; }
#define CHUNK(s) st16_sc1(od + (s) * 2048, u32x4{pk(O0[2 * (s)] * r0, O0[2 * (s) + 1] * r0), pk(O1[2 * (s)] * r1, O1[2 * (s) + 1] * r1), \
                                                 pk(O2[2 * (s)] * r2, O2[2 * (s) + 1] * r2), pk(O3[2 * (s)] * r3, O3[2 * (s) + 1] * r3)})
      char* od = ws + WS_ACT + (u32)(g1 * ACT_G + (96 + 5 * j1) * 2048 + ta * 1024 + lane * 16);
      lds_wait(lds, C_QR, 4);
      PUT(0, 10);
      if (kb == 0) { PUT(1, 6); PUT(2, 7); PUT(3, 8); PUT(4, 9); }
      else { PUT(1, 0); PUT(2, 1); PUT(3, 2); PUT(4, 3); PUT(5, 4); PUT(6, 5); }
      lds_add(lds, C_PAIR + w);
      lds_wait(lds, C_PAIR + (w ^ 2), 1);
      GET(0, 10);
      const float r0 = 1.f / O0[10], r1 = 1.f / O1[10], r2 = 1.f / O2[10], r3 = 1.f / O3[10];
      if (kb == 0) {
        GET(1, 0); GET(2, 1); GET(3, 2); GET(4, 3); GET(5, 4); GET(6, 5);
        CHUNK(0); CHUNK(1); CHUNK(2);
      } else {
        GET(1, 6); GET(2, 7); GET(3, 8); GET(4, 9);
        CHUNK(3); CHUNK(4);
      }
      drain();
      if (lds_add(lds, C_O) == 3 && lane == 0) st4_sc1(ws + WS_OFLAG + (u32)(g1 * 8 + j1) * 64, tag);
    } else {
      drain();
    }
    __builtin_amdgcn_s_barrier();
    if (FUSED && bid == 0 && w == 0) {
      for (int i = 0; i < 4; ++i) poll_tag(ws + WS_HFLAG + (u32)(i * 64 + lane) * 64, tag);
      if (lane == 0) st4_sc1(ws + WS_G, tag);
    }
  } else {
    const int v = w - 4;
    const u32 ring = (u32)(size_t)(lds + L_GR + v * 12288);
    LOADXALL;
    int drow[3], dchk[3];
#pragma unroll
    for (int i = 0; i < 3; ++i) { int n = i * 64 + lane; drow[i] = n / 12; dchk[i] = n % 12; }
    const char* a[3];
#pragma unroll
    for (int i = 0; i < 3; ++i)
      a[i] = (dchk[i] < 6 ? wg + dchk[i] * 16 : wu + (dchk[i] - 6) * 16) + (drow[i] * 3072 + 384 * j1 + 96 * v - i * 1024);
    const int odd = j3 & 1, cb = (160 * (j3 >> 1) + 64 * odd) * 2;
    const char* nd[3];
    const char* no[3];
#pragma unroll
    for (int i = 0; i < 3; ++i) {
      int r = drow[i], c = (dchk[i] + 4 * odd) % 12;
      int fr = (r & 3) + 8 * ((r >> 2) & 1) + 4 * (r >> 3);
      nd[i] = wd + ((384 * v + fr) * 1280 + cb + c * 16 - i * 1024);
      int orow = (8 * v + ((r & 7) >> 1)) * 20 + 10 * (r >> 3) + (r & 1);
      no[i] = wo + (orow * 1280 + cb + c * 16 - i * 1024);
    }
    if (PH & 1) {
#pragma unroll
      for (int s = 0; s < 4; ++s) { glds3(a[0], a[1], a[2], ring + s * 3072); a[0] += 16 * 3072; a[1] += 16 * 3072; a[2] += 16 * 3072; }
    } else {
      a[0] = nd[0]; a[1] = nd[1]; a[2] = nd[2];
#pragma unroll
      for (int s = 0; s < 4; ++s) { glds3(a[0], a[1], a[2], ring + s * 3072); a[0] += 16 * 1280; a[1] += 16 * 1280; a[2] += 16 * 1280; }
    }
    asm volatile("s_waitcnt vmcnt(12)" : XPIN::"memory");
    const u32 g0 = __builtin_amdgcn_readfirstlane(gv);
    u32 tag = g0 * 0x9E3779B1u + 0x7F4A7C15u;
    if (tag == g0) tag ^= 0x10u;
    layernorm(xr, lds, w, lane);
    asm volatile("s_waitcnt lgkmcnt(0)" ::: "memory");
    __builtin_amdgcn_s_barrier();
    asm volatile("" ::: "memory");

    if (PH & 1) {
      f32x16 c0, c1, c2, c3, c4, c5;
      u32 rw = ring + (8 * h + trq) * 192 + trb * 96 + trp * 8;
      u32 rx = (u32)(size_t)(lds + L_XN) + l31 * XS + h * 16;
      u64 st = 16 * 3072, nst = 16 * 1280;
      u32 cnt = 10;
      asm volatile(
      "v_mov_b32 v64, 0\n"
      "v_mov_b32 v65, 0\n"
      "v_mov_b32 v66, 0\n"
      "v_mov_b32 v67, 0\n"
      "v_mov_b32 v68, 0\n"
      "v_mov_b32 v69, 0\n"
      "v_mov_b32 v70, 0\n"
      "v_mov_b32 v71, 0\n"
      "v_mov_b32 v72, 0\n"
      "v_mov_b32 v73, 0\n"
      "v_mov_b32 v74, 0\n"
      "v_mov_b32 v75, 0\n"
      "v_mov_b32 v76, 0\n"
      "v_mov_b32 v77, 0\n"
      "v_mov_b32 v78, 0\n"
      "v_mov_b32 v79, 0\n"
      "v_mov_b32 v80, 0\n"
      "v_mov_b32 v81, 0\n"
      "v_mov_b32 v82, 0\n"
      "v_mov_b32 v83, 0\n"
      "v_mov_b32 v84, 0\n"
      "v_mov_b32 v85, 0\n"
      "v_mov_b32 v86, 0\n"
      "v_mov_b32 v87, 0\n"
      "v_mov_b32 v88, 0\n"
      "v_mov_b32 v89, 0\n"
      "v_mov_b32 v90, 0\n"
      "v_mov_b32 v91, 0\n"
      "v_mov_b32 v92, 0\n"
      "v_mov_b32 v93, 0\n"
      "v_mov_b32 v94, 0\n"
      "v_mov_b32 v95, 0\n"
      "v_mov_b32 v96, 0\n"
      "v_mov_b32 v97, 0\n"
      "v_mov_b32 v98, 0\n"
      "v_mov_b32 v99, 0\n"
      "v_mov_b32 v100, 0\n"
      "v_mov_b32 v101, 0\n"
      "v_mov_b32 v102, 0\n"
      "v_mov_b32 v103, 0\n"
      "v_mov_b32 v104, 0\n"
      "v_mov_b32 v105, 0\n"
      "v_mov_b32 v106, 0\n"
      "v_mov_b32 v107, 0\n"
      "v_mov_b32 v108, 0\n"
      "v_mov_b32 v109, 0\n"
      "v_mov_b32 v110, 0\n"
      "v_mov_b32 v111, 0\n"
      "v_mov_b32 v112, 0\n"
      "v_mov_b32 v113, 0\n"
      "v_mov_b32 v114, 0\n"
      "v_mov_b32 v115, 0\n"
      "v_mov_b32 v116, 0\n"
      "v_mov_b32 v117, 0\n"
      "v_mov_b32 v118, 0\n"
      "v_mov_b32 v119, 0\n"
      "v_mov_b32 v120, 0\n"
      "v_mov_b32 v121, 0\n"
      "v_mov_b32 v122, 0\n"
      "v_mov_b32 v123, 0\n"
      "v_mov_b32 v124, 0\n"
      "v_mov_b32 v125, 0\n"
      "v_mov_b32 v126, 0\n"
      "v_mov_b32 v127, 0\n"
      "v_mov_b32 v128, 0\n"
      "v_mov_b32 v129, 0\n"
      "v_mov_b32 v130, 0\n"
      "v_mov_b32 v131, 0\n"
      "v_mov_b32 v132, 0\n"
      "v_mov_b32 v133, 0\n"
      "v_mov_b32 v134, 0\n"
      "v_mov_b32 v135, 0\n"
      "v_mov_b32 v136, 0\n"
      "v_mov_b32 v137, 0\n"
      "v_mov_b32 v138, 0\n"
      "v_mov_b32 v139, 0\n"
      "v_mov_b32 v140, 0\n"
      "v_mov_b32 v141, 0\n"
      "v_mov_b32 v142, 0\n"
      "v_mov_b32 v143, 0\n"
      "v_mov_b32 v144, 0\n"
      "v_mov_b32 v145, 0\n"
      "v_mov_b32 v146, 0\n"
      "v_mov_b32 v147, 0\n"
      "v_mov_b32 v148, 0\n"
      "v_mov_b32 v149, 0\n"
      "v_mov_b32 v150, 0\n"
      "v_mov_b32 v151, 0\n"
      "v_mov_b32 v152, 0\n"
      "v_mov_b32 v153, 0\n"
      "v_mov_b32 v154, 0\n"
      "v_mov_b32 v155, 0\n"
      "v_mov_b32 v156, 0\n"
      "v_mov_b32 v157, 0\n"
      "v_mov_b32 v158, 0\n"
      "v_mov_b32 v159, 0\n"
      "s_waitcnt vmcnt(9)\n"
      "ds_read_b64_tr_b16 v[160:161], %[rw] offset:0\n"
      "ds_read_b64_tr_b16 v[162:163], %[rw] offset:768\n"
      "ds_read_b64_tr_b16 v[164:165], %[rw] offset:32\n"
      "ds_read_b64_tr_b16 v[166:167], %[rw] offset:800\n"
      "ds_read_b64_tr_b16 v[168:169], %[rw] offset:64\n"
      "ds_read_b64_tr_b16 v[170:171], %[rw] offset:832\n"
      "ds_read_b128 v[184:187], %[rx] offset:0\n"
      "ds_read_b128 v[188:191], %[rx] offset:41472\n"
      "1:\n"
      "s_cmp_lg_u32 %[cnt], 1\n"
      "s_cbranch_scc1 2f\n"
      "v_mov_b64 %[a0], %[n0]\n"
      "v_mov_b64 %[a1], %[n1]\n"
      "v_mov_b64 %[a2], %[n2]\n"
      "s_mov_b64 %[st], %[nst]\n"
      "2:\n"
      "s_waitcnt vmcnt(6) lgkmcnt(0)\n"
      "v_mfma_f32_32x32x16_bf16 v[64:79], v[160:163], v[184:187], v[64:79]\n"
      "ds_read_b128 v[192:195], %[rx] offset:32\n"
      "ds_read_b128 v[196:199], %[rx] offset:41504\n"
      "ds_read_b64_tr_b16 v[172:173], %[rw] offset:3072\n"
      "ds_read_b64_tr_b16 v[174:175], %[rw] offset:3840\n"
      "v_mfma_f32_32x32x16_bf16 v[80:95], v[160:163], v[188:191], v[80:95]\n"
      "ds_read_b64_tr_b16 v[176:177], %[rw] offset:3104\n"
      "ds_read_b64_tr_b16 v[178:179], %[rw] offset:3872\n"
      "ds_read_b64_tr_b16 v[180:181], %[rw] offset:3136\n"
      "ds_read_b64_tr_b16 v[182:183], %[rw] offset:3904\n"
      "s_add_u32 m0, %[ring], 0\n"
      "v_mfma_f32_32x32x16_bf16 v[96:111], v[164:167], v[184:187], v[96:111]\n"
      "global_load_lds_dwordx4 %[a0], off\n"
      "v_mfma_f32_32x32x16_bf16 v[112:127], v[164:167], v[188:191], v[112:127]\n"
      "global_load_lds_dwordx4 %[a1], off offset:1024\n"
      "v_mfma_f32_32x32x16_bf16 v[128:143], v[168:171], v[184:187], v[128:143]\n"
      "global_load_lds_dwordx4 %[a2], off offset:2048\n"
      "v_mfma_f32_32x32x16_bf16 v[144:159], v[168:171], v[188:191], v[144:159]\n"
      "v_lshl_add_u64 %[a0], %[a0], 0, %[st]\n"
      "v_lshl_add_u64 %[a1], %[a1], 0, %[st]\n"
      "v_lshl_add_u64 %[a2], %[a2], 0, %[st]\n"
      "s_waitcnt vmcnt(6) lgkmcnt(0)\n"
      "v_mfma_f32_32x32x16_bf16 v[64:79], v[172:175], v[192:195], v[64:79]\n"
      "ds_read_b128 v[184:187], %[rx] offset:64\n"
      "ds_read_b128 v[188:191], %[rx] offset:41536\n"
      "ds_read_b64_tr_b16 v[160:161], %[rw] offset:6144\n"
      "ds_read_b64_tr_b16 v[162:163], %[rw] offset:6912\n"
      "v_mfma_f32_32x32x16_bf16 v[80:95], v[172:175], v[196:199], v[80:95]\n"
      "ds_read_b64_tr_b16 v[164:165], %[rw] offset:6176\n"
      "ds_read_b64_tr_b16 v[166:167], %[rw] offset:6944\n"
      "ds_read_b64_tr_b16 v[168:169], %[rw] offset:6208\n"
      "ds_read_b64_tr_b16 v[170:171], %[rw] offset:6976\n"
      "s_add_u32 m0, %[ring], 3072\n"
      "v_mfma_f32_32x32x16_bf16 v[96:111], v[176:179], v[192:195], v[96:111]\n"
      "global_load_lds_dwordx4 %[a0], off\n"
      "v_mfma_f32_32x32x16_bf16 v[112:127], v[176:179], v[196:199], v[112:127]\n"
      "global_load_lds_dwordx4 %[a1], off offset:1024\n"
      "v_mfma_f32_32x32x16_bf16 v[128:143], v[180:183], v[192:195], v[128:143]\n"
      "global_load_lds_dwordx4 %[a2], off offset:2048\n"
      "v_mfma_f32_32x32x16_bf16 v[144:159], v[180:183], v[196:199], v[144:159]\n"
      "v_lshl_add_u64 %[a0], %[a0], 0, %[st]\n"
      "v_lshl_add_u64 %[a1], %[a1], 0, %[st]\n"
      "v_lshl_add_u64 %[a2], %[a2], 0, %[st]\n"
      "s_waitcnt vmcnt(6) lgkmcnt(0)\n"
      "v_mfma_f32_32x32x16_bf16 v[64:79], v[160:163], v[184:187], v[64:79]\n"
      "ds_read_b128 v[192:195], %[rx] offset:96\n"
      "ds_read_b128 v[196:199], %[rx] offset:41568\n"
      "ds_read_b64_tr_b16 v[172:173], %[rw] offset:9216\n"
      "ds_read_b64_tr_b16 v[174:175], %[rw] offset:9984\n"
      "v_mfma_f32_32x32x16_bf16 v[80:95], v[160:163], v[188:191], v[80:95]\n"
      "ds_read_b64_tr_b16 v[176:177], %[rw] offset:9248\n"
      "ds_read_b64_tr_b16 v[178:179], %[rw] offset:10016\n"
      "ds_read_b64_tr_b16 v[180:181], %[rw] offset:9280\n"
      "ds_read_b64_tr_b16 v[182:183], %[rw] offset:10048\n"
      "s_add_u32 m0, %[ring], 6144\n"
      "v_mfma_f32_32x32x16_bf16 v[96:111], v[164:167], v[184:187], v[96:111]\n"
      "global_load_lds_dwordx4 %[a0], off\n"
      "v_mfma_f32_32x32x16_bf16 v[112:127], v[164:167], v[188:191], v[112:127]\n"
      "global_load_lds_dwordx4 %[a1], off offset:1024\n"
      "v_mfma_f32_32x32x16_bf16 v[128:143], v[168:171], v[184:187], v[128:143]\n"
      "global_load_lds_dwordx4 %[a2], off offset:2048\n"
      "v_mfma_f32_32x32x16_bf16 v[144:159], v[168:171], v[188:191], v[144:159]\n"
      "v_lshl_add_u64 %[a0], %[a0], 0, %[st]\n"
      "v_lshl_add_u64 %[a1], %[a1], 0, %[st]\n"
      "v_lshl_add_u64 %[a2], %[a2], 0, %[st]\n"
      "s_waitcnt vmcnt(6) lgkmcnt(0)\n"
      "v_mfma_f32_32x32x16_bf16 v[64:79], v[172:175], v[192:195], v[64:79]\n"
      "ds_read_b128 v[184:187], %[rx] offset:128\n"
      "ds_read_b128 v[188:191], %[rx] offset:41600\n"
      "ds_read_b64_tr_b16 v[160:161], %[rw] offset:0\n"
      "ds_read_b64_tr_b16 v[162:163], %[rw] offset:768\n"
      "v_mfma_f32_32x32x16_bf16 v[80:95], v[172:175], v[196:199], v[80:95]\n"
      "ds_read_b64_tr_b16 v[164:165], %[rw] offset:32\n"
      "ds_read_b64_tr_b16 v[166:167], %[rw] offset:800\n"
      "ds_read_b64_tr_b16 v[168:169], %[rw] offset:64\n"
      "ds_read_b64_tr_b16 v[170:171], %[rw] offset:832\n"
      "s_add_u32 m0, %[ring], 9216\n"
      "v_mfma_f32_32x32x16_bf16 v[96:111], v[176:179], v[192:195], v[96:111]\n"
      "global_load_lds_dwordx4 %[a0], off\n"
      "v_mfma_f32_32x32x16_bf16 v[112:127], v[176:179], v[196:199], v[112:127]\n"
      "global_load_lds_dwordx4 %[a1], off offset:1024\n"
      "v_mfma_f32_32x32x16_bf16 v[128:143], v[180:183], v[192:195], v[128:143]\n"
      "global_load_lds_dwordx4 %[a2], off offset:2048\n"
      "v_mfma_f32_32x32x16_bf16 v[144:159], v[180:183], v[196:199], v[144:159]\n"
      "v_lshl_add_u64 %[a0], %[a0], 0, %[st]\n"
      "v_lshl_add_u64 %[a1], %[a1], 0, %[st]\n"
      "v_lshl_add_u64 %[a2], %[a2], 0, %[st]\n"
      "v_add_u32 %[rx], 128, %[rx]\n"
      "s_sub_u32 %[cnt], %[cnt], 1\n"
      "s_cmp_lg_u32 %[cnt], 0\n"
      "s_cbranch_scc1 1b\n"
      "s_waitcnt lgkmcnt(0)\n"
      "s_nop 15\n"
          : "=&{v[64:79]}"(c0), "=&{v[80:95]}"(c1), "=&{v[96:111]}"(c2), "=&{v[112:127]}"(c3), "=&{v[128:143]}"(c4), "=&{v[144:159]}"(c5),
            [a0] "+v"(a[0]), [a1] "+v"(a[1]), [a2] "+v"(a[2]), [st] "+s"(st), [cnt] "+s"(cnt), [rx] "+v"(rx)
          : [n0] "v"(nd[0]), [n1] "v"(nd[1]), [n2] "v"(nd[2]), [nst] "s"(nst), [rw] "v"(rw), [ring] "s"(ring)
          : "memory", "scc", "v160", "v161", "v162", "v163", "v164", "v165", "v166", "v167", "v168", "v169", "v170", "v171", "v172", "v173", "v174", "v175", "v176", "v177", "v178", "v179", "v180", "v181", "v182", "v183", "v184", "v185", "v186", "v187", "v188", "v189", "v190", "v191", "v192", "v193", "v194", "v195", "v196", "v197", "v198", "v199");
      char* hd = ws + WS_ACT + (u32)(g1 * ACT_G + (12 * j1 + 3 * v) * 2048 + lane * 16);
#define HST(c, n) st16_sc1(hd + (n) * 1024, u32x4{pk(c[0] * c[8], c[1] * c[9]), pk(c[2] * c[10], c[3] * c[11]), pk(c[4] * c[12], c[5] * c[13]), pk(c[6] * c[14], c[7] * c[15])})
      HST(c0, 0); HST(c1, 1); HST(c2, 2); HST(c3, 3); HST(c4, 4); HST(c5, 5);
      drain();
      if (lds_add(lds, C_H) == 3 && lane == 0) st4_sc1(ws + WS_HFLAG + (u32)(g1 * 8 + j1) * 64, tag);
    } else {
      drain();
      lds_add(lds, C_H);
    }
    if (PH & 4) {
      if (FUSED) poll_tag(ws + WS_HFLAG + (u32)(g3 * 8 + 2 * v + (lane & 1)) * 64, tag);
      f32x16 c0, c1, c2, c3, c4;
      const u32 rw = ring + (8 * h + trq) * 192 + trb * 32 + trp * 8;
      const u32 v0 = odd * 1024 + lane * 16, v1 = (odd ^ 1) * 1024 + lane * 16;
      {
        u32x4 srd = mk_srd(ws + WS_ACT + (u32)(g3 * ACT_G), ACT_G);
        u64 st = 16 * 1280, nst = 2 * 1280;
        u32 cnt = 6, so = 24 * v * 2048;
        asm volatile(
      "s_nop 4\n"
      "buffer_load_dwordx4 v[168:171], %[v0], s[40:43], %[so] offen sc1\n"
      "buffer_load_dwordx4 v[172:175], %[v1], s[40:43], %[so] offen sc1\n"
      "s_add_u32 %[so], %[so], 2048\n"
      "buffer_load_dwordx4 v[176:179], %[v0], s[40:43], %[so] offen sc1\n"
      "buffer_load_dwordx4 v[180:183], %[v1], s[40:43], %[so] offen sc1\n"
      "s_add_u32 %[so], %[so], 2048\n"
      "buffer_load_dwordx4 v[184:187], %[v0], s[40:43], %[so] offen sc1\n"
      "buffer_load_dwordx4 v[188:191], %[v1], s[40:43], %[so] offen sc1\n"
      "s_add_u32 %[so], %[so], 2048\n"
      "buffer_load_dwordx4 v[192:195], %[v0], s[40:43], %[so] offen sc1\n"
      "buffer_load_dwordx4 v[196:199], %[v1], s[40:43], %[so] offen sc1\n"
      "s_add_u32 %[so], %[so], 2048\n"
      "v_mov_b32 v64, 0\n"
      "v_mov_b32 v65, 0\n"
      "v_mov_b32 v66, 0\n"
      "v_mov_b32 v67, 0\n"
      "v_mov_b32 v68, 0\n"
      "v_mov_b32 v69, 0\n"
      "v_mov_b32 v70, 0\n"
      "v_mov_b32 v71, 0\n"
      "v_mov_b32 v72, 0\n"
      "v_mov_b32 v73, 0\n"
      "v_mov_b32 v74, 0\n"
      "v_mov_b32 v75, 0\n"
      "v_mov_b32 v76, 0\n"
      "v_mov_b32 v77, 0\n"
      "v_mov_b32 v78, 0\n"
      "v_mov_b32 v79, 0\n"
      "v_mov_b32 v80, 0\n"
      "v_mov_b32 v81, 0\n"
      "v_mov_b32 v82, 0\n"
      "v_mov_b32 v83, 0\n"
      "v_mov_b32 v84, 0\n"
      "v_mov_b32 v85, 0\n"
      "v_mov_b32 v86, 0\n"
      "v_mov_b32 v87, 0\n"
      "v_mov_b32 v88, 0\n"
      "v_mov_b32 v89, 0\n"
      "v_mov_b32 v90, 0\n"
      "v_mov_b32 v91, 0\n"
      "v_mov_b32 v92, 0\n"
      "v_mov_b32 v93, 0\n"
      "v_mov_b32 v94, 0\n"
      "v_mov_b32 v95, 0\n"
      "v_mov_b32 v96, 0\n"
      "v_mov_b32 v97, 0\n"
      "v_mov_b32 v98, 0\n"
      "v_mov_b32 v99, 0\n"
      "v_mov_b32 v100, 0\n"
      "v_mov_b32 v101, 0\n"
      "v_mov_b32 v102, 0\n"
      "v_mov_b32 v103, 0\n"
      "v_mov_b32 v104, 0\n"
      "v_mov_b32 v105, 0\n"
      "v_mov_b32 v106, 0\n"
      "v_mov_b32 v107, 0\n"
      "v_mov_b32 v108, 0\n"
      "v_mov_b32 v109, 0\n"
      "v_mov_b32 v110, 0\n"
      "v_mov_b32 v111, 0\n"
      "v_mov_b32 v112, 0\n"
      "v_mov_b32 v113, 0\n"
      "v_mov_b32 v114, 0\n"
      "v_mov_b32 v115, 0\n"
      "v_mov_b32 v116, 0\n"
      "v_mov_b32 v117, 0\n"
      "v_mov_b32 v118, 0\n"
      "v_mov_b32 v119, 0\n"
      "v_mov_b32 v120, 0\n"
      "v_mov_b32 v121, 0\n"
      "v_mov_b32 v122, 0\n"
      "v_mov_b32 v123, 0\n"
      "v_mov_b32 v124, 0\n"
      "v_mov_b32 v125, 0\n"
      "v_mov_b32 v126, 0\n"
      "v_mov_b32 v127, 0\n"
      "v_mov_b32 v128, 0\n"
      "v_mov_b32 v129, 0\n"
      "v_mov_b32 v130, 0\n"
      "v_mov_b32 v131, 0\n"
      "v_mov_b32 v132, 0\n"
      "v_mov_b32 v133, 0\n"
      "v_mov_b32 v134, 0\n"
      "v_mov_b32 v135, 0\n"
      "v_mov_b32 v136, 0\n"
      "v_mov_b32 v137, 0\n"
      "v_mov_b32 v138, 0\n"
      "v_mov_b32 v139, 0\n"
      "v_mov_b32 v140, 0\n"
      "v_mov_b32 v141, 0\n"
      "v_mov_b32 v142, 0\n"
      "v_mov_b32 v143, 0\n"
      "ds_read_b64_tr_b16 v[144:145], %[rw] offset:0\n"
      "ds_read_b64_tr_b16 v[146:147], %[rw] offset:768\n"
      "ds_read_b64_tr_b16 v[148:149], %[rw] offset:64\n"
      "ds_read_b64_tr_b16 v[150:151], %[rw] offset:832\n"
      "ds_read_b64_tr_b16 v[152:153], %[rw] offset:128\n"
      "ds_read_b64_tr_b16 v[154:155], %[rw] offset:896\n"
      "s_waitcnt vmcnt(4)\n"
      "1:\n"
      "s_cmp_lg_u32 %[cnt], 1\n"
      "s_cbranch_scc1 2f\n"
      "v_mov_b64 %[a0], %[n0]\n"
      "v_mov_b64 %[a1], %[n1]\n"
      "v_mov_b64 %[a2], %[n2]\n"
      "s_mov_b64 %[st], %[nst]\n"
      "s_mov_b32 s42, 0\n"
      "2:\n"
      "s_waitcnt vmcnt(12) lgkmcnt(0)\n"
      "v_mfma_f32_32x32x16_bf16 v[64:79], v[144:147], v[168:171], v[64:79]\n"
      "ds_read_b64_tr_b16 v[156:157], %[rw] offset:3072\n"
      "ds_read_b64_tr_b16 v[158:159], %[rw] offset:3840\n"
      "ds_read_b64_tr_b16 v[160:161], %[rw] offset:3136\n"
      "v_mfma_f32_32x32x16_bf16 v[80:95], v[144:147], v[172:175], v[80:95]\n"
      "ds_read_b64_tr_b16 v[162:163], %[rw] offset:3904\n"
      "ds_read_b64_tr_b16 v[164:165], %[rw] offset:3200\n"
      "ds_read_b64_tr_b16 v[166:167], %[rw] offset:3968\n"
      "s_add_u32 m0, %[ring], 0\n"
      "v_mfma_f32_32x32x16_bf16 v[96:111], v[148:151], v[168:171], v[96:111]\n"
      "global_load_lds_dwordx4 %[a0], off\n"
      "v_mfma_f32_32x32x16_bf16 v[112:127], v[148:151], v[172:175], v[112:127]\n"
      "global_load_lds_dwordx4 %[a1], off offset:1024\n"
      "v_mfma_f32_32x32x16_bf16 v[128:143], v[152:155], v[168:171], v[128:143]\n"
      "global_load_lds_dwordx4 %[a2], off offset:2048\n"
      "buffer_load_dwordx4 v[168:171], %[v0], s[40:43], %[so] offen sc1\n"
      "buffer_load_dwordx4 v[172:175], %[v1], s[40:43], %[so] offen sc1\n"
      "s_add_u32 %[so], %[so], 2048\n"
      "v_lshl_add_u64 %[a0], %[a0], 0, %[st]\n"
      "v_lshl_add_u64 %[a1], %[a1], 0, %[st]\n"
      "v_lshl_add_u64 %[a2], %[a2], 0, %[st]\n"
      "s_waitcnt vmcnt(12) lgkmcnt(0)\n"
      "v_mfma_f32_32x32x16_bf16 v[64:79], v[156:159], v[176:179], v[64:79]\n"
      "ds_read_b64_tr_b16 v[144:145], %[rw] offset:6144\n"
      "ds_read_b64_tr_b16 v[146:147], %[rw] offset:6912\n"
      "ds_read_b64_tr_b16 v[148:149], %[rw] offset:6208\n"
      "v_mfma_f32_32x32x16_bf16 v[80:95], v[156:159], v[180:183], v[80:95]\n"
      "ds_read_b64_tr_b16 v[150:151], %[rw] offset:6976\n"
      "ds_read_b64_tr_b16 v[152:153], %[rw] offset:6272\n"
      "ds_read_b64_tr_b16 v[154:155], %[rw] offset:7040\n"
      "s_add_u32 m0, %[ring], 3072\n"
      "v_mfma_f32_32x32x16_bf16 v[96:111], v[160:163], v[176:179], v[96:111]\n"
      "global_load_lds_dwordx4 %[a0], off\n"
      "v_mfma_f32_32x32x16_bf16 v[112:127], v[160:163], v[180:183], v[112:127]\n"
      "global_load_lds_dwordx4 %[a1], off offset:1024\n"
      "v_mfma_f32_32x32x16_bf16 v[128:143], v[164:167], v[176:179], v[128:143]\n"
      "global_load_lds_dwordx4 %[a2], off offset:2048\n"
      "buffer_load_dwordx4 v[176:179], %[v0], s[40:43], %[so] offen sc1\n"
      "buffer_load_dwordx4 v[180:183], %[v1], s[40:43], %[so] offen sc1\n"
      "s_add_u32 %[so], %[so], 2048\n"
      "v_lshl_add_u64 %[a0], %[a0], 0, %[st]\n"
      "v_lshl_add_u64 %[a1], %[a1], 0, %[st]\n"
      "v_lshl_add_u64 %[a2], %[a2], 0, %[st]\n"
      "s_waitcnt vmcnt(12) lgkmcnt(0)\n"
      "v_mfma_f32_32x32x16_bf16 v[64:79], v[144:147], v[184:187], v[64:79]\n"
      "ds_read_b64_tr_b16 v[156:157], %[rw] offset:9216\n"
      "ds_read_b64_tr_b16 v[158:159], %[rw] offset:9984\n"
      "ds_read_b64_tr_b16 v[160:161], %[rw] offset:9280\n"
      "v_mfma_f32_32x32x16_bf16 v[80:95], v[144:147], v[188:191], v[80:95]\n"
      "ds_read_b64_tr_b16 v[162:163], %[rw] offset:10048\n"
      "ds_read_b64_tr_b16 v[164:165], %[rw] offset:9344\n"
      "ds_read_b64_tr_b16 v[166:167], %[rw] offset:10112\n"
      "s_add_u32 m0, %[ring], 6144\n"
      "v_mfma_f32_32x32x16_bf16 v[96:111], v[148:151], v[184:187], v[96:111]\n"
      "global_load_lds_dwordx4 %[a0], off\n"
      "v_mfma_f32_32x32x16_bf16 v[112:127], v[148:151], v[188:191], v[112:127]\n"
      "global_load_lds_dwordx4 %[a1], off offset:1024\n"
      "v_mfma_f32_32x32x16_bf16 v[128:143], v[152:155], v[184:187], v[128:143]\n"
      "global_load_lds_dwordx4 %[a2], off offset:2048\n"
      "buffer_load_dwordx4 v[184:187], %[v0], s[40:43], %[so] offen sc1\n"
      "buffer_load_dwordx4 v[188:191], %[v1], s[40:43], %[so] offen sc1\n"
      "s_add_u32 %[so], %[so], 2048\n"
      "v_lshl_add_u64 %[a0], %[a0], 0, %[st]\n"
      "v_lshl_add_u64 %[a1], %[a1], 0, %[st]\n"
      "v_lshl_add_u64 %[a2], %[a2], 0, %[st]\n"
      "s_waitcnt vmcnt(12) lgkmcnt(0)\n"
      "v_mfma_f32_32x32x16_bf16 v[64:79], v[156:159], v[192:195], v[64:79]\n"
      "ds_read_b64_tr_b16 v[144:145], %[rw] offset:0\n"
      "ds_read_b64_tr_b16 v[146:147], %[rw] offset:768\n"
      "ds_read_b64_tr_b16 v[148:149], %[rw] offset:64\n"
      "v_mfma_f32_32x32x16_bf16 v[80:95], v[156:159], v[196:199], v[80:95]\n"
      "ds_read_b64_tr_b16 v[150:151], %[rw] offset:832\n"
      "ds_read_b64_tr_b16 v[152:153], %[rw] offset:128\n"
      "ds_read_b64_tr_b16 v[154:155], %[rw] offset:896\n"
      "s_add_u32 m0, %[ring], 9216\n"
      "v_mfma_f32_32x32x16_bf16 v[96:111], v[160:163], v[192:195], v[96:111]\n"
      "global_load_lds_dwordx4 %[a0], off\n"
      "v_mfma_f32_32x32x16_bf16 v[112:127], v[160:163], v[196:199], v[112:127]\n"
      "global_load_lds_dwordx4 %[a1], off offset:1024\n"
      "v_mfma_f32_32x32x16_bf16 v[128:143], v[164:167], v[192:195], v[128:143]\n"
      "global_load_lds_dwordx4 %[a2], off offset:2048\n"
      "buffer_load_dwordx4 v[192:195], %[v0], s[40:43], %[so] offen sc1\n"
      "buffer_load_dwordx4 v[196:199], %[v1], s[40:43], %[so] offen sc1\n"
      "s_add_u32 %[so], %[so], 2048\n"
      "v_lshl_add_u64 %[a0], %[a0], 0, %[st]\n"
      "v_lshl_add_u64 %[a1], %[a1], 0, %[st]\n"
      "v_lshl_add_u64 %[a2], %[a2], 0, %[st]\n"
      "s_sub_u32 %[cnt], %[cnt], 1\n"
      "s_cmp_lg_u32 %[cnt], 0\n"
      "s_cbranch_scc1 1b\n"
      "s_waitcnt vmcnt(0) lgkmcnt(0)\n"
      "s_nop 15\n"
            : "=&{v[64:79]}"(c0), "=&{v[80:95]}"(c1), "=&{v[96:111]}"(c2), "=&{v[112:127]}"(c3), "=&{v[128:143]}"(c4),
              [a0] "+v"(a[0]), [a1] "+v"(a[1]), [a2] "+v"(a[2]), [st] "+s"(st), [cnt] "+s"(cnt), [so] "+s"(so), "+{s[40:43]}"(srd)
            : [n0] "v"(no[0]), [n1] "v"(no[1]), [n2] "v"(no[2]), [nst] "s"(nst), [rw] "v"(rw), [ring] "s"(ring), [v0] "v"(v0), [v1] "v"(v1)
            : "memory", "scc", "v144", "v145", "v146", "v147", "v148", "v149", "v150", "v151", "v152", "v153", "v154", "v155", "v156", "v157", "v158", "v159", "v160", "v161", "v162", "v163", "v164", "v165", "v166", "v167", "v168", "v169", "v170", "v171", "v172", "v173", "v174", "v175", "v176", "v177", "v178", "v179", "v180", "v181", "v182", "v183", "v184", "v185", "v186", "v187", "v188", "v189", "v190", "v191", "v192", "v193", "v194", "v195", "v196", "v197", "v198", "v199");
      }
      lds_wait(lds, C_H, 4);
      if (PH & 2) lds_wait(lds, C_Q, 4);
      const u32 xq = (u32)(size_t)(lds + L_XN) + v * 20736;
      glds3(a[0], a[1], a[2], xq);
#pragma unroll
      for (int s = 0; s < 5; ++s) glds3(no[0] + (80 + 2 * s) * 1280, no[1] + (80 + 2 * s) * 1280, no[2] + (80 + 2 * s) * 1280, xq + (s + 1) * 3072);
      drain();
      __builtin_amdgcn_s_barrier();
      if (FUSED) poll_tag(ws + WS_OFLAG + (u32)(g3 * 8 + 2 * v + (lane & 1)) * 64, tag);
      {
        const u32x4 srd = mk_srd(ws + WS_ACT + (u32)(g3 * ACT_G), ACT_G);
        u32 so = (96 + 10 * v) * 2048;
        const u32 rw2 = rw - ring + xq;
        asm volatile(
      "s_nop 4\n"
      "buffer_load_dwordx4 v[168:171], %[v0], %[srd], %[so] offen sc1\n"
      "buffer_load_dwordx4 v[172:175], %[v1], %[srd], %[so] offen sc1\n"
      "s_add_u32 %[so], %[so], 2048\n"
      "buffer_load_dwordx4 v[176:179], %[v0], %[srd], %[so] offen sc1\n"
      "buffer_load_dwordx4 v[180:183], %[v1], %[srd], %[so] offen sc1\n"
      "s_add_u32 %[so], %[so], 2048\n"
      "buffer_load_dwordx4 v[184:187], %[v0], %[srd], %[so] offen sc1\n"
      "buffer_load_dwordx4 v[188:191], %[v1], %[srd], %[so] offen sc1\n"
      "s_add_u32 %[so], %[so], 2048\n"
      "buffer_load_dwordx4 v[192:195], %[v0], %[srd], %[so] offen sc1\n"
      "buffer_load_dwordx4 v[196:199], %[v1], %[srd], %[so] offen sc1\n"
      "s_add_u32 %[so], %[so], 2048\n"
      "buffer_load_dwordx4 v[200:203], %[v0], %[srd], %[so] offen sc1\n"
      "buffer_load_dwordx4 v[204:207], %[v1], %[srd], %[so] offen sc1\n"
      "s_add_u32 %[so], %[so], 2048\n"
      "buffer_load_dwordx4 v[208:211], %[v0], %[srd], %[so] offen sc1\n"
      "buffer_load_dwordx4 v[212:215], %[v1], %[srd], %[so] offen sc1\n"
      "s_add_u32 %[so], %[so], 2048\n"
      "buffer_load_dwordx4 v[216:219], %[v0], %[srd], %[so] offen sc1\n"
      "buffer_load_dwordx4 v[220:223], %[v1], %[srd], %[so] offen sc1\n"
      "s_add_u32 %[so], %[so], 2048\n"
      "buffer_load_dwordx4 v[224:227], %[v0], %[srd], %[so] offen sc1\n"
      "buffer_load_dwordx4 v[228:231], %[v1], %[srd], %[so] offen sc1\n"
      "s_add_u32 %[so], %[so], 2048\n"
      "buffer_load_dwordx4 v[232:235], %[v0], %[srd], %[so] offen sc1\n"
      "buffer_load_dwordx4 v[236:239], %[v1], %[srd], %[so] offen sc1\n"
      "s_add_u32 %[so], %[so], 2048\n"
      "buffer_load_dwordx4 v[240:243], %[v0], %[srd], %[so] offen sc1\n"
      "buffer_load_dwordx4 v[244:247], %[v1], %[srd], %[so] offen sc1\n"
      "s_add_u32 %[so], %[so], 2048\n"
      "ds_read_b64_tr_b16 v[144:145], %[rw] offset:0\n"
      "ds_read_b64_tr_b16 v[146:147], %[rw] offset:768\n"
      "ds_read_b64_tr_b16 v[148:149], %[rw] offset:64\n"
      "ds_read_b64_tr_b16 v[150:151], %[rw] offset:832\n"
      "ds_read_b64_tr_b16 v[152:153], %[rw] offset:128\n"
      "ds_read_b64_tr_b16 v[154:155], %[rw] offset:896\n"
      "s_waitcnt vmcnt(18) lgkmcnt(0)\n"
      "v_mfma_f32_32x32x16_bf16 v[64:79], v[144:147], v[168:171], v[64:79]\n"
      "ds_read_b64_tr_b16 v[156:157], %[rw] offset:3072\n"
      "ds_read_b64_tr_b16 v[158:159], %[rw] offset:3840\n"
      "ds_read_b64_tr_b16 v[160:161], %[rw] offset:3136\n"
      "v_mfma_f32_32x32x16_bf16 v[80:95], v[144:147], v[172:175], v[80:95]\n"
      "ds_read_b64_tr_b16 v[162:163], %[rw] offset:3904\n"
      "ds_read_b64_tr_b16 v[164:165], %[rw] offset:3200\n"
      "ds_read_b64_tr_b16 v[166:167], %[rw] offset:3968\n"
      "v_mfma_f32_32x32x16_bf16 v[96:111], v[148:151], v[168:171], v[96:111]\n"
      "v_mfma_f32_32x32x16_bf16 v[112:127], v[148:151], v[172:175], v[112:127]\n"
      "v_mfma_f32_32x32x16_bf16 v[128:143], v[152:155], v[168:171], v[128:143]\n"
      "s_waitcnt vmcnt(18) lgkmcnt(0)\n"
      "v_mfma_f32_32x32x16_bf16 v[64:79], v[156:159], v[176:179], v[64:79]\n"
      "ds_read_b64_tr_b16 v[156:157], %[rw] offset:3072\n"
      "ds_read_b64_tr_b16 v[158:159], %[rw] offset:3840\n"
      "ds_read_b64_tr_b16 v[160:161], %[rw] offset:3136\n"
      "v_mfma_f32_32x32x16_bf16 v[80:95], v[156:159], v[180:183], v[80:95]\n"
      "ds_read_b64_tr_b16 v[162:163], %[rw] offset:3904\n"
      "ds_read_b64_tr_b16 v[164:165], %[rw] offset:3200\n"
      "ds_read_b64_tr_b16 v[166:167], %[rw] offset:3968\n"
      "v_mfma_f32_32x32x16_bf16 v[96:111], v[160:163], v[176:179], v[96:111]\n"
      "v_mfma_f32_32x32x16_bf16 v[112:127], v[160:163], v[180:183], v[112:127]\n"
      "v_mfma_f32_32x32x16_bf16 v[128:143], v[164:167], v[176:179], v[128:143]\n"
      "s_waitcnt vmcnt(18) lgkmcnt(0)\n"
      "v_mfma_f32_32x32x16_bf16 v[64:79], v[144:147], v[184:187], v[64:79]\n"
      "ds_read_b64_tr_b16 v[156:157], %[rw] offset:3072\n"
      "ds_read_b64_tr_b16 v[158:159], %[rw] offset:3840\n"
      "ds_read_b64_tr_b16 v[160:161], %[rw] offset:3136\n"
      "v_mfma_f32_32x32x16_bf16 v[80:95], v[144:147], v[188:191], v[80:95]\n"
      "ds_read_b64_tr_b16 v[162:163], %[rw] offset:3904\n"
      "ds_read_b64_tr_b16 v[164:165], %[rw] offset:3200\n"
      "ds_read_b64_tr_b16 v[166:167], %[rw] offset:3968\n"
      "v_mfma_f32_32x32x16_bf16 v[96:111], v[148:151], v[184:187], v[96:111]\n"
      "v_mfma_f32_32x32x16_bf16 v[112:127], v[148:151], v[188:191], v[112:127]\n"
      "v_mfma_f32_32x32x16_bf16 v[128:143], v[152:155], v[184:187], v[128:143]\n"
      "s_waitcnt vmcnt(18) lgkmcnt(0)\n"
      "v_mfma_f32_32x32x16_bf16 v[64:79], v[156:159], v[192:195], v[64:79]\n"
      "ds_read_b64_tr_b16 v[156:157], %[rw] offset:3072\n"
      "ds_read_b64_tr_b16 v[158:159], %[rw] offset:3840\n"
      "ds_read_b64_tr_b16 v[160:161], %[rw] offset:3136\n"
      "v_mfma_f32_32x32x16_bf16 v[80:95], v[156:159], v[196:199], v[80:95]\n"
      "ds_read_b64_tr_b16 v[162:163], %[rw] offset:3904\n"
      "ds_read_b64_tr_b16 v[164:165], %[rw] offset:3200\n"
      "ds_read_b64_tr_b16 v[166:167], %[rw] offset:3968\n"
      "v_mfma_f32_32x32x16_bf16 v[96:111], v[160:163], v[192:195], v[96:111]\n"
      "v_mfma_f32_32x32x16_bf16 v[112:127], v[160:163], v[196:199], v[112:127]\n"
      "v_mfma_f32_32x32x16_bf16 v[128:143], v[164:167], v[192:195], v[128:143]\n"
      "s_waitcnt vmcnt(18) lgkmcnt(0)\n"
      "v_mfma_f32_32x32x16_bf16 v[64:79], v[144:147], v[200:203], v[64:79]\n"
      "ds_read_b64_tr_b16 v[156:157], %[rw] offset:3072\n"
      "ds_read_b64_tr_b16 v[158:159], %[rw] offset:3840\n"
      "ds_read_b64_tr_b16 v[160:161], %[rw] offset:3136\n"
      "v_mfma_f32_32x32x16_bf16 v[80:95], v[144:147], v[204:207], v[80:95]\n"
      "ds_read_b64_tr_b16 v[162:163], %[rw] offset:3904\n"
      "ds_read_b64_tr_b16 v[164:165], %[rw] offset:3200\n"
      "ds_read_b64_tr_b16 v[166:167], %[rw] offset:3968\n"
      "v_mfma_f32_32x32x16_bf16 v[96:111], v[148:151], v[200:203], v[96:111]\n"
      "v_mfma_f32_32x32x16_bf16 v[112:127], v[148:151], v[204:207], v[112:127]\n"
      "v_mfma_f32_32x32x16_bf16 v[128:143], v[152:155], v[200:203], v[128:143]\n"
      "s_waitcnt vmcnt(18) lgkmcnt(0)\n"
      "v_mfma_f32_32x32x16_bf16 v[64:79], v[156:159], v[208:211], v[64:79]\n"
      "ds_read_b64_tr_b16 v[156:157], %[rw] offset:3072\n"
      "ds_read_b64_tr_b16 v[158:159], %[rw] offset:3840\n"
      "ds_read_b64_tr_b16 v[160:161], %[rw] offset:3136\n"
      "v_mfma_f32_32x32x16_bf16 v[80:95], v[156:159], v[212:215], v[80:95]\n"
      "ds_read_b64_tr_b16 v[162:163], %[rw] offset:3904\n"
      "ds_read_b64_tr_b16 v[164:165], %[rw] offset:3200\n"
      "ds_read_b64_tr_b16 v[166:167], %[rw] offset:3968\n"
      "v_mfma_f32_32x32x16_bf16 v[96:111], v[160:163], v[208:211], v[96:111]\n"
      "v_mfma_f32_32x32x16_bf16 v[112:127], v[160:163], v[212:215], v[112:127]\n"
      "v_mfma_f32_32x32x16_bf16 v[128:143], v[164:167], v[208:211], v[128:143]\n"
      "s_waitcnt vmcnt(18) lgkmcnt(0)\n"
      "v_mfma_f32_32x32x16_bf16 v[64:79], v[144:147], v[216:219], v[64:79]\n"
      "ds_read_b64_tr_b16 v[156:157], %[rw] offset:3072\n"
      "ds_read_b64_tr_b16 v[158:159], %[rw] offset:3840\n"
      "ds_read_b64_tr_b16 v[160:161], %[rw] offset:3136\n"
      "v_mfma_f32_32x32x16_bf16 v[80:95], v[144:147], v[220:223], v[80:95]\n"
      "ds_read_b64_tr_b16 v[162:163], %[rw] offset:3904\n"
      "ds_read_b64_tr_b16 v[164:165], %[rw] offset:3200\n"
      "ds_read_b64_tr_b16 v[166:167], %[rw] offset:3968\n"
      "v_mfma_f32_32x32x16_bf16 v[96:111], v[148:151], v[216:219], v[96:111]\n"
      "v_mfma_f32_32x32x16_bf16 v[112:127], v[148:151], v[220:223], v[112:127]\n"
      "v_mfma_f32_32x32x16_bf16 v[128:143], v[152:155], v[216:219], v[128:143]\n"
      "s_waitcnt vmcnt(18) lgkmcnt(0)\n"
      "v_mfma_f32_32x32x16_bf16 v[64:79], v[156:159], v[224:227], v[64:79]\n"
      "ds_read_b64_tr_b16 v[156:157], %[rw] offset:3072\n"
      "ds_read_b64_tr_b16 v[158:159], %[rw] offset:3840\n"
      "ds_read_b64_tr_b16 v[160:161], %[rw] offset:3136\n"
      "v_mfma_f32_32x32x16_bf16 v[80:95], v[156:159], v[228:231], v[80:95]\n"
      "ds_read_b64_tr_b16 v[162:163], %[rw] offset:3904\n"
      "ds_read_b64_tr_b16 v[164:165], %[rw] offset:3200\n"
      "ds_read_b64_tr_b16 v[166:167], %[rw] offset:3968\n"
      "v_mfma_f32_32x32x16_bf16 v[96:111], v[160:163], v[224:227], v[96:111]\n"
      "v_mfma_f32_32x32x16_bf16 v[112:127], v[160:163], v[228:231], v[112:127]\n"
      "v_mfma_f32_32x32x16_bf16 v[128:143], v[164:167], v[224:227], v[128:143]\n"
      "s_waitcnt vmcnt(18) lgkmcnt(0)\n"
      "v_mfma_f32_32x32x16_bf16 v[64:79], v[144:147], v[232:235], v[64:79]\n"
      "ds_read_b64_tr_b16 v[156:157], %[rw] offset:3072\n"
      "ds_read_b64_tr_b16 v[158:159], %[rw] offset:3840\n"
      "ds_read_b64_tr_b16 v[160:161], %[rw] offset:3136\n"
      "v_mfma_f32_32x32x16_bf16 v[80:95], v[144:147], v[236:239], v[80:95]\n"
      "ds_read_b64_tr_b16 v[162:163], %[rw] offset:3904\n"
      "ds_read_b64_tr_b16 v[164:165], %[rw] offset:3200\n"
      "ds_read_b64_tr_b16 v[166:167], %[rw] offset:3968\n"
      "v_mfma_f32_32x32x16_bf16 v[96:111], v[148:151], v[232:235], v[96:111]\n"
      "v_mfma_f32_32x32x16_bf16 v[112:127], v[148:151], v[236:239], v[112:127]\n"
      "v_mfma_f32_32x32x16_bf16 v[128:143], v[152:155], v[232:235], v[128:143]\n"
      "s_waitcnt vmcnt(18) lgkmcnt(0)\n"
      "v_mfma_f32_32x32x16_bf16 v[64:79], v[156:159], v[240:243], v[64:79]\n"
      "ds_read_b64_tr_b16 v[156:157], %[rw] offset:3072\n"
      "ds_read_b64_tr_b16 v[158:159], %[rw] offset:3840\n"
      "ds_read_b64_tr_b16 v[160:161], %[rw] offset:3136\n"
      "v_mfma_f32_32x32x16_bf16 v[80:95], v[156:159], v[244:247], v[80:95]\n"
      "ds_read_b64_tr_b16 v[162:163], %[rw] offset:3904\n"
      "ds_read_b64_tr_b16 v[164:165], %[rw] offset:3200\n"
      "ds_read_b64_tr_b16 v[166:167], %[rw] offset:3968\n"
      "v_mfma_f32_32x32x16_bf16 v[96:111], v[160:163], v[240:243], v[96:111]\n"
      "v_mfma_f32_32x32x16_bf16 v[112:127], v[160:163], v[244:247], v[112:127]\n"
      "v_mfma_f32_32x32x16_bf16 v[128:143], v[164:167], v[240:243], v[128:143]\n"
      "s_nop 15\n"
            : "+{v[64:79]}"(c0), "+{v[80:95]}"(c1), "+{v[96:111]}"(c2), "+{v[112:127]}"(c3), "+{v[128:143]}"(c4), [so] "+s"(so)
            : [srd] "s"(srd), [rw] "v"(rw), [rw2] "v"(rw2), [v0] "v"(v0), [v1] "v"(v1)
            : "memory", "scc", "v144", "v145", "v146", "v147", "v148", "v149", "v150", "v151", "v152", "v153", "v154", "v155", "v156", "v157", "v158", "v159", "v160", "v161", "v162", "v163", "v164", "v165", "v166", "v167", "v168", "v169", "v170", "v171", "v172", "v173", "v174", "v175", "v176", "v177", "v178", "v179", "v180", "v181", "v182", "v183", "v184", "v185", "v186", "v187", "v188", "v189", "v190", "v191", "v192", "v193", "v194", "v195", "v196", "v197", "v198", "v199", "v200", "v201", "v202", "v203", "v204", "v205", "v206", "v207", "v208", "v209", "v210", "v211", "v212", "v213", "v214", "v215", "v216", "v217", "v218", "v219", "v220", "v221", "v222", "v223", "v224", "v225", "v226", "v227", "v228", "v229", "v230", "v231", "v232", "v233", "v234", "v235", "v236", "v237", "v238", "v239", "v240", "v241", "v242", "v243", "v244", "v245", "v246", "v247");
      }
      LAS char* pp = lds + L_XN + v * 20736 + lane * 16;
#define PST(c, n) for (int r = 0; r < 4; ++r) *(LAS f32x4*)(pp + ((n) * 4 + r) * 1024) = f32x4{c[4 * r], c[4 * r + 1], c[4 * r + 2], c[4 * r + 3]}
      PST(c0, 0); PST(c1, 1); PST(c2, 2); PST(c3, 3); PST(c4, 4);
    } else {
      __builtin_amdgcn_s_barrier();
    }
  }
  if (PH & 4) {
    asm volatile("s_waitcnt lgkmcnt(0)" ::: "memory");
    __builtin_amdgcn_s_barrier();
    asm volatile("" ::: "memory");
    const int odd = j3 & 1;
    for (int un = tid; un < 640; un += 512) {
      const int n = un >> 7, tok = (un >> 2) & 31, rq = un & 3;
      const int t = n >> 1, tb = odd ^ (n & 1);
      const int col = 160 * (j3 >> 1) + 64 * odd + ((t + odd) % 3) * 32 + rq * 8;
      const u32 go = (u32)((64 * g3 + 32 * tb + tok) * 1280 + col * 2);
      const u32x4 xv = *(const u32x4*)(x + go);
      f32x4 lo = {}, hi = {};
#pragma unroll
      for (int p = 0; p < 4; ++p) {
        LAS char* s = lds + L_XN + p * 20736 + (n * 4 + rq) * 1024 + tok * 16;
        lo += *(LAS f32x4*)s;
        hi += *(LAS f32x4*)(s + 512);
      }
      u32x4 o = {pk(lo[0] + bflo(xv[0]), lo[1] + bfhi(xv[0])), pk(lo[2] + bflo(xv[1]), lo[3] + bfhi(xv[1])),
                 pk(hi[0] + bflo(xv[2]), hi[1] + bfhi(xv[2])), pk(hi[2] + bflo(xv[3]), hi[3] + bfhi(xv[3]))};
      *(u32x4*)(out + go) = o;
    }
  }
}

template <int PH>
static void launch(void* const* d_in, void* d_out, void* d_ws, hipStream_t stream) {
  (void)hipFuncSetAttribute((const void*)fused_kernel<PH>, hipFuncAttributeMaxDynamicSharedMemorySize, LDS_BYTES);
  fused_kernel<PH><<<256, 512, LDS_BYTES, stream>>>((const char*)d_in[0], (const char*)d_in[1], (const char*)d_in[2], (const char*)d_in[3],
                                                    (const char*)d_in[4], (const char*)d_in[6], (const char*)d_in[7], (const char*)d_in[8],
                                                    (char*)d_out, (char*)d_ws);
}

extern "C" void kernel_launch(void* const* d_in, const int* in_sizes, int n_in, void* d_out, int out_size, void* d_ws, size_t ws_size,
                              hipStream_t stream) {
  launch<7>(d_in, d_out, d_ws, stream);
}
```

```cpp
#include <hip/hip_runtime.h>
#include <hip/hip_bf16.h>
#include <stdint.h>

typedef unsigned int u32;
typedef unsigned long long u64;
typedef __bf16 bf16x2 __attribute__((ext_vector_type(2)));
typedef __bf16 bf16x8 __attribute__((ext_vector_type(8)));
typedef short s16x4 __attribute__((ext_vector_type(4)));
typedef float f32x2 __attribute__((ext_vector_type(2)));
typedef float f32x8 __attribute__((ext_vector_type(8)));
typedef __bf16 bf16x16 __attribute__((ext_vector_type(16)));
typedef float f32x16 __attribute__((ext_vector_type(16)));
typedef u32 u32x2 __attribute__((ext_vector_type(2)));
typedef u32 u32x4 __attribute__((ext_vector_type(4)));
typedef u32 u32x8 __attribute__((ext_vector_type(8)));
typedef u32 u32x16 __attribute__((ext_vector_type(16)));
typedef u32 u32x32 __attribute__((ext_vector_type(32)));
#define LAS __attribute__((address_space(3)))
#define DI __device__ __forceinline__

constexpr u32 WS_G = 0;
constexpr u32 WS_KVFLAG = 1024;
constexpr u32 WS_HFLAG = 4096;
constexpr u32 WS_OFLAG = 4096 + 16384;
constexpr u32 WS_KV = 65536;
constexpr u32 REC = 2624, CHUNK = 16 * REC;
constexpr u32 WS_ACT = 65536 + 262144;
constexpr u32 ACT_G = 68 * 4096;
constexpr u32 WS_WD = WS_ACT + 32 * ACT_G;
constexpr u32 WD_J = 68 * 5120;
constexpr u32 XS = 1312;
constexpr u32 L_XN = 0;
constexpr u32 SLOT = 14848;
constexpr u32 L_GR = 64 * XS;
constexpr u32 L_AR = L_GR + 3 * SLOT;
constexpr u32 AR_SZ = SLOT / 2;
constexpr u32 L_SPARE = L_GR + 5 * SLOT;
constexpr u32 L_JUNK = L_SPARE + 5120;
constexpr u32 L_MISC = L_JUNK + 256;
constexpr u32 LDS_BYTES = L_MISC + 256;
constexpr u32 L_QX = 3 * CHUNK;
constexpr u32 L_RT = L_QX + 10240;
constexpr u32 L_PAIR = 2 * CHUNK;
static_assert(L_GR == 2 * CHUNK && L_RT + 25600 <= L_JUNK && LDS_BYTES <= 163840, "LDS map");
enum { C_H = 2, C_WO = 3, C_PAIR = 4  , C_RDY = 16  , C_DONE = 20  , W_UNSAFE = 24, W_QLIM = 25, C_LNA = 27  , C_XCH = 30, C_BFLY = 32   };

constexpr int CODE_LINES = 262;

DI int lane_id() {
  int l;
  asm volatile("v_mbcnt_lo_u32_b32 %0, -1, 0\n\tv_mbcnt_hi_u32_b32 %0, -1, %0" : "=v"(l));
  __builtin_assume(l >= 0 && l < 64);
  return l;
}
DI u32 pk(float a, float b) { f32x2 f = {a, b}; return __builtin_bit_cast(u32, __builtin_convertvector(f, bf16x2)); }
DI float asf(u32 u) { return __builtin_bit_cast(float, u); }
DI float bflo(u32 u) { return __builtin_bit_cast(float, u << 16); }
DI float rbf(float a) { return bflo(pk(a, 0.f)); }
DI float max32(float v) { u32 a = __builtin_bit_cast(u32, v); auto r = __builtin_amdgcn_permlane32_swap(a, a, false, false); u32 r0 = r[0], r1 = r[1]; return __builtin_fmaxf(asf(r0), asf(r1)); }
DI f32x16 mfma16(bf16x8 a, bf16x8 b, f32x16 c) { return __builtin_amdgcn_mfma_f32_32x32x16_bf16(a, b, c, 0, 0, 0); }
DI void touch(const char* p, u32 junk) {
  asm volatile("s_mov_b32 m0, %1\n\ts_nop 0\n\tglobal_load_lds_dword %0, off sc1" ::"v"(p), "s"(junk) : "memory");
}
DI u32x4 mk_srd(const void* p, u32 bytes) {
  u64 a = (u64)p;
  u32x4 r = {(u32)a, (u32)(a >> 32) & 0xffffu, bytes, 0x00020000u};
  return r;
}
DI void st4_sc1(char* p, u32 v) { asm volatile("global_store_dword %0, %1, off sc1" ::"v"(p), "v"(v) : "memory"); }
#define LD1(R, i) "global_load_dwordx4 v[" #R "+4*" #i ":" #R "+4*" #i "+3], %2, %3 offset:128*" #i "\n\t"
#define LOAD10(R) LD1(R, 0) LD1(R, 1) LD1(R, 2) LD1(R, 3) LD1(R, 4) LD1(R, 5) LD1(R, 6) LD1(R, 7) LD1(R, 8) LD1(R, 9)
DI u32 make_tag(u32 gv) {
  const u32 g0 = __builtin_amdgcn_readfirstlane(gv), tag = g0 * 0x9E3779B1u + 0x7F4A7C15u;
  return tag == g0 ? tag ^ 0x10u : tag;
}

#define IDX_DEFS                                                                                        \
  const int xcd = bid & 7, loc = bid >> 3, wl = w & 3;                                                  \
  const int g1 = (xcd & 1) * 16 + (loc >> 1), j1 = (xcd >> 1) * 2 + (loc & 1);                          \
  const int g3 = ((xcd & 1) ^ 1) * 16 + (xcd >> 1) * 4 + (loc >> 3), j3 = loc & 7;                      \
  const u32 k0 = 2 * ((5 * (loc >> 1)) >> 2);                                                           \
  const int kind = j1 >> 2, tq = j1 & 3;                             \
  const u32x4 srw = mk_srd((wl < 2 ? wg : wu) - 2048, 640 * 3072 + 2048), srq = mk_srd(wq, 640 * 1280);                 \
  const u32 dw = ringb + wl * 3072, dq = ringb + 12288 + wl * 1024;                                     \
  const u32 qhi = wl < 2 ? ~0u : (wl == 2 ? 0u : 7u);                                                   \
  (void)g3; (void)j3; (void)k0; (void)kind; (void)srw; (void)srq; (void)dw; (void)dq; (void)qhi
#define LANE_DEFS                                                                                       \
  const int lane = lane_id(), l31 = lane & 31, h = lane >> 5, gq = lane >> 4, i16 = lane & 15;          \
  const int trq = i16 >> 2, trp = lane & 3, trb = gq & 1;                     \
  const u32 vo = lane * 16;                                                                             \
  (void)l31; (void)h; (void)trq; (void)trp; (void)trb; (void)vo
#define DMA_SETUP                                                                                       \
  u32 od_[3], oq;                                                                                       \
  od_[0] = (u32)((8 * (wl & 1) + (lane >> 3)) * 3072 + 384 * j1 + 2048 + (((lane & 7) ^ ((lane >> 3) & 6)) * 16)); \
  od_[1] = od_[0] - 896; od_[2] = od_[0] - 1792;                                                        \
  const int nq = 64 * wl + lane < 160 ? 64 * wl + lane : 159;                                           \
  oq = (u32)((nq / 10) * 1280 + 160 * j1 + (nq % 10) * 16)
#define XN_DST (ldsb + L_XN + (ra + (lane >> 3)) * XS + ((lane >> 2) & 1) * 64 + (lane & 1) * 32 + ((lane >> 1) & 1) * 8)
#define DMA_OPS [o0] "v"(od_[0]), [o1] "v"(od_[1]), [o2] "v"(od_[2]), [oq] "v"(oq), [dw] "s"(dw), [dq] "s"(dq), [qhi] "s"(qhi), \
                "{s[40:43]}"(srw), "{s[44:47]}"(srq)

__global__ void __launch_bounds__(512, 2)
fused_kernel(const char* __restrict__ x, const char* __restrict__ wq, const char* __restrict__ wk, const char* __restrict__ wv,
             const char* __restrict__ wo, const char* __restrict__ wg, const char* __restrict__ wu, const char* __restrict__ wd,
             char* __restrict__ out, char* __restrict__ ws) {
  u64 pc;
  asm volatile("s_getpc_b64 %0" : "=s"(pc));
  extern __shared__ __attribute__((aligned(16))) char smem[];
  LAS char* lds = (LAS char*)smem;
  int w = __builtin_amdgcn_readfirstlane(__builtin_amdgcn_workitem_id_x() >> 6);
  int bid = __builtin_amdgcn_workgroup_id_x();
  const u32 ldsb = (u32)(size_t)lds, junk = ldsb + L_JUNK, ringb = ldsb + L_GR;
  u32 xo, ra;
  u32x32 xa;
  u32x8 xa2;

  {
    const u32 tid = __builtin_amdgcn_workitem_id_x(), n = (bid >> 3) + ((tid >> 6) + (tid & 1) * 8) * 32;
    asm volatile("s_mov_b32 m0, %2\n\ts_nop 0\n\tglobal_load_lds_dword %0, %1" ::"v"((n < CODE_LINES ? n : CODE_LINES - 1) * 128), "s"(pc), "s"(junk) : "memory");
    ra = 32 * ((w >> 1) & 1) + 16 * (((bid >> 3) ^ (w >> 2)) & 1) + 8 * (w & 1);
    xo = (u32)(((bid & 1) * 1024 + (bid >> 4) * 64 + ra) * 1280) + ((tid >> 3) & 7) * 1280 + (tid & 7) * 16;
    asm volatile("s_cmp_gt_u32 %4, 3\n\ts_cbranch_scc1 1f\n\t" LOAD10(192) "1:"
                 : "=&{v[192:223]}"(xa), "=&{v[224:231]}"(xa2) : "v"(xo), "s"(x), "s"(w) : "memory", "scc");
    __builtin_amdgcn_sched_barrier(0);
  }

  if (w >= 4) {
    __builtin_amdgcn_s_setprio(3);
    IDX_DEFS;
    const int v = w - 4;
    const bool unit = loc < 17;
    const int bks = unit ? 4 * loc + v : 0;
    LANE_DEFS;
    DMA_SETUP;
    u32 gv, uo[5];
    {
      const int ko = 2 * bks - 96, nat = bks % 3 == 1;
      const int b0 = bks < 48 ? 32 * bks : 80 * (ko / 5) + 2 * (ko % 5);
      const int b1 = bks < 48 ? b0 + (nat ? 16 : 8) : 80 * ((ko + 1) / 5) + 2 * ((ko + 1) % 5);
      const int c1 = bks < 48 ? (nat ? 8 : 4) : 10, c2 = bks < 48 ? (nat ? 4 : 16) : 40, c3 = bks < 48 ? 2 : 20;
#pragma unroll
      for (int i = 0; i < 5; ++i) {
        const int n = i * 64 + lane, rho = n / 10;
        const int row = (rho & 16 ? b1 : b0) + c1 * ((rho >> 3) & 1) + c2 * ((rho >> 2) & 1) + c3 * ((rho >> 1) & 1) + (rho & 1);
        uo[i] = (u32)(row * 1280 + xcd * 160 + (n - 10 * rho) * 16);
      }
    }
    u32 rx0 = ldsb + L_XN + i16 * XS + gq * 16;
    const int rr = 4 * gq + trq, gx = (rr >> 1) & 3, p0 = 3 * v + (v & 1), pl = (v & 1) ? 3 * v : 3 * v + 2;
    const u32 rwb = ringb + (rr >> 3) * 3072 + (rr & 7) * 128 + trp * 8;
#define RW(c) (rwb + ((c) >> 2) * 1024 + (((c) & 3) ^ gx) * 32)
    u32 rw0 = RW(p0), rw1 = RW(p0 + 1), rw2 = RW(pl);
    u32 rq = ringb + 12288 + rr * 160 + v * 40 + trp * 8, qx = ldsb + L_QX + v * 2048 + i16 * 32 + (gq & 1) * 16 + (gq >> 1) * 8;
    u32x16 GP = {uo[0], uo[1], uo[2], uo[3], uo[4], rx0, rw0, rw1, rw2, rw0 + 6144, rw1 + 6144, rw2 + 6144, rq, qx, rx0 + k0 * 32, ldsb + L_QX + 8192 + v * 512 + i16 * 8};
    u32x16 GA, SA;
    u32x8 GB, SB;
    u32x4 SC, SW, SH, SK;
    {
      const u32 lv = ldsb + L_PAIR + vo, f1 = ldsb + L_MISC + (C_BFLY + v) * 4;
      GA = u32x16{vo, vo + 4096, vo + v * 1024, vo + (v ^ 1) * 1024, vo + (v ^ 2) * 1024, vo + (v ^ 3) * 1024,
                  lv + v * 10240, lv + (v ^ 2) * 10240, lv + (v ^ 3) * 10240, f1, f1 ^ 8, f1 ^ 20, ldsb + L_MISC, 1u, 0u,
                  (u32)(48 * 5120 + (loc >> 3) * 25600 + v * 6400 + (lane < 50 ? lane : 49) * 128)};
      const int pn_ = lane < 10 ? lane : 9;
      const int pb = pn_ < 2 ? (2 * (g3 & 15) + pn_) * 8 + 2 * v + (g3 >> 4) : (pn_ < 5 ? 3 * v + pn_ - 2 : 7 + pn_) * 8 + j3;
      GB = u32x8{(u32)((gq & 1) * 1024 + (2 * (v & 1) + (gq >> 1)) * 256 + i16 * 16), ldsb + L_RT + v * 5120 + (8 * gq + trq) * 160 + trp * 40,
                 (u32)((64 * g3 + 16 * v + (lane >> 2)) * 1280 + j3 * 160 + (lane & 3) * 52), (u32)pb * 64, 0u, 0u, 0u, 0u};
    }
#define GPAR "+{v[8:23]}"(GA), "+{v[24:31]}"(GB), "+{v[32:47]}"(GP)
    asm volatile("s_barrier" : GPAR, "+v"(od_[0]), "+v"(od_[1]), "+v"(od_[2]), "+v"(oq)::"memory");
    {
      u32 sw = k0 * (16 * 3072), sq = k0 * (16 * 1280);
      asm volatile(
      "v_mov_b32 %[gv], 0\n"
      "s_nop 3\n"
      "global_load_dword %[gv], %[gv], %[ep] sc1\n"
      "global_load_dwordx4 v[192:195], %[xo], %[x] offset:0\n"
      "global_load_dwordx4 v[196:199], %[xo], %[x] offset:128\n"
      "global_load_dwordx4 v[200:203], %[xo], %[x] offset:256\n"
      "global_load_dwordx4 v[204:207], %[xo], %[x] offset:384\n"
      "global_load_dwordx4 v[208:211], %[xo], %[x] offset:512\n"
      "global_load_dwordx4 v[212:215], %[xo], %[x] offset:640\n"
      "global_load_dwordx4 v[216:219], %[xo], %[x] offset:768\n"
      "global_load_dwordx4 v[220:223], %[xo], %[x] offset:896\n"
      "global_load_dwordx4 v[224:227], %[xo], %[x] offset:1024\n"
      "global_load_dwordx4 v[228:231], %[xo], %[x] offset:1152\n"
      "s_mov_b32 m0, %[ard]\n"
      "s_nop 0\n"
      "global_load_lds_dwordx4 %[vo], %[wsrc] offset:0\n"
      "global_load_lds_dwordx4 %[vo], %[wsrc] offset:1024\n"
      "global_load_lds_dwordx4 %[vo], %[wsrc] offset:2048\n"
      "global_load_lds_dwordx4 %[vo], %[wsrc] offset:3072\n"
      "s_add_u32 m0, %[ard], 4096\n"
      "s_nop 0\n"
      "global_load_lds_dwordx4 %[vo4], %[wsrc]\n"
      "global_load_lds_dwordx4 %[vo4], %[wsrc] offset:1024\n"
      "s_mov_b64 exec, 0xffff\n"
      "global_load_lds_dwordx4 %[vo4], %[wsrc] offset:2048\n"
      "s_mov_b64 exec, -1\n"
      "s_add_u32 m0, %[dw], 0\n"
      "s_nop 0\n"
      "buffer_load_dwordx4 %[o0], s[40:43], %[sw] offen lds\n"
      "buffer_load_dwordx4 %[o1], s[40:43], %[sw] offen offset:1024 lds\n"
      "buffer_load_dwordx4 %[o2], s[40:43], %[sw] offen offset:2048 lds\n"
      "s_cmp_eq_u32 %[qhi], 7\n"
      "s_cbranch_scc1 101f\n"
      "s_add_u32 m0, %[dq], 0\n"
      "s_mov_b32 exec_hi, %[qhi]\n"
      "buffer_load_dwordx4 %[oq], s[44:47], %[sq] offen lds\n"
      "s_mov_b32 exec_hi, -1\n"
      "101:\n"
      "s_add_u32 %[sw], %[sw], 49152\n"
      "s_add_u32 %[sq], %[sq], 20480\n"
      "s_cmp_lg_u32 %[sq], 819200\n"
      "s_cselect_b32 %[sw], %[sw], 0\n"
      "s_cselect_b32 %[sq], %[sq], 0\n"
      "s_setprio 1\n"
      "s_mov_b32 s94, 0xffff0000\n"
      "s_waitcnt vmcnt(18)\n"
      "v_dot2_f32_bf16 v128, v192, %[ones], 0\n"
      "v_dot2_f32_bf16 v132, v192, v192, 0\n"
      "v_dot2_f32_bf16 v129, v193, %[ones], 0\n"
      "v_dot2_f32_bf16 v133, v193, v193, 0\n"
      "v_dot2_f32_bf16 v130, v194, %[ones], 0\n"
      "v_dot2_f32_bf16 v134, v194, v194, 0\n"
      "v_dot2_f32_bf16 v131, v195, %[ones], 0\n"
      "v_dot2_f32_bf16 v135, v195, v195, 0\n"
      "s_nop 1\n"
      "v_dot2c_f32_bf16 v128, v196, %[ones]\n"
      "v_dot2c_f32_bf16 v132, v196, v196\n"
      "v_dot2c_f32_bf16 v129, v197, %[ones]\n"
      "v_dot2c_f32_bf16 v133, v197, v197\n"
      "v_dot2c_f32_bf16 v130, v198, %[ones]\n"
      "v_dot2c_f32_bf16 v134, v198, v198\n"
      "v_dot2c_f32_bf16 v131, v199, %[ones]\n"
      "v_dot2c_f32_bf16 v135, v199, v199\n"
      "s_waitcnt vmcnt(16)\n"
      "v_dot2c_f32_bf16 v128, v200, %[ones]\n"
      "v_dot2c_f32_bf16 v132, v200, v200\n"
      "v_dot2c_f32_bf16 v129, v201, %[ones]\n"
      "v_dot2c_f32_bf16 v133, v201, v201\n"
      "v_dot2c_f32_bf16 v130, v202, %[ones]\n"
      "v_dot2c_f32_bf16 v134, v202, v202\n"
      "v_dot2c_f32_bf16 v131, v203, %[ones]\n"
      "v_dot2c_f32_bf16 v135, v203, v203\n"
      "v_dot2c_f32_bf16 v128, v204, %[ones]\n"
      "v_dot2c_f32_bf16 v132, v204, v204\n"
      "v_dot2c_f32_bf16 v129, v205, %[ones]\n"
      "v_dot2c_f32_bf16 v133, v205, v205\n"
      "v_dot2c_f32_bf16 v130, v206, %[ones]\n"
      "v_dot2c_f32_bf16 v134, v206, v206\n"
      "v_dot2c_f32_bf16 v131, v207, %[ones]\n"
      "v_dot2c_f32_bf16 v135, v207, v207\n"
      "s_waitcnt vmcnt(14)\n"
      "v_dot2c_f32_bf16 v128, v208, %[ones]\n"
      "v_dot2c_f32_bf16 v132, v208, v208\n"
      "v_dot2c_f32_bf16 v129, v209, %[ones]\n"
      "v_dot2c_f32_bf16 v133, v209, v209\n"
      "v_dot2c_f32_bf16 v130, v210, %[ones]\n"
      "v_dot2c_f32_bf16 v134, v210, v210\n"
      "v_dot2c_f32_bf16 v131, v211, %[ones]\n"
      "v_dot2c_f32_bf16 v135, v211, v211\n"
      "v_dot2c_f32_bf16 v128, v212, %[ones]\n"
      "v_dot2c_f32_bf16 v132, v212, v212\n"
      "v_dot2c_f32_bf16 v129, v213, %[ones]\n"
      "v_dot2c_f32_bf16 v133, v213, v213\n"
      "v_dot2c_f32_bf16 v130, v214, %[ones]\n"
      "v_dot2c_f32_bf16 v134, v214, v214\n"
      "v_dot2c_f32_bf16 v131, v215, %[ones]\n"
      "v_dot2c_f32_bf16 v135, v215, v215\n"
      "s_waitcnt vmcnt(12)\n"
      "v_dot2c_f32_bf16 v128, v216, %[ones]\n"
      "v_dot2c_f32_bf16 v132, v216, v216\n"
      "v_dot2c_f32_bf16 v129, v217, %[ones]\n"
      "v_dot2c_f32_bf16 v133, v217, v217\n"
      "v_dot2c_f32_bf16 v130, v218, %[ones]\n"
      "v_dot2c_f32_bf16 v134, v218, v218\n"
      "v_dot2c_f32_bf16 v131, v219, %[ones]\n"
      "v_dot2c_f32_bf16 v135, v219, v219\n"
      "v_dot2c_f32_bf16 v128, v220, %[ones]\n"
      "v_dot2c_f32_bf16 v132, v220, v220\n"
      "v_dot2c_f32_bf16 v129, v221, %[ones]\n"
      "v_dot2c_f32_bf16 v133, v221, v221\n"
      "v_dot2c_f32_bf16 v130, v222, %[ones]\n"
      "v_dot2c_f32_bf16 v134, v222, v222\n"
      "v_dot2c_f32_bf16 v131, v223, %[ones]\n"
      "v_dot2c_f32_bf16 v135, v223, v223\n"
      "s_waitcnt vmcnt(10)\n"
      "v_dot2c_f32_bf16 v128, v224, %[ones]\n"
      "v_dot2c_f32_bf16 v132, v224, v224\n"
      "v_dot2c_f32_bf16 v129, v225, %[ones]\n"
      "v_dot2c_f32_bf16 v133, v225, v225\n"
      "v_dot2c_f32_bf16 v130, v226, %[ones]\n"
      "v_dot2c_f32_bf16 v134, v226, v226\n"
      "v_dot2c_f32_bf16 v131, v227, %[ones]\n"
      "v_dot2c_f32_bf16 v135, v227, v227\n"
      "v_dot2c_f32_bf16 v128, v228, %[ones]\n"
      "v_dot2c_f32_bf16 v132, v228, v228\n"
      "v_dot2c_f32_bf16 v129, v229, %[ones]\n"
      "v_dot2c_f32_bf16 v133, v229, v229\n"
      "v_dot2c_f32_bf16 v130, v230, %[ones]\n"
      "v_dot2c_f32_bf16 v134, v230, v230\n"
      "v_dot2c_f32_bf16 v131, v231, %[ones]\n"
      "v_dot2c_f32_bf16 v135, v231, v231\n"
      "s_cmp_lg_u32 %[qhi], 7\n"
      "s_cbranch_scc1 1f\n"
      "s_waitcnt vmcnt(3)\n"
      "1:\n"
      "s_waitcnt vmcnt(4)\n"
      "s_mov_b64 exec, 1\n"
      "ds_add_u32 %[ca], %[one]\n"
      "s_mov_b64 exec, -1\n"
      "v_lshlrev_b32 v144, 16, v192\n"
      "v_and_b32 v145, s94, v192\n"
      "v_lshlrev_b32 v146, 16, v193\n"
      "v_and_b32 v147, s94, v193\n"
      "v_add_f32 v128, v128, v129\n"
      "v_add_f32 v130, v130, v131\n"
      "v_add_f32 v132, v132, v133\n"
      "v_add_f32 v134, v134, v135\n"
      "v_lshlrev_b32 v148, 16, v194\n"
      "v_and_b32 v149, s94, v194\n"
      "v_add_f32 v128, v128, v130\n"
      "v_add_f32 v132, v132, v134\n"
      "v_lshlrev_b32 v150, 16, v195\n"
      "v_and_b32 v151, s94, v195\n"
      "v_add_f32_dpp v128, v128, v128 quad_perm:[1,0,3,2] row_mask:0xf bank_mask:0xf\n"
      "v_add_f32_dpp v132, v132, v132 quad_perm:[1,0,3,2] row_mask:0xf bank_mask:0xf\n"
      "v_lshlrev_b32 v152, 16, v196\n"
      "v_and_b32 v153, s94, v196\n"
      "v_add_f32_dpp v128, v128, v128 quad_perm:[2,3,0,1] row_mask:0xf bank_mask:0xf\n"
      "v_add_f32_dpp v132, v132, v132 quad_perm:[2,3,0,1] row_mask:0xf bank_mask:0xf\n"
      "v_lshlrev_b32 v154, 16, v197\n"
      "v_and_b32 v155, s94, v197\n"
      "v_add_f32_dpp v128, v128, v128 row_half_mirror row_mask:0xf bank_mask:0xf\n"
      "v_add_f32_dpp v132, v132, v132 row_half_mirror row_mask:0xf bank_mask:0xf\n"
      "v_lshlrev_b32 v156, 16, v198\n"
      "v_and_b32 v157, s94, v198\n"
      "v_mul_f32 v128, 0x3acccccd, v128\n"
      "v_mul_f32 v132, 0x3acccccd, v132\n"
      "v_lshlrev_b32 v158, 16, v199\n"
      "v_and_b32 v159, s94, v199\n"
      "v_fma_f32 v132, -v128, v128, v132\n"
      "v_lshlrev_b32 v160, 16, v200\n"
      "v_and_b32 v161, s94, v200\n"
      "v_max_f32 v132, 0, v132\n"
      "v_lshlrev_b32 v162, 16, v201\n"
      "v_and_b32 v163, s94, v201\n"
      "v_cvt_pk_bf16_f32 v132, v128, v132\n"
      "v_lshlrev_b32 v164, 16, v202\n"
      "v_and_b32 v165, s94, v202\n"
      "v_lshlrev_b32 v128, 16, v132\n"
      "v_and_b32 v132, s94, v132\n"
      "v_lshlrev_b32 v166, 16, v203\n"
      "v_and_b32 v167, s94, v203\n"
      "v_add_f32 v132, 0x3727c5ac, v132\n"
      "v_lshlrev_b32 v168, 16, v204\n"
      "v_and_b32 v169, s94, v204\n"
      "v_cvt_pk_bf16_f32 v132, 0, v132\n"
      "v_lshlrev_b32 v170, 16, v205\n"
      "v_and_b32 v171, s94, v205\n"
      "v_sqrt_f32 v132, v132\n"
      "v_lshlrev_b32 v172, 16, v206\n"
      "v_cvt_pk_bf16_f32 v132, 0, v132\n"
      "v_and_b32 v173, s94, v206\n"
      "v_rcp_f32 v136, v132\n"
      "v_lshlrev_b32 v174, 16, v207\n"
      "v_mul_f32_e64 v138, -v128, v136\n"
      "v_and_b32 v175, s94, v207\n"
      "v_pk_fma_f32 v[144:145], v[144:145], v[136:137], v[138:139] op_sel_hi:[1,0,0]\n"
      "v_pk_fma_f32 v[146:147], v[146:147], v[136:137], v[138:139] op_sel_hi:[1,0,0]\n"
      "v_pk_fma_f32 v[148:149], v[148:149], v[136:137], v[138:139] op_sel_hi:[1,0,0]\n"
      "v_pk_fma_f32 v[150:151], v[150:151], v[136:137], v[138:139] op_sel_hi:[1,0,0]\n"
      "v_pk_fma_f32 v[152:153], v[152:153], v[136:137], v[138:139] op_sel_hi:[1,0,0]\n"
      "v_pk_fma_f32 v[154:155], v[154:155], v[136:137], v[138:139] op_sel_hi:[1,0,0]\n"
      "v_pk_fma_f32 v[156:157], v[156:157], v[136:137], v[138:139] op_sel_hi:[1,0,0]\n"
      "v_pk_fma_f32 v[158:159], v[158:159], v[136:137], v[138:139] op_sel_hi:[1,0,0]\n"
      "v_cvt_pk_bf16_f32 v192, v144, v145\n"
      "v_cvt_pk_bf16_f32 v193, v146, v147\n"
      "v_cvt_pk_bf16_f32 v194, v148, v149\n"
      "v_cvt_pk_bf16_f32 v195, v150, v151\n"
      "v_cvt_pk_bf16_f32 v196, v152, v153\n"
      "v_cvt_pk_bf16_f32 v197, v154, v155\n"
      "v_cvt_pk_bf16_f32 v198, v156, v157\n"
      "v_cvt_pk_bf16_f32 v199, v158, v159\n"
      "ds_write2_b64 %[da], v[192:193], v[194:195] offset0:0 offset1:2\n"
      "ds_write2_b64 %[da], v[196:197], v[198:199] offset0:16 offset1:18\n"
      "v_lshlrev_b32 v144, 16, v208\n"
      "v_and_b32 v145, s94, v208\n"
      "v_lshlrev_b32 v146, 16, v209\n"
      "v_and_b32 v147, s94, v209\n"
      "v_lshlrev_b32 v148, 16, v210\n"
      "v_and_b32 v149, s94, v210\n"
      "v_lshlrev_b32 v150, 16, v211\n"
      "v_and_b32 v151, s94, v211\n"
      "v_lshlrev_b32 v152, 16, v212\n"
      "v_and_b32 v153, s94, v212\n"
      "v_lshlrev_b32 v154, 16, v213\n"
      "v_and_b32 v155, s94, v213\n"
      "v_lshlrev_b32 v156, 16, v214\n"
      "v_and_b32 v157, s94, v214\n"
      "v_lshlrev_b32 v158, 16, v215\n"
      "v_and_b32 v159, s94, v215\n"
      "v_pk_fma_f32 v[160:161], v[160:161], v[136:137], v[138:139] op_sel_hi:[1,0,0]\n"
      "v_pk_fma_f32 v[162:163], v[162:163], v[136:137], v[138:139] op_sel_hi:[1,0,0]\n"
      "v_pk_fma_f32 v[164:165], v[164:165], v[136:137], v[138:139] op_sel_hi:[1,0,0]\n"
      "v_pk_fma_f32 v[166:167], v[166:167], v[136:137], v[138:139] op_sel_hi:[1,0,0]\n"
      "v_pk_fma_f32 v[168:169], v[168:169], v[136:137], v[138:139] op_sel_hi:[1,0,0]\n"
      "v_pk_fma_f32 v[170:171], v[170:171], v[136:137], v[138:139] op_sel_hi:[1,0,0]\n"
      "v_pk_fma_f32 v[172:173], v[172:173], v[136:137], v[138:139] op_sel_hi:[1,0,0]\n"
      "v_pk_fma_f32 v[174:175], v[174:175], v[136:137], v[138:139] op_sel_hi:[1,0,0]\n"
      "v_cvt_pk_bf16_f32 v200, v160, v161\n"
      "v_cvt_pk_bf16_f32 v201, v162, v163\n"
      "v_cvt_pk_bf16_f32 v202, v164, v165\n"
      "v_cvt_pk_bf16_f32 v203, v166, v167\n"
      "v_cvt_pk_bf16_f32 v204, v168, v169\n"
      "v_cvt_pk_bf16_f32 v205, v170, v171\n"
      "v_cvt_pk_bf16_f32 v206, v172, v173\n"
      "v_cvt_pk_bf16_f32 v207, v174, v175\n"
      "ds_write2_b64 %[da], v[200:201], v[202:203] offset0:32 offset1:34\n"
      "ds_write2_b64 %[da], v[204:205], v[206:207] offset0:48 offset1:50\n"
      "v_lshlrev_b32 v160, 16, v216\n"
      "v_and_b32 v161, s94, v216\n"
      "v_lshlrev_b32 v162, 16, v217\n"
      "v_and_b32 v163, s94, v217\n"
      "v_lshlrev_b32 v164, 16, v218\n"
      "v_and_b32 v165, s94, v218\n"
      "v_lshlrev_b32 v166, 16, v219\n"
      "v_and_b32 v167, s94, v219\n"
      "v_lshlrev_b32 v168, 16, v220\n"
      "v_and_b32 v169, s94, v220\n"
      "v_lshlrev_b32 v170, 16, v221\n"
      "v_and_b32 v171, s94, v221\n"
      "v_lshlrev_b32 v172, 16, v222\n"
      "v_and_b32 v173, s94, v222\n"
      "v_lshlrev_b32 v174, 16, v223\n"
      "v_and_b32 v175, s94, v223\n"
      "v_pk_fma_f32 v[144:145], v[144:145], v[136:137], v[138:139] op_sel_hi:[1,0,0]\n"
      "v_pk_fma_f32 v[146:147], v[146:147], v[136:137], v[138:139] op_sel_hi:[1,0,0]\n"
      "v_pk_fma_f32 v[148:149], v[148:149], v[136:137], v[138:139] op_sel_hi:[1,0,0]\n"
      "v_pk_fma_f32 v[150:151], v[150:151], v[136:137], v[138:139] op_sel_hi:[1,0,0]\n"
      "v_pk_fma_f32 v[152:153], v[152:153], v[136:137], v[138:139] op_sel_hi:[1,0,0]\n"
      "v_pk_fma_f32 v[154:155], v[154:155], v[136:137], v[138:139] op_sel_hi:[1,0,0]\n"
      "v_pk_fma_f32 v[156:157], v[156:157], v[136:137], v[138:139] op_sel_hi:[1,0,0]\n"
      "v_pk_fma_f32 v[158:159], v[158:159], v[136:137], v[138:139] op_sel_hi:[1,0,0]\n"
      "v_cvt_pk_bf16_f32 v208, v144, v145\n"
      "v_cvt_pk_bf16_f32 v209, v146, v147\n"
      "v_cvt_pk_bf16_f32 v210, v148, v149\n"
      "v_cvt_pk_bf16_f32 v211, v150, v151\n"
      "v_cvt_pk_bf16_f32 v212, v152, v153\n"
      "v_cvt_pk_bf16_f32 v213, v154, v155\n"
      "v_cvt_pk_bf16_f32 v214, v156, v157\n"
      "v_cvt_pk_bf16_f32 v215, v158, v159\n"
      "ds_write2_b64 %[da], v[208:209], v[210:211] offset0:64 offset1:66\n"
      "ds_write2_b64 %[da], v[212:213], v[214:215] offset0:80 offset1:82\n"
      "v_lshlrev_b32 v144, 16, v224\n"
      "v_and_b32 v145, s94, v224\n"
      "v_lshlrev_b32 v146, 16, v225\n"
      "v_and_b32 v147, s94, v225\n"
      "v_lshlrev_b32 v148, 16, v226\n"
      "v_and_b32 v149, s94, v226\n"
      "v_lshlrev_b32 v150, 16, v227\n"
      "v_and_b32 v151, s94, v227\n"
      "v_lshlrev_b32 v152, 16, v228\n"
      "v_and_b32 v153, s94, v228\n"
      "v_lshlrev_b32 v154, 16, v229\n"
      "v_and_b32 v155, s94, v229\n"
      "v_lshlrev_b32 v156, 16, v230\n"
      "v_and_b32 v157, s94, v230\n"
      "v_lshlrev_b32 v158, 16, v231\n"
      "v_and_b32 v159, s94, v231\n"
      "v_pk_fma_f32 v[160:161], v[160:161], v[136:137], v[138:139] op_sel_hi:[1,0,0]\n"
      "v_pk_fma_f32 v[162:163], v[162:163], v[136:137], v[138:139] op_sel_hi:[1,0,0]\n"
      "v_pk_fma_f32 v[164:165], v[164:165], v[136:137], v[138:139] op_sel_hi:[1,0,0]\n"
      "v_pk_fma_f32 v[166:167], v[166:167], v[136:137], v[138:139] op_sel_hi:[1,0,0]\n"
      "v_pk_fma_f32 v[168:169], v[168:169], v[136:137], v[138:139] op_sel_hi:[1,0,0]\n"
      "v_pk_fma_f32 v[170:171], v[170:171], v[136:137], v[138:139] op_sel_hi:[1,0,0]\n"
      "v_pk_fma_f32 v[172:173], v[172:173], v[136:137], v[138:139] op_sel_hi:[1,0,0]\n"
      "v_pk_fma_f32 v[174:175], v[174:175], v[136:137], v[138:139] op_sel_hi:[1,0,0]\n"
      "v_cvt_pk_bf16_f32 v216, v160, v161\n"
      "v_cvt_pk_bf16_f32 v217, v162, v163\n"
      "v_cvt_pk_bf16_f32 v218, v164, v165\n"
      "v_cvt_pk_bf16_f32 v219, v166, v167\n"
      "v_cvt_pk_bf16_f32 v220, v168, v169\n"
      "v_cvt_pk_bf16_f32 v221, v170, v171\n"
      "v_cvt_pk_bf16_f32 v222, v172, v173\n"
      "v_cvt_pk_bf16_f32 v223, v174, v175\n"
      "ds_write2_b64 %[da], v[216:217], v[218:219] offset0:96 offset1:98\n"
      "ds_write2_b64 %[da], v[220:221], v[222:223] offset0:112 offset1:114\n"
      "v_pk_fma_f32 v[144:145], v[144:145], v[136:137], v[138:139] op_sel_hi:[1,0,0]\n"
      "v_pk_fma_f32 v[146:147], v[146:147], v[136:137], v[138:139] op_sel_hi:[1,0,0]\n"
      "v_pk_fma_f32 v[148:149], v[148:149], v[136:137], v[138:139] op_sel_hi:[1,0,0]\n"
      "v_pk_fma_f32 v[150:151], v[150:151], v[136:137], v[138:139] op_sel_hi:[1,0,0]\n"
      "v_pk_fma_f32 v[152:153], v[152:153], v[136:137], v[138:139] op_sel_hi:[1,0,0]\n"
      "v_pk_fma_f32 v[154:155], v[154:155], v[136:137], v[138:139] op_sel_hi:[1,0,0]\n"
      "v_pk_fma_f32 v[156:157], v[156:157], v[136:137], v[138:139] op_sel_hi:[1,0,0]\n"
      "v_pk_fma_f32 v[158:159], v[158:159], v[136:137], v[138:139] op_sel_hi:[1,0,0]\n"
      "v_cvt_pk_bf16_f32 v224, v144, v145\n"
      "v_cvt_pk_bf16_f32 v225, v146, v147\n"
      "v_cvt_pk_bf16_f32 v226, v148, v149\n"
      "v_cvt_pk_bf16_f32 v227, v150, v151\n"
      "v_cvt_pk_bf16_f32 v228, v152, v153\n"
      "v_cvt_pk_bf16_f32 v229, v154, v155\n"
      "v_cvt_pk_bf16_f32 v230, v156, v157\n"
      "v_cvt_pk_bf16_f32 v231, v158, v159\n"
      "ds_write2_b64 %[da], v[224:225], v[226:227] offset0:128 offset1:130\n"
      "ds_write2_b64 %[da], v[228:229], v[230:231] offset0:144 offset1:146\n"
      "s_add_u32 %[sw], %[sw], 98304\n"
      "s_add_u32 %[sq], %[sq], 40960\n"
      "s_add_u32 %[sw], %[sw], 49152\n"
      "s_add_u32 %[sq], %[sq], 20480\n"
      "s_cmp_lg_u32 %[sq], 819200\n"
      "s_cselect_b32 %[sw], %[sw], 0\n"
      "s_cselect_b32 %[sq], %[sq], 0\n"
      "s_mov_b32 s91, 0x100000\n"
      "s_branch 103f\n"
      "104:\n"
      "s_sleep 1\n"
      "103:\n"
      "ds_read_b32 v129, %[ca] offset:8\n"
      "s_sub_u32 s91, s91, 1\n"
      "s_cmp_eq_u32 s91, 0\n"
      "s_cbranch_scc1 105f\n"
      "s_waitcnt lgkmcnt(0)\n"
      "v_readfirstlane_b32 s90, v129\n"
      "s_cmp_lt_u32 s90, 4\n"
      "s_cbranch_scc1 104b\n"
      "105:\n"
      "s_add_u32 m0, %[dw], 59392\n"
      "s_nop 0\n"
      "buffer_load_dwordx4 %[o0], s[40:43], %[sw] offen lds\n"
      "buffer_load_dwordx4 %[o1], s[40:43], %[sw] offen offset:1024 lds\n"
      "buffer_load_dwordx4 %[o2], s[40:43], %[sw] offen offset:2048 lds\n"
      "s_cmp_eq_u32 %[qhi], 7\n"
      "s_cbranch_scc1 106f\n"
      "s_add_u32 m0, %[dq], 59392\n"
      "s_mov_b32 exec_hi, %[qhi]\n"
      "buffer_load_dwordx4 %[oq], s[44:47], %[sq] offen lds\n"
      "s_mov_b32 exec_hi, -1\n"
      "106:\n"
      "s_add_u32 %[sw], %[sw], 49152\n"
      "s_add_u32 %[sq], %[sq], 20480\n"
      "s_cmp_lg_u32 %[sq], 819200\n"
      "s_cselect_b32 %[sw], %[sw], 0\n"
      "s_cselect_b32 %[sq], %[sq], 0\n"
      "s_waitcnt vmcnt(4)\n"
      "s_cmp_lg_u32 %[qhi], 7\n"
      "s_cbranch_scc1 2f\n"
      "s_waitcnt vmcnt(3)\n"
      "2:\n"
      "s_waitcnt lgkmcnt(0)\n"
          : [gv] "=&v"(gv), [sw] "+s"(sw), [sq] "+s"(sq)
          : [da] "v"(XN_DST), [ca] "v"(ldsb + L_MISC + C_LNA * 4), [one] "v"(1), [ones] "v"(0x3f803f80u), [xo] "v"(xo), [x] "s"(x), [ep] "s"(ws + WS_G),
            [vo] "v"(vo), [vo4] "v"(vo + 4096), [wsrc] "s"((kind ? wv : wk) + wl * 6400), [ard] "s"(ldsb + L_AR + wl * AR_SZ), DMA_OPS
          : "memory", "scc", "s90", "s91", "s94", "v128", "v129", "v130", "v131", "v132", "v133", "v134", "v135", "v136", "v137", "v138", "v139", "v140", "v141", "v142", "v143", "v144", "v145", "v146", "v147", "v148", "v149", "v150", "v151", "v152", "v153", "v154", "v155", "v156", "v157", "v158", "v159", "v160", "v161", "v162", "v163", "v164", "v165", "v166", "v167", "v168", "v169", "v170", "v171", "v172", "v173", "v174", "v175", "v192", "v193", "v194", "v195", "v196", "v197", "v198", "v199", "v200", "v201", "v202", "v203", "v204", "v205", "v206", "v207", "v208", "v209", "v210", "v211", "v212", "v213", "v214", "v215", "v216", "v217", "v218", "v219", "v220", "v221", "v222", "v223", "v224", "v225", "v226", "v227", "v228", "v229", "v230", "v231");
    }
    GA[14] = make_tag(gv);
    u32x32 C0, C1, C2, C3;
    {
      u32 kx = k0 * 32, cnt;
      asm volatile("s_barrier\n"
      "s_mov_b32 %[cnt], 1\n"
      "s_setprio 2\n"
      "ds_read_b64_tr_b16 v[176:177], v38 offset:0\n"
      "ds_read_b64_tr_b16 v[188:189], v41 offset:0\n"
      "ds_read_b64_tr_b16 v[180:181], v39 offset:0\n"
      "ds_read_b64_tr_b16 v[192:193], v42 offset:0\n"
      "ds_read_b64_tr_b16 v[184:185], v40 offset:0\n"
      "ds_read_b64_tr_b16 v[196:197], v43 offset:0\n"
      "ds_read_b64_tr_b16 v[200:201], v44 offset:0\n"
      "ds_read_b64_tr_b16 v[204:205], v44 offset:32\n"
      "ds_read_b128 v[240:243], v46 offset:0\n"
      "ds_read_b128 v[244:247], v46 offset:20992\n"
      "s_waitcnt lgkmcnt(0)\n"
      "s_barrier\n"
      "ds_read_b64_tr_b16 v[178:179], v38 offset:14848\n"
      "ds_read_b64_tr_b16 v[190:191], v41 offset:14848\n"
      "ds_read_b64_tr_b16 v[182:183], v39 offset:14848\n"
      "ds_read_b64_tr_b16 v[194:195], v42 offset:14848\n"
      "ds_read_b64_tr_b16 v[186:187], v40 offset:14848\n"
      "ds_read_b64_tr_b16 v[198:199], v43 offset:14848\n"
      "ds_read_b64_tr_b16 v[202:203], v44 offset:14848\n"
      "ds_read_b64_tr_b16 v[206:207], v44 offset:14880\n"
      "s_waitcnt lgkmcnt(0)\n"
      "v_mfma_f32_16x16x32_bf16 v[48:51], v[176:179], v[240:243], 0\n"
      "v_mfma_f32_16x16x32_bf16 v[52:55], v[176:179], v[244:247], 0\n"
      "ds_read_b128 v[248:251], v46 offset:41984\n"
      "v_mfma_f32_16x16x32_bf16 v[64:67], v[180:183], v[240:243], 0\n"
      "ds_read_b128 v[252:255], v46 offset:62976\n"
      "s_barrier\n"
      "v_mfma_f32_16x16x32_bf16 v[68:71], v[180:183], v[244:247], 0\n"
      "ds_read_b64_tr_b16 v[208:209], v38 offset:29696\n"
      "v_mfma_f32_16x16x32_bf16 v[80:83], v[184:187], v[240:243], 0\n"
      "ds_read_b64_tr_b16 v[220:221], v41 offset:29696\n"
      "v_mfma_f32_16x16x32_bf16 v[84:87], v[184:187], v[244:247], 0\n"
      "ds_read_b64_tr_b16 v[212:213], v39 offset:29696\n"
      "v_mfma_f32_16x16x32_bf16 v[96:99], v[188:191], v[240:243], 0\n"
      "ds_read_b64_tr_b16 v[224:225], v42 offset:29696\n"
      "v_mfma_f32_16x16x32_bf16 v[100:103], v[188:191], v[244:247], 0\n"
      "ds_read_b64_tr_b16 v[216:217], v40 offset:29696\n"
      "v_mfma_f32_16x16x32_bf16 v[112:115], v[192:195], v[240:243], 0\n"
      "ds_read_b64_tr_b16 v[228:229], v43 offset:29696\n"
      "v_mfma_f32_16x16x32_bf16 v[116:119], v[192:195], v[244:247], 0\n"
      "ds_read_b64_tr_b16 v[232:233], v44 offset:29696\n"
      "v_mfma_f32_16x16x32_bf16 v[128:131], v[196:199], v[240:243], 0\n"
      "ds_read_b64_tr_b16 v[236:237], v44 offset:29728\n"
      "v_mfma_f32_16x16x32_bf16 v[132:135], v[196:199], v[244:247], 0\n"
      "v_mfma_f32_16x16x32_bf16 v[144:147], v[200:203], v[240:243], 0\n"
      "v_mfma_f32_16x16x32_bf16 v[148:151], v[200:203], v[244:247], 0\n"
      "v_mfma_f32_16x16x32_bf16 v[160:163], v[204:207], v[240:243], 0\n"
      "v_mfma_f32_16x16x32_bf16 v[164:167], v[204:207], v[244:247], 0\n"
      "s_waitcnt vmcnt(0) lgkmcnt(0)\n"
      "v_mfma_f32_16x16x32_bf16 v[56:59], v[176:179], v[248:251], 0\n"
      "s_add_u32 %[kx], %[kx], 64\n"
      "s_cmp_lg_u32 %[kx], 1280\n"
      "s_cselect_b32 %[kx], %[kx], 0\n"
      "v_add_u32 v46, %[kx], v37\n"
      "v_mfma_f32_16x16x32_bf16 v[60:63], v[176:179], v[252:255], 0\n"
      "ds_read_b128 v[240:243], v46 offset:0\n"
      "v_mfma_f32_16x16x32_bf16 v[72:75], v[180:183], v[248:251], 0\n"
      "ds_read_b128 v[244:247], v46 offset:20992\n"
      "s_barrier\n"
      "v_mfma_f32_16x16x32_bf16 v[76:79], v[180:183], v[252:255], 0\n"
      "ds_read_b64_tr_b16 v[210:211], v38 offset:44544\n"
      "v_mfma_f32_16x16x32_bf16 v[88:91], v[184:187], v[248:251], 0\n"
      "ds_read_b64_tr_b16 v[222:223], v41 offset:44544\n"
      "v_mfma_f32_16x16x32_bf16 v[92:95], v[184:187], v[252:255], 0\n"
      "ds_read_b64_tr_b16 v[214:215], v39 offset:44544\n"
      "v_mfma_f32_16x16x32_bf16 v[104:107], v[188:191], v[248:251], 0\n"
      "ds_read_b64_tr_b16 v[226:227], v42 offset:44544\n"
      "v_mfma_f32_16x16x32_bf16 v[108:111], v[188:191], v[252:255], 0\n"
      "ds_read_b64_tr_b16 v[218:219], v40 offset:44544\n"
      "v_mfma_f32_16x16x32_bf16 v[120:123], v[192:195], v[248:251], 0\n"
      "ds_read_b64_tr_b16 v[230:231], v43 offset:44544\n"
      "v_mfma_f32_16x16x32_bf16 v[124:127], v[192:195], v[252:255], 0\n"
      "ds_read_b64_tr_b16 v[234:235], v44 offset:44544\n"
      "v_mfma_f32_16x16x32_bf16 v[136:139], v[196:199], v[248:251], 0\n"
      "ds_read_b64_tr_b16 v[238:239], v44 offset:44576\n"
      "v_mfma_f32_16x16x32_bf16 v[140:143], v[196:199], v[252:255], 0\n"
      "v_mfma_f32_16x16x32_bf16 v[152:155], v[200:203], v[248:251], 0\n"
      "v_mfma_f32_16x16x32_bf16 v[156:159], v[200:203], v[252:255], 0\n"
      "v_mfma_f32_16x16x32_bf16 v[168:171], v[204:207], v[248:251], 0\n"
      "v_mfma_f32_16x16x32_bf16 v[172:175], v[204:207], v[252:255], 0\n"
      "1:\n"
      "s_waitcnt lgkmcnt(0)\n"
      "v_mfma_f32_16x16x32_bf16 v[48:51], v[208:211], v[240:243], v[48:51]\n"
      "v_mfma_f32_16x16x32_bf16 v[52:55], v[208:211], v[244:247], v[52:55]\n"
      "ds_read_b128 v[248:251], v46 offset:41984\n"
      "v_mfma_f32_16x16x32_bf16 v[64:67], v[212:215], v[240:243], v[64:67]\n"
      "ds_read_b128 v[252:255], v46 offset:62976\n"
      "s_barrier\n"
      "v_mfma_f32_16x16x32_bf16 v[68:71], v[212:215], v[244:247], v[68:71]\n"
      "ds_read_b64_tr_b16 v[176:177], v38 offset:59392\n"
      "v_mfma_f32_16x16x32_bf16 v[80:83], v[216:219], v[240:243], v[80:83]\n"
      "ds_read_b64_tr_b16 v[188:189], v41 offset:59392\n"
      "v_mfma_f32_16x16x32_bf16 v[84:87], v[216:219], v[244:247], v[84:87]\n"
      "ds_read_b64_tr_b16 v[180:181], v39 offset:59392\n"
      "v_mfma_f32_16x16x32_bf16 v[96:99], v[220:223], v[240:243], v[96:99]\n"
      "ds_read_b64_tr_b16 v[192:193], v42 offset:59392\n"
      "v_mfma_f32_16x16x32_bf16 v[100:103], v[220:223], v[244:247], v[100:103]\n"
      "ds_read_b64_tr_b16 v[184:185], v40 offset:59392\n"
      "v_mfma_f32_16x16x32_bf16 v[112:115], v[224:227], v[240:243], v[112:115]\n"
      "ds_read_b64_tr_b16 v[196:197], v43 offset:59392\n"
      "v_mfma_f32_16x16x32_bf16 v[116:119], v[224:227], v[244:247], v[116:119]\n"
      "ds_read_b64_tr_b16 v[200:201], v44 offset:59392\n"
      "v_mfma_f32_16x16x32_bf16 v[128:131], v[228:231], v[240:243], v[128:131]\n"
      "ds_read_b64_tr_b16 v[204:205], v44 offset:59424\n"
      "v_mfma_f32_16x16x32_bf16 v[132:135], v[228:231], v[244:247], v[132:135]\n"
      "v_mfma_f32_16x16x32_bf16 v[144:147], v[232:235], v[240:243], v[144:147]\n"
      "v_mfma_f32_16x16x32_bf16 v[148:151], v[232:235], v[244:247], v[148:151]\n"
      "v_mfma_f32_16x16x32_bf16 v[160:163], v[236:239], v[240:243], v[160:163]\n"
      "v_mfma_f32_16x16x32_bf16 v[164:167], v[236:239], v[244:247], v[164:167]\n"
      "s_waitcnt lgkmcnt(0)\n"
      "v_mfma_f32_16x16x32_bf16 v[56:59], v[208:211], v[248:251], v[56:59]\n"
      "s_add_u32 %[kx], %[kx], 64\n"
      "s_cmp_lg_u32 %[kx], 1280\n"
      "s_cselect_b32 %[kx], %[kx], 0\n"
      "v_add_u32 v46, %[kx], v37\n"
      "v_mfma_f32_16x16x32_bf16 v[60:63], v[208:211], v[252:255], v[60:63]\n"
      "ds_read_b128 v[240:243], v46 offset:0\n"
      "v_mfma_f32_16x16x32_bf16 v[72:75], v[212:215], v[248:251], v[72:75]\n"
      "ds_read_b128 v[244:247], v46 offset:20992\n"
      "s_barrier\n"
      "v_mfma_f32_16x16x32_bf16 v[76:79], v[212:215], v[252:255], v[76:79]\n"
      "ds_read_b64_tr_b16 v[178:179], v38 offset:0\n"
      "v_mfma_f32_16x16x32_bf16 v[88:91], v[216:219], v[248:251], v[88:91]\n"
      "ds_read_b64_tr_b16 v[190:191], v41 offset:0\n"
      "v_mfma_f32_16x16x32_bf16 v[92:95], v[216:219], v[252:255], v[92:95]\n"
      "ds_read_b64_tr_b16 v[182:183], v39 offset:0\n"
      "v_mfma_f32_16x16x32_bf16 v[104:107], v[220:223], v[248:251], v[104:107]\n"
      "ds_read_b64_tr_b16 v[194:195], v42 offset:0\n"
      "v_mfma_f32_16x16x32_bf16 v[108:111], v[220:223], v[252:255], v[108:111]\n"
      "ds_read_b64_tr_b16 v[186:187], v40 offset:0\n"
      "v_mfma_f32_16x16x32_bf16 v[120:123], v[224:227], v[248:251], v[120:123]\n"
      "ds_read_b64_tr_b16 v[198:199], v43 offset:0\n"
      "v_mfma_f32_16x16x32_bf16 v[124:127], v[224:227], v[252:255], v[124:127]\n"
      "ds_read_b64_tr_b16 v[202:203], v44 offset:0\n"
      "v_mfma_f32_16x16x32_bf16 v[136:139], v[228:231], v[248:251], v[136:139]\n"
      "ds_read_b64_tr_b16 v[206:207], v44 offset:32\n"
      "v_mfma_f32_16x16x32_bf16 v[140:143], v[228:231], v[252:255], v[140:143]\n"
      "v_mfma_f32_16x16x32_bf16 v[152:155], v[232:235], v[248:251], v[152:155]\n"
      "v_mfma_f32_16x16x32_bf16 v[156:159], v[232:235], v[252:255], v[156:159]\n"
      "v_mfma_f32_16x16x32_bf16 v[168:171], v[236:239], v[248:251], v[168:171]\n"
      "v_mfma_f32_16x16x32_bf16 v[172:175], v[236:239], v[252:255], v[172:175]\n"
      "s_waitcnt lgkmcnt(0)\n"
      "v_mfma_f32_16x16x32_bf16 v[48:51], v[176:179], v[240:243], v[48:51]\n"
      "v_mfma_f32_16x16x32_bf16 v[52:55], v[176:179], v[244:247], v[52:55]\n"
      "ds_read_b128 v[248:251], v46 offset:41984\n"
      "v_mfma_f32_16x16x32_bf16 v[64:67], v[180:183], v[240:243], v[64:67]\n"
      "ds_read_b128 v[252:255], v46 offset:62976\n"
      "s_barrier\n"
      "v_mfma_f32_16x16x32_bf16 v[68:71], v[180:183], v[244:247], v[68:71]\n"
      "ds_read_b64_tr_b16 v[208:209], v38 offset:14848\n"
      "v_mfma_f32_16x16x32_bf16 v[80:83], v[184:187], v[240:243], v[80:83]\n"
      "ds_read_b64_tr_b16 v[220:221], v41 offset:14848\n"
      "v_mfma_f32_16x16x32_bf16 v[84:87], v[184:187], v[244:247], v[84:87]\n"
      "ds_read_b64_tr_b16 v[212:213], v39 offset:14848\n"
      "v_mfma_f32_16x16x32_bf16 v[96:99], v[188:191], v[240:243], v[96:99]\n"
      "ds_read_b64_tr_b16 v[224:225], v42 offset:14848\n"
      "v_mfma_f32_16x16x32_bf16 v[100:103], v[188:191], v[244:247], v[100:103]\n"
      "ds_read_b64_tr_b16 v[216:217], v40 offset:14848\n"
      "v_mfma_f32_16x16x32_bf16 v[112:115], v[192:195], v[240:243], v[112:115]\n"
      "ds_read_b64_tr_b16 v[228:229], v43 offset:14848\n"
      "v_mfma_f32_16x16x32_bf16 v[116:119], v[192:195], v[244:247], v[116:119]\n"
      "ds_read_b64_tr_b16 v[232:233], v44 offset:14848\n"
      "v_mfma_f32_16x16x32_bf16 v[128:131], v[196:199], v[240:243], v[128:131]\n"
      "ds_read_b64_tr_b16 v[236:237], v44 offset:14880\n"
      "v_mfma_f32_16x16x32_bf16 v[132:135], v[196:199], v[244:247], v[132:135]\n"
      "v_mfma_f32_16x16x32_bf16 v[144:147], v[200:203], v[240:243], v[144:147]\n"
      "v_mfma_f32_16x16x32_bf16 v[148:151], v[200:203], v[244:247], v[148:151]\n"
      "v_mfma_f32_16x16x32_bf16 v[160:163], v[204:207], v[240:243], v[160:163]\n"
      "v_mfma_f32_16x16x32_bf16 v[164:167], v[204:207], v[244:247], v[164:167]\n"
      "s_waitcnt lgkmcnt(0)\n"
      "v_mfma_f32_16x16x32_bf16 v[56:59], v[176:179], v[248:251], v[56:59]\n"
      "s_add_u32 %[kx], %[kx], 64\n"
      "s_cmp_lg_u32 %[kx], 1280\n"
      "s_cselect_b32 %[kx], %[kx], 0\n"
      "v_add_u32 v46, %[kx], v37\n"
      "v_mfma_f32_16x16x32_bf16 v[60:63], v[176:179], v[252:255], v[60:63]\n"
      "ds_read_b128 v[240:243], v46 offset:0\n"
      "v_mfma_f32_16x16x32_bf16 v[72:75], v[180:183], v[248:251], v[72:75]\n"
      "ds_read_b128 v[244:247], v46 offset:20992\n"
      "s_barrier\n"
      "v_mfma_f32_16x16x32_bf16 v[76:79], v[180:183], v[252:255], v[76:79]\n"
      "ds_read_b64_tr_b16 v[210:211], v38 offset:29696\n"
      "v_mfma_f32_16x16x32_bf16 v[88:91], v[184:187], v[248:251], v[88:91]\n"
      "ds_read_b64_tr_b16 v[222:223], v41 offset:29696\n"
      "v_mfma_f32_16x16x32_bf16 v[92:95], v[184:187], v[252:255], v[92:95]\n"
      "ds_read_b64_tr_b16 v[214:215], v39 offset:29696\n"
      "v_mfma_f32_16x16x32_bf16 v[104:107], v[188:191], v[248:251], v[104:107]\n"
      "ds_read_b64_tr_b16 v[226:227], v42 offset:29696\n"
      "v_mfma_f32_16x16x32_bf16 v[108:111], v[188:191], v[252:255], v[108:111]\n"
      "ds_read_b64_tr_b16 v[218:219], v40 offset:29696\n"
      "v_mfma_f32_16x16x32_bf16 v[120:123], v[192:195], v[248:251], v[120:123]\n"
      "ds_read_b64_tr_b16 v[230:231], v43 offset:29696\n"
      "v_mfma_f32_16x16x32_bf16 v[124:127], v[192:195], v[252:255], v[124:127]\n"
      "ds_read_b64_tr_b16 v[234:235], v44 offset:29696\n"
      "v_mfma_f32_16x16x32_bf16 v[136:139], v[196:199], v[248:251], v[136:139]\n"
      "ds_read_b64_tr_b16 v[238:239], v44 offset:29728\n"
      "v_mfma_f32_16x16x32_bf16 v[140:143], v[196:199], v[252:255], v[140:143]\n"
      "v_mfma_f32_16x16x32_bf16 v[152:155], v[200:203], v[248:251], v[152:155]\n"
      "v_mfma_f32_16x16x32_bf16 v[156:159], v[200:203], v[252:255], v[156:159]\n"
      "v_mfma_f32_16x16x32_bf16 v[168:171], v[204:207], v[248:251], v[168:171]\n"
      "v_mfma_f32_16x16x32_bf16 v[172:175], v[204:207], v[252:255], v[172:175]\n"
      "s_waitcnt lgkmcnt(0)\n"
      "v_mfma_f32_16x16x32_bf16 v[48:51], v[208:211], v[240:243], v[48:51]\n"
      "v_mfma_f32_16x16x32_bf16 v[52:55], v[208:211], v[244:247], v[52:55]\n"
      "ds_read_b128 v[248:251], v46 offset:41984\n"
      "v_mfma_f32_16x16x32_bf16 v[64:67], v[212:215], v[240:243], v[64:67]\n"
      "ds_read_b128 v[252:255], v46 offset:62976\n"
      "s_barrier\n"
      "v_mfma_f32_16x16x32_bf16 v[68:71], v[212:215], v[244:247], v[68:71]\n"
      "ds_read_b64_tr_b16 v[176:177], v38 offset:44544\n"
      "v_mfma_f32_16x16x32_bf16 v[80:83], v[216:219], v[240:243], v[80:83]\n"
      "ds_read_b64_tr_b16 v[188:189], v41 offset:44544\n"
      "v_mfma_f32_16x16x32_bf16 v[84:87], v[216:219], v[244:247], v[84:87]\n"
      "ds_read_b64_tr_b16 v[180:181], v39 offset:44544\n"
      "v_mfma_f32_16x16x32_bf16 v[96:99], v[220:223], v[240:243], v[96:99]\n"
      "ds_read_b64_tr_b16 v[192:193], v42 offset:44544\n"
      "v_mfma_f32_16x16x32_bf16 v[100:103], v[220:223], v[244:247], v[100:103]\n"
      "ds_read_b64_tr_b16 v[184:185], v40 offset:44544\n"
      "v_mfma_f32_16x16x32_bf16 v[112:115], v[224:227], v[240:243], v[112:115]\n"
      "ds_read_b64_tr_b16 v[196:197], v43 offset:44544\n"
      "v_mfma_f32_16x16x32_bf16 v[116:119], v[224:227], v[244:247], v[116:119]\n"
      "ds_read_b64_tr_b16 v[200:201], v44 offset:44544\n"
      "v_mfma_f32_16x16x32_bf16 v[128:131], v[228:231], v[240:243], v[128:131]\n"
      "ds_read_b64_tr_b16 v[204:205], v44 offset:44576\n"
      "v_mfma_f32_16x16x32_bf16 v[132:135], v[228:231], v[244:247], v[132:135]\n"
      "v_mfma_f32_16x16x32_bf16 v[144:147], v[232:235], v[240:243], v[144:147]\n"
      "v_mfma_f32_16x16x32_bf16 v[148:151], v[232:235], v[244:247], v[148:151]\n"
      "v_mfma_f32_16x16x32_bf16 v[160:163], v[236:239], v[240:243], v[160:163]\n"
      "v_mfma_f32_16x16x32_bf16 v[164:167], v[236:239], v[244:247], v[164:167]\n"
      "s_waitcnt lgkmcnt(0)\n"
      "v_mfma_f32_16x16x32_bf16 v[56:59], v[208:211], v[248:251], v[56:59]\n"
      "s_add_u32 %[kx], %[kx], 64\n"
      "s_cmp_lg_u32 %[kx], 1280\n"
      "s_cselect_b32 %[kx], %[kx], 0\n"
      "v_add_u32 v46, %[kx], v37\n"
      "v_mfma_f32_16x16x32_bf16 v[60:63], v[208:211], v[252:255], v[60:63]\n"
      "ds_read_b128 v[240:243], v46 offset:0\n"
      "v_mfma_f32_16x16x32_bf16 v[72:75], v[212:215], v[248:251], v[72:75]\n"
      "ds_read_b128 v[244:247], v46 offset:20992\n"
      "s_barrier\n"
      "v_mfma_f32_16x16x32_bf16 v[76:79], v[212:215], v[252:255], v[76:79]\n"
      "ds_read_b64_tr_b16 v[178:179], v38 offset:59392\n"
      "v_mfma_f32_16x16x32_bf16 v[88:91], v[216:219], v[248:251], v[88:91]\n"
      "ds_read_b64_tr_b16 v[190:191], v41 offset:59392\n"
      "v_mfma_f32_16x16x32_bf16 v[92:95], v[216:219], v[252:255], v[92:95]\n"
      "ds_read_b64_tr_b16 v[182:183], v39 offset:59392\n"
      "v_mfma_f32_16x16x32_bf16 v[104:107], v[220:223], v[248:251], v[104:107]\n"
      "ds_read_b64_tr_b16 v[194:195], v42 offset:59392\n"
      "v_mfma_f32_16x16x32_bf16 v[108:111], v[220:223], v[252:255], v[108:111]\n"
      "ds_read_b64_tr_b16 v[186:187], v40 offset:59392\n"
      "v_mfma_f32_16x16x32_bf16 v[120:123], v[224:227], v[248:251], v[120:123]\n"
      "ds_read_b64_tr_b16 v[198:199], v43 offset:59392\n"
      "v_mfma_f32_16x16x32_bf16 v[124:127], v[224:227], v[252:255], v[124:127]\n"
      "ds_read_b64_tr_b16 v[202:203], v44 offset:59392\n"
      "v_mfma_f32_16x16x32_bf16 v[136:139], v[228:231], v[248:251], v[136:139]\n"
      "ds_read_b64_tr_b16 v[206:207], v44 offset:59424\n"
      "v_mfma_f32_16x16x32_bf16 v[140:143], v[228:231], v[252:255], v[140:143]\n"
      "v_mfma_f32_16x16x32_bf16 v[152:155], v[232:235], v[248:251], v[152:155]\n"
      "v_mfma_f32_16x16x32_bf16 v[156:159], v[232:235], v[252:255], v[156:159]\n"
      "v_mfma_f32_16x16x32_bf16 v[168:171], v[236:239], v[248:251], v[168:171]\n"
      "v_mfma_f32_16x16x32_bf16 v[172:175], v[236:239], v[252:255], v[172:175]\n"
      "s_waitcnt lgkmcnt(0)\n"
      "v_mfma_f32_16x16x32_bf16 v[48:51], v[176:179], v[240:243], v[48:51]\n"
      "v_mfma_f32_16x16x32_bf16 v[52:55], v[176:179], v[244:247], v[52:55]\n"
      "ds_read_b128 v[248:251], v46 offset:41984\n"
      "v_mfma_f32_16x16x32_bf16 v[64:67], v[180:183], v[240:243], v[64:67]\n"
      "ds_read_b128 v[252:255], v46 offset:62976\n"
      "s_barrier\n"
      "v_mfma_f32_16x16x32_bf16 v[68:71], v[180:183], v[244:247], v[68:71]\n"
      "ds_read_b64_tr_b16 v[208:209], v38 offset:0\n"
      "v_mfma_f32_16x16x32_bf16 v[80:83], v[184:187], v[240:243], v[80:83]\n"
      "ds_read_b64_tr_b16 v[220:221], v41 offset:0\n"
      "v_mfma_f32_16x16x32_bf16 v[84:87], v[184:187], v[244:247], v[84:87]\n"
      "ds_read_b64_tr_b16 v[212:213], v39 offset:0\n"
      "v_mfma_f32_16x16x32_bf16 v[96:99], v[188:191], v[240:243], v[96:99]\n"
      "ds_read_b64_tr_b16 v[224:225], v42 offset:0\n"
      "v_mfma_f32_16x16x32_bf16 v[100:103], v[188:191], v[244:247], v[100:103]\n"
      "ds_read_b64_tr_b16 v[216:217], v40 offset:0\n"
      "v_mfma_f32_16x16x32_bf16 v[112:115], v[192:195], v[240:243], v[112:115]\n"
      "ds_read_b64_tr_b16 v[228:229], v43 offset:0\n"
      "v_mfma_f32_16x16x32_bf16 v[116:119], v[192:195], v[244:247], v[116:119]\n"
      "ds_read_b64_tr_b16 v[232:233], v44 offset:0\n"
      "v_mfma_f32_16x16x32_bf16 v[128:131], v[196:199], v[240:243], v[128:131]\n"
      "ds_read_b64_tr_b16 v[236:237], v44 offset:32\n"
      "v_mfma_f32_16x16x32_bf16 v[132:135], v[196:199], v[244:247], v[132:135]\n"
      "v_mfma_f32_16x16x32_bf16 v[144:147], v[200:203], v[240:243], v[144:147]\n"
      "v_mfma_f32_16x16x32_bf16 v[148:151], v[200:203], v[244:247], v[148:151]\n"
      "v_mfma_f32_16x16x32_bf16 v[160:163], v[204:207], v[240:243], v[160:163]\n"
      "v_mfma_f32_16x16x32_bf16 v[164:167], v[204:207], v[244:247], v[164:167]\n"
      "s_waitcnt lgkmcnt(0)\n"
      "v_mfma_f32_16x16x32_bf16 v[56:59], v[176:179], v[248:251], v[56:59]\n"
      "s_add_u32 %[kx], %[kx], 64\n"
      "s_cmp_lg_u32 %[kx], 1280\n"
      "s_cselect_b32 %[kx], %[kx], 0\n"
      "v_add_u32 v46, %[kx], v37\n"
      "v_mfma_f32_16x16x32_bf16 v[60:63], v[176:179], v[252:255], v[60:63]\n"
      "ds_read_b128 v[240:243], v46 offset:0\n"
      "v_mfma_f32_16x16x32_bf16 v[72:75], v[180:183], v[248:251], v[72:75]\n"
      "ds_read_b128 v[244:247], v46 offset:20992\n"
      "s_barrier\n"
      "v_mfma_f32_16x16x32_bf16 v[76:79], v[180:183], v[252:255], v[76:79]\n"
      "ds_read_b64_tr_b16 v[210:211], v38 offset:14848\n"
      "v_mfma_f32_16x16x32_bf16 v[88:91], v[184:187], v[248:251], v[88:91]\n"
      "ds_read_b64_tr_b16 v[222:223], v41 offset:14848\n"
      "v_mfma_f32_16x16x32_bf16 v[92:95], v[184:187], v[252:255], v[92:95]\n"
      "ds_read_b64_tr_b16 v[214:215], v39 offset:14848\n"
      "v_mfma_f32_16x16x32_bf16 v[104:107], v[188:191], v[248:251], v[104:107]\n"
      "ds_read_b64_tr_b16 v[226:227], v42 offset:14848\n"
      "v_mfma_f32_16x16x32_bf16 v[108:111], v[188:191], v[252:255], v[108:111]\n"
      "ds_read_b64_tr_b16 v[218:219], v40 offset:14848\n"
      "v_mfma_f32_16x16x32_bf16 v[120:123], v[192:195], v[248:251], v[120:123]\n"
      "ds_read_b64_tr_b16 v[230:231], v43 offset:14848\n"
      "v_mfma_f32_16x16x32_bf16 v[124:127], v[192:195], v[252:255], v[124:127]\n"
      "ds_read_b64_tr_b16 v[234:235], v44 offset:14848\n"
      "v_mfma_f32_16x16x32_bf16 v[136:139], v[196:199], v[248:251], v[136:139]\n"
      "ds_read_b64_tr_b16 v[238:239], v44 offset:14880\n"
      "v_mfma_f32_16x16x32_bf16 v[140:143], v[196:199], v[252:255], v[140:143]\n"
      "v_mfma_f32_16x16x32_bf16 v[152:155], v[200:203], v[248:251], v[152:155]\n"
      "v_mfma_f32_16x16x32_bf16 v[156:159], v[200:203], v[252:255], v[156:159]\n"
      "v_mfma_f32_16x16x32_bf16 v[168:171], v[204:207], v[248:251], v[168:171]\n"
      "v_mfma_f32_16x16x32_bf16 v[172:175], v[204:207], v[252:255], v[172:175]\n"
      "s_waitcnt lgkmcnt(0)\n"
      "v_mfma_f32_16x16x32_bf16 v[48:51], v[208:211], v[240:243], v[48:51]\n"
      "v_mfma_f32_16x16x32_bf16 v[52:55], v[208:211], v[244:247], v[52:55]\n"
      "ds_read_b128 v[248:251], v46 offset:41984\n"
      "v_mfma_f32_16x16x32_bf16 v[64:67], v[212:215], v[240:243], v[64:67]\n"
      "ds_read_b128 v[252:255], v46 offset:62976\n"
      "s_barrier\n"
      "v_mfma_f32_16x16x32_bf16 v[68:71], v[212:215], v[244:247], v[68:71]\n"
      "ds_read_b64_tr_b16 v[176:177], v38 offset:29696\n"
      "v_mfma_f32_16x16x32_bf16 v[80:83], v[216:219], v[240:243], v[80:83]\n"
      "ds_read_b64_tr_b16 v[188:189], v41 offset:29696\n"
      "v_mfma_f32_16x16x32_bf16 v[84:87], v[216:219], v[244:247], v[84:87]\n"
      "ds_read_b64_tr_b16 v[180:181], v39 offset:29696\n"
      "v_mfma_f32_16x16x32_bf16 v[96:99], v[220:223], v[240:243], v[96:99]\n"
      "ds_read_b64_tr_b16 v[192:193], v42 offset:29696\n"
      "v_mfma_f32_16x16x32_bf16 v[100:103], v[220:223], v[244:247], v[100:103]\n"
      "ds_read_b64_tr_b16 v[184:185], v40 offset:29696\n"
      "v_mfma_f32_16x16x32_bf16 v[112:115], v[224:227], v[240:243], v[112:115]\n"
      "ds_read_b64_tr_b16 v[196:197], v43 offset:29696\n"
      "v_mfma_f32_16x16x32_bf16 v[116:119], v[224:227], v[244:247], v[116:119]\n"
      "ds_read_b64_tr_b16 v[200:201], v44 offset:29696\n"
      "v_mfma_f32_16x16x32_bf16 v[128:131], v[228:231], v[240:243], v[128:131]\n"
      "ds_read_b64_tr_b16 v[204:205], v44 offset:29728\n"
      "v_mfma_f32_16x16x32_bf16 v[132:135], v[228:231], v[244:247], v[132:135]\n"
      "v_mfma_f32_16x16x32_bf16 v[144:147], v[232:235], v[240:243], v[144:147]\n"
      "v_mfma_f32_16x16x32_bf16 v[148:151], v[232:235], v[244:247], v[148:151]\n"
      "v_mfma_f32_16x16x32_bf16 v[160:163], v[236:239], v[240:243], v[160:163]\n"
      "v_mfma_f32_16x16x32_bf16 v[164:167], v[236:239], v[244:247], v[164:167]\n"
      "s_waitcnt lgkmcnt(0)\n"
      "v_mfma_f32_16x16x32_bf16 v[56:59], v[208:211], v[248:251], v[56:59]\n"
      "s_add_u32 %[kx], %[kx], 64\n"
      "s_cmp_lg_u32 %[kx], 1280\n"
      "s_cselect_b32 %[kx], %[kx], 0\n"
      "v_add_u32 v46, %[kx], v37\n"
      "v_mfma_f32_16x16x32_bf16 v[60:63], v[208:211], v[252:255], v[60:63]\n"
      "ds_read_b128 v[240:243], v46 offset:0\n"
      "v_mfma_f32_16x16x32_bf16 v[72:75], v[212:215], v[248:251], v[72:75]\n"
      "ds_read_b128 v[244:247], v46 offset:20992\n"
      "s_barrier\n"
      "v_mfma_f32_16x16x32_bf16 v[76:79], v[212:215], v[252:255], v[76:79]\n"
      "ds_read_b64_tr_b16 v[178:179], v38 offset:44544\n"
      "v_mfma_f32_16x16x32_bf16 v[88:91], v[216:219], v[248:251], v[88:91]\n"
      "ds_read_b64_tr_b16 v[190:191], v41 offset:44544\n"
      "v_mfma_f32_16x16x32_bf16 v[92:95], v[216:219], v[252:255], v[92:95]\n"
      "ds_read_b64_tr_b16 v[182:183], v39 offset:44544\n"
      "v_mfma_f32_16x16x32_bf16 v[104:107], v[220:223], v[248:251], v[104:107]\n"
      "ds_read_b64_tr_b16 v[194:195], v42 offset:44544\n"
      "v_mfma_f32_16x16x32_bf16 v[108:111], v[220:223], v[252:255], v[108:111]\n"
      "ds_read_b64_tr_b16 v[186:187], v40 offset:44544\n"
      "v_mfma_f32_16x16x32_bf16 v[120:123], v[224:227], v[248:251], v[120:123]\n"
      "ds_read_b64_tr_b16 v[198:199], v43 offset:44544\n"
      "v_mfma_f32_16x16x32_bf16 v[124:127], v[224:227], v[252:255], v[124:127]\n"
      "ds_read_b64_tr_b16 v[202:203], v44 offset:44544\n"
      "v_mfma_f32_16x16x32_bf16 v[136:139], v[228:231], v[248:251], v[136:139]\n"
      "ds_read_b64_tr_b16 v[206:207], v44 offset:44576\n"
      "v_mfma_f32_16x16x32_bf16 v[140:143], v[228:231], v[252:255], v[140:143]\n"
      "v_mfma_f32_16x16x32_bf16 v[152:155], v[232:235], v[248:251], v[152:155]\n"
      "v_mfma_f32_16x16x32_bf16 v[156:159], v[232:235], v[252:255], v[156:159]\n"
      "v_mfma_f32_16x16x32_bf16 v[168:171], v[236:239], v[248:251], v[168:171]\n"
      "v_mfma_f32_16x16x32_bf16 v[172:175], v[236:239], v[252:255], v[172:175]\n"
      "s_waitcnt lgkmcnt(0)\n"
      "v_mfma_f32_16x16x32_bf16 v[48:51], v[176:179], v[240:243], v[48:51]\n"
      "v_mfma_f32_16x16x32_bf16 v[52:55], v[176:179], v[244:247], v[52:55]\n"
      "ds_read_b128 v[248:251], v46 offset:41984\n"
      "v_mfma_f32_16x16x32_bf16 v[64:67], v[180:183], v[240:243], v[64:67]\n"
      "ds_read_b128 v[252:255], v46 offset:62976\n"
      "s_barrier\n"
      "v_mfma_f32_16x16x32_bf16 v[68:71], v[180:183], v[244:247], v[68:71]\n"
      "ds_read_b64_tr_b16 v[208:209], v38 offset:59392\n"
      "v_mfma_f32_16x16x32_bf16 v[80:83], v[184:187], v[240:243], v[80:83]\n"
      "ds_read_b64_tr_b16 v[220:221], v41 offset:59392\n"
      "v_mfma_f32_16x16x32_bf16 v[84:87], v[184:187], v[244:247], v[84:87]\n"
      "ds_read_b64_tr_b16 v[212:213], v39 offset:59392\n"
      "v_mfma_f32_16x16x32_bf16 v[96:99], v[188:191], v[240:243], v[96:99]\n"
      "ds_read_b64_tr_b16 v[224:225], v42 offset:59392\n"
      "v_mfma_f32_16x16x32_bf16 v[100:103], v[188:191], v[244:247], v[100:103]\n"
      "ds_read_b64_tr_b16 v[216:217], v40 offset:59392\n"
      "v_mfma_f32_16x16x32_bf16 v[112:115], v[192:195], v[240:243], v[112:115]\n"
      "ds_read_b64_tr_b16 v[228:229], v43 offset:59392\n"
      "v_mfma_f32_16x16x32_bf16 v[116:119], v[192:195], v[244:247], v[116:119]\n"
      "ds_read_b64_tr_b16 v[232:233], v44 offset:59392\n"
      "v_mfma_f32_16x16x32_bf16 v[128:131], v[196:199], v[240:243], v[128:131]\n"
      "ds_read_b64_tr_b16 v[236:237], v44 offset:59424\n"
      "v_mfma_f32_16x16x32_bf16 v[132:135], v[196:199], v[244:247], v[132:135]\n"
      "v_mfma_f32_16x16x32_bf16 v[144:147], v[200:203], v[240:243], v[144:147]\n"
      "v_mfma_f32_16x16x32_bf16 v[148:151], v[200:203], v[244:247], v[148:151]\n"
      "v_mfma_f32_16x16x32_bf16 v[160:163], v[204:207], v[240:243], v[160:163]\n"
      "v_mfma_f32_16x16x32_bf16 v[164:167], v[204:207], v[244:247], v[164:167]\n"
      "s_waitcnt lgkmcnt(0)\n"
      "v_mfma_f32_16x16x32_bf16 v[56:59], v[176:179], v[248:251], v[56:59]\n"
      "s_add_u32 %[kx], %[kx], 64\n"
      "s_cmp_lg_u32 %[kx], 1280\n"
      "s_cselect_b32 %[kx], %[kx], 0\n"
      "v_add_u32 v46, %[kx], v37\n"
      "v_mfma_f32_16x16x32_bf16 v[60:63], v[176:179], v[252:255], v[60:63]\n"
      "ds_read_b128 v[240:243], v46 offset:0\n"
      "v_mfma_f32_16x16x32_bf16 v[72:75], v[180:183], v[248:251], v[72:75]\n"
      "ds_read_b128 v[244:247], v46 offset:20992\n"
      "s_barrier\n"
      "v_mfma_f32_16x16x32_bf16 v[76:79], v[180:183], v[252:255], v[76:79]\n"
      "ds_read_b64_tr_b16 v[210:211], v38 offset:0\n"
      "v_mfma_f32_16x16x32_bf16 v[88:91], v[184:187], v[248:251], v[88:91]\n"
      "ds_read_b64_tr_b16 v[222:223], v41 offset:0\n"
      "v_mfma_f32_16x16x32_bf16 v[92:95], v[184:187], v[252:255], v[92:95]\n"
      "ds_read_b64_tr_b16 v[214:215], v39 offset:0\n"
      "v_mfma_f32_16x16x32_bf16 v[104:107], v[188:191], v[248:251], v[104:107]\n"
      "ds_read_b64_tr_b16 v[226:227], v42 offset:0\n"
      "v_mfma_f32_16x16x32_bf16 v[108:111], v[188:191], v[252:255], v[108:111]\n"
      "ds_read_b64_tr_b16 v[218:219], v40 offset:0\n"
      "v_mfma_f32_16x16x32_bf16 v[120:123], v[192:195], v[248:251], v[120:123]\n"
      "ds_read_b64_tr_b16 v[230:231], v43 offset:0\n"
      "v_mfma_f32_16x16x32_bf16 v[124:127], v[192:195], v[252:255], v[124:127]\n"
      "ds_read_b64_tr_b16 v[234:235], v44 offset:0\n"
      "v_mfma_f32_16x16x32_bf16 v[136:139], v[196:199], v[248:251], v[136:139]\n"
      "ds_read_b64_tr_b16 v[238:239], v44 offset:32\n"
      "v_mfma_f32_16x16x32_bf16 v[140:143], v[196:199], v[252:255], v[140:143]\n"
      "v_mfma_f32_16x16x32_bf16 v[152:155], v[200:203], v[248:251], v[152:155]\n"
      "v_mfma_f32_16x16x32_bf16 v[156:159], v[200:203], v[252:255], v[156:159]\n"
      "v_mfma_f32_16x16x32_bf16 v[168:171], v[204:207], v[248:251], v[168:171]\n"
      "v_mfma_f32_16x16x32_bf16 v[172:175], v[204:207], v[252:255], v[172:175]\n"
      "s_waitcnt lgkmcnt(0)\n"
      "v_mfma_f32_16x16x32_bf16 v[48:51], v[208:211], v[240:243], v[48:51]\n"
      "v_mfma_f32_16x16x32_bf16 v[52:55], v[208:211], v[244:247], v[52:55]\n"
      "ds_read_b128 v[248:251], v46 offset:41984\n"
      "v_mfma_f32_16x16x32_bf16 v[64:67], v[212:215], v[240:243], v[64:67]\n"
      "ds_read_b128 v[252:255], v46 offset:62976\n"
      "s_barrier\n"
      "v_mfma_f32_16x16x32_bf16 v[68:71], v[212:215], v[244:247], v[68:71]\n"
      "ds_read_b64_tr_b16 v[176:177], v38 offset:14848\n"
      "v_mfma_f32_16x16x32_bf16 v[80:83], v[216:219], v[240:243], v[80:83]\n"
      "ds_read_b64_tr_b16 v[188:189], v41 offset:14848\n"
      "v_mfma_f32_16x16x32_bf16 v[84:87], v[216:219], v[244:247], v[84:87]\n"
      "ds_read_b64_tr_b16 v[180:181], v39 offset:14848\n"
      "v_mfma_f32_16x16x32_bf16 v[96:99], v[220:223], v[240:243], v[96:99]\n"
      "ds_read_b64_tr_b16 v[192:193], v42 offset:14848\n"
      "v_mfma_f32_16x16x32_bf16 v[100:103], v[220:223], v[244:247], v[100:103]\n"
      "ds_read_b64_tr_b16 v[184:185], v40 offset:14848\n"
      "v_mfma_f32_16x16x32_bf16 v[112:115], v[224:227], v[240:243], v[112:115]\n"
      "ds_read_b64_tr_b16 v[196:197], v43 offset:14848\n"
      "v_mfma_f32_16x16x32_bf16 v[116:119], v[224:227], v[244:247], v[116:119]\n"
      "ds_read_b64_tr_b16 v[200:201], v44 offset:14848\n"
      "v_mfma_f32_16x16x32_bf16 v[128:131], v[228:231], v[240:243], v[128:131]\n"
      "ds_read_b64_tr_b16 v[204:205], v44 offset:14880\n"
      "v_mfma_f32_16x16x32_bf16 v[132:135], v[228:231], v[244:247], v[132:135]\n"
      "v_mfma_f32_16x16x32_bf16 v[144:147], v[232:235], v[240:243], v[144:147]\n"
      "v_mfma_f32_16x16x32_bf16 v[148:151], v[232:235], v[244:247], v[148:151]\n"
      "v_mfma_f32_16x16x32_bf16 v[160:163], v[236:239], v[240:243], v[160:163]\n"
      "v_mfma_f32_16x16x32_bf16 v[164:167], v[236:239], v[244:247], v[164:167]\n"
      "s_waitcnt lgkmcnt(0)\n"
      "v_mfma_f32_16x16x32_bf16 v[56:59], v[208:211], v[248:251], v[56:59]\n"
      "s_add_u32 %[kx], %[kx], 64\n"
      "s_cmp_lg_u32 %[kx], 1280\n"
      "s_cselect_b32 %[kx], %[kx], 0\n"
      "v_add_u32 v46, %[kx], v37\n"
      "v_mfma_f32_16x16x32_bf16 v[60:63], v[208:211], v[252:255], v[60:63]\n"
      "ds_read_b128 v[240:243], v46 offset:0\n"
      "v_mfma_f32_16x16x32_bf16 v[72:75], v[212:215], v[248:251], v[72:75]\n"
      "ds_read_b128 v[244:247], v46 offset:20992\n"
      "s_barrier\n"
      "v_mfma_f32_16x16x32_bf16 v[76:79], v[212:215], v[252:255], v[76:79]\n"
      "ds_read_b64_tr_b16 v[178:179], v38 offset:29696\n"
      "v_mfma_f32_16x16x32_bf16 v[88:91], v[216:219], v[248:251], v[88:91]\n"
      "ds_read_b64_tr_b16 v[190:191], v41 offset:29696\n"
      "v_mfma_f32_16x16x32_bf16 v[92:95], v[216:219], v[252:255], v[92:95]\n"
      "ds_read_b64_tr_b16 v[182:183], v39 offset:29696\n"
      "v_mfma_f32_16x16x32_bf16 v[104:107], v[220:223], v[248:251], v[104:107]\n"
      "ds_read_b64_tr_b16 v[194:195], v42 offset:29696\n"
      "v_mfma_f32_16x16x32_bf16 v[108:111], v[220:223], v[252:255], v[108:111]\n"
      "ds_read_b64_tr_b16 v[186:187], v40 offset:29696\n"
      "v_mfma_f32_16x16x32_bf16 v[120:123], v[224:227], v[248:251], v[120:123]\n"
      "ds_read_b64_tr_b16 v[198:199], v43 offset:29696\n"
      "v_mfma_f32_16x16x32_bf16 v[124:127], v[224:227], v[252:255], v[124:127]\n"
      "ds_read_b64_tr_b16 v[202:203], v44 offset:29696\n"
      "v_mfma_f32_16x16x32_bf16 v[136:139], v[228:231], v[248:251], v[136:139]\n"
      "ds_read_b64_tr_b16 v[206:207], v44 offset:29728\n"
      "v_mfma_f32_16x16x32_bf16 v[140:143], v[228:231], v[252:255], v[140:143]\n"
      "v_mfma_f32_16x16x32_bf16 v[152:155], v[232:235], v[248:251], v[152:155]\n"
      "v_mfma_f32_16x16x32_bf16 v[156:159], v[232:235], v[252:255], v[156:159]\n"
      "v_mfma_f32_16x16x32_bf16 v[168:171], v[236:239], v[248:251], v[168:171]\n"
      "v_mfma_f32_16x16x32_bf16 v[172:175], v[236:239], v[252:255], v[172:175]\n"
      "s_waitcnt lgkmcnt(0)\n"
      "v_mfma_f32_16x16x32_bf16 v[48:51], v[176:179], v[240:243], v[48:51]\n"
      "v_mfma_f32_16x16x32_bf16 v[52:55], v[176:179], v[244:247], v[52:55]\n"
      "ds_read_b128 v[248:251], v46 offset:41984\n"
      "v_mfma_f32_16x16x32_bf16 v[64:67], v[180:183], v[240:243], v[64:67]\n"
      "ds_read_b128 v[252:255], v46 offset:62976\n"
      "s_barrier\n"
      "v_mfma_f32_16x16x32_bf16 v[68:71], v[180:183], v[244:247], v[68:71]\n"
      "ds_read_b64_tr_b16 v[208:209], v38 offset:44544\n"
      "v_mfma_f32_16x16x32_bf16 v[80:83], v[184:187], v[240:243], v[80:83]\n"
      "ds_read_b64_tr_b16 v[220:221], v41 offset:44544\n"
      "v_mfma_f32_16x16x32_bf16 v[84:87], v[184:187], v[244:247], v[84:87]\n"
      "ds_read_b64_tr_b16 v[212:213], v39 offset:44544\n"
      "v_mfma_f32_16x16x32_bf16 v[96:99], v[188:191], v[240:243], v[96:99]\n"
      "ds_read_b64_tr_b16 v[224:225], v42 offset:44544\n"
      "v_mfma_f32_16x16x32_bf16 v[100:103], v[188:191], v[244:247], v[100:103]\n"
      "ds_read_b64_tr_b16 v[216:217], v40 offset:44544\n"
      "v_mfma_f32_16x16x32_bf16 v[112:115], v[192:195], v[240:243], v[112:115]\n"
      "ds_read_b64_tr_b16 v[228:229], v43 offset:44544\n"
      "v_mfma_f32_16x16x32_bf16 v[116:119], v[192:195], v[244:247], v[116:119]\n"
      "ds_read_b64_tr_b16 v[232:233], v44 offset:44544\n"
      "v_mfma_f32_16x16x32_bf16 v[128:131], v[196:199], v[240:243], v[128:131]\n"
      "ds_read_b64_tr_b16 v[236:237], v44 offset:44576\n"
      "v_mfma_f32_16x16x32_bf16 v[132:135], v[196:199], v[244:247], v[132:135]\n"
      "v_mfma_f32_16x16x32_bf16 v[144:147], v[200:203], v[240:243], v[144:147]\n"
      "v_mfma_f32_16x16x32_bf16 v[148:151], v[200:203], v[244:247], v[148:151]\n"
      "v_mfma_f32_16x16x32_bf16 v[160:163], v[204:207], v[240:243], v[160:163]\n"
      "v_mfma_f32_16x16x32_bf16 v[164:167], v[204:207], v[244:247], v[164:167]\n"
      "s_waitcnt lgkmcnt(0)\n"
      "v_mfma_f32_16x16x32_bf16 v[56:59], v[176:179], v[248:251], v[56:59]\n"
      "s_add_u32 %[kx], %[kx], 64\n"
      "s_cmp_lg_u32 %[kx], 1280\n"
      "s_cselect_b32 %[kx], %[kx], 0\n"
      "v_add_u32 v46, %[kx], v37\n"
      "v_mfma_f32_16x16x32_bf16 v[60:63], v[176:179], v[252:255], v[60:63]\n"
      "ds_read_b128 v[240:243], v46 offset:0\n"
      "v_mfma_f32_16x16x32_bf16 v[72:75], v[180:183], v[248:251], v[72:75]\n"
      "ds_read_b128 v[244:247], v46 offset:20992\n"
      "s_barrier\n"
      "v_mfma_f32_16x16x32_bf16 v[76:79], v[180:183], v[252:255], v[76:79]\n"
      "ds_read_b64_tr_b16 v[210:211], v38 offset:59392\n"
      "v_mfma_f32_16x16x32_bf16 v[88:91], v[184:187], v[248:251], v[88:91]\n"
      "ds_read_b64_tr_b16 v[222:223], v41 offset:59392\n"
      "v_mfma_f32_16x16x32_bf16 v[92:95], v[184:187], v[252:255], v[92:95]\n"
      "ds_read_b64_tr_b16 v[214:215], v39 offset:59392\n"
      "v_mfma_f32_16x16x32_bf16 v[104:107], v[188:191], v[248:251], v[104:107]\n"
      "ds_read_b64_tr_b16 v[226:227], v42 offset:59392\n"
      "v_mfma_f32_16x16x32_bf16 v[108:111], v[188:191], v[252:255], v[108:111]\n"
      "ds_read_b64_tr_b16 v[218:219], v40 offset:59392\n"
      "v_mfma_f32_16x16x32_bf16 v[120:123], v[192:195], v[248:251], v[120:123]\n"
      "ds_read_b64_tr_b16 v[230:231], v43 offset:59392\n"
      "v_mfma_f32_16x16x32_bf16 v[124:127], v[192:195], v[252:255], v[124:127]\n"
      "ds_read_b64_tr_b16 v[234:235], v44 offset:59392\n"
      "v_mfma_f32_16x16x32_bf16 v[136:139], v[196:199], v[248:251], v[136:139]\n"
      "ds_read_b64_tr_b16 v[238:239], v44 offset:59424\n"
      "v_mfma_f32_16x16x32_bf16 v[140:143], v[196:199], v[252:255], v[140:143]\n"
      "v_mfma_f32_16x16x32_bf16 v[152:155], v[200:203], v[248:251], v[152:155]\n"
      "v_mfma_f32_16x16x32_bf16 v[156:159], v[200:203], v[252:255], v[156:159]\n"
      "v_mfma_f32_16x16x32_bf16 v[168:171], v[204:207], v[248:251], v[168:171]\n"
      "v_mfma_f32_16x16x32_bf16 v[172:175], v[204:207], v[252:255], v[172:175]\n"
      "s_waitcnt lgkmcnt(0)\n"
      "v_mfma_f32_16x16x32_bf16 v[48:51], v[208:211], v[240:243], v[48:51]\n"
      "v_mfma_f32_16x16x32_bf16 v[52:55], v[208:211], v[244:247], v[52:55]\n"
      "ds_read_b128 v[248:251], v46 offset:41984\n"
      "v_mfma_f32_16x16x32_bf16 v[64:67], v[212:215], v[240:243], v[64:67]\n"
      "ds_read_b128 v[252:255], v46 offset:62976\n"
      "s_barrier\n"
      "v_mfma_f32_16x16x32_bf16 v[68:71], v[212:215], v[244:247], v[68:71]\n"
      "ds_read_b64_tr_b16 v[176:177], v38 offset:0\n"
      "v_mfma_f32_16x16x32_bf16 v[80:83], v[216:219], v[240:243], v[80:83]\n"
      "ds_read_b64_tr_b16 v[188:189], v41 offset:0\n"
      "v_mfma_f32_16x16x32_bf16 v[84:87], v[216:219], v[244:247], v[84:87]\n"
      "ds_read_b64_tr_b16 v[180:181], v39 offset:0\n"
      "v_mfma_f32_16x16x32_bf16 v[96:99], v[220:223], v[240:243], v[96:99]\n"
      "ds_read_b64_tr_b16 v[192:193], v42 offset:0\n"
      "v_mfma_f32_16x16x32_bf16 v[100:103], v[220:223], v[244:247], v[100:103]\n"
      "ds_read_b64_tr_b16 v[184:185], v40 offset:0\n"
      "v_mfma_f32_16x16x32_bf16 v[112:115], v[224:227], v[240:243], v[112:115]\n"
      "ds_read_b64_tr_b16 v[196:197], v43 offset:0\n"
      "v_mfma_f32_16x16x32_bf16 v[116:119], v[224:227], v[244:247], v[116:119]\n"
      "ds_read_b64_tr_b16 v[200:201], v44 offset:0\n"
      "v_mfma_f32_16x16x32_bf16 v[128:131], v[228:231], v[240:243], v[128:131]\n"
      "ds_read_b64_tr_b16 v[204:205], v44 offset:32\n"
      "v_mfma_f32_16x16x32_bf16 v[132:135], v[228:231], v[244:247], v[132:135]\n"
      "v_mfma_f32_16x16x32_bf16 v[144:147], v[232:235], v[240:243], v[144:147]\n"
      "v_mfma_f32_16x16x32_bf16 v[148:151], v[232:235], v[244:247], v[148:151]\n"
      "v_mfma_f32_16x16x32_bf16 v[160:163], v[236:239], v[240:243], v[160:163]\n"
      "v_mfma_f32_16x16x32_bf16 v[164:167], v[236:239], v[244:247], v[164:167]\n"
      "s_waitcnt lgkmcnt(0)\n"
      "v_mfma_f32_16x16x32_bf16 v[56:59], v[208:211], v[248:251], v[56:59]\n"
      "s_add_u32 %[kx], %[kx], 64\n"
      "s_cmp_lg_u32 %[kx], 1280\n"
      "s_cselect_b32 %[kx], %[kx], 0\n"
      "v_add_u32 v46, %[kx], v37\n"
      "v_mfma_f32_16x16x32_bf16 v[60:63], v[208:211], v[252:255], v[60:63]\n"
      "ds_read_b128 v[240:243], v46 offset:0\n"
      "v_mfma_f32_16x16x32_bf16 v[72:75], v[212:215], v[248:251], v[72:75]\n"
      "ds_read_b128 v[244:247], v46 offset:20992\n"
      "s_barrier\n"
      "v_mfma_f32_16x16x32_bf16 v[76:79], v[212:215], v[252:255], v[76:79]\n"
      "ds_read_b64_tr_b16 v[178:179], v38 offset:14848\n"
      "v_mfma_f32_16x16x32_bf16 v[88:91], v[216:219], v[248:251], v[88:91]\n"
      "ds_read_b64_tr_b16 v[190:191], v41 offset:14848\n"
      "v_mfma_f32_16x16x32_bf16 v[92:95], v[216:219], v[252:255], v[92:95]\n"
      "ds_read_b64_tr_b16 v[182:183], v39 offset:14848\n"
      "v_mfma_f32_16x16x32_bf16 v[104:107], v[220:223], v[248:251], v[104:107]\n"
      "ds_read_b64_tr_b16 v[194:195], v42 offset:14848\n"
      "v_mfma_f32_16x16x32_bf16 v[108:111], v[220:223], v[252:255], v[108:111]\n"
      "ds_read_b64_tr_b16 v[186:187], v40 offset:14848\n"
      "v_mfma_f32_16x16x32_bf16 v[120:123], v[224:227], v[248:251], v[120:123]\n"
      "ds_read_b64_tr_b16 v[198:199], v43 offset:14848\n"
      "v_mfma_f32_16x16x32_bf16 v[124:127], v[224:227], v[252:255], v[124:127]\n"
      "ds_read_b64_tr_b16 v[202:203], v44 offset:14848\n"
      "v_mfma_f32_16x16x32_bf16 v[136:139], v[228:231], v[248:251], v[136:139]\n"
      "ds_read_b64_tr_b16 v[206:207], v44 offset:14880\n"
      "v_mfma_f32_16x16x32_bf16 v[140:143], v[228:231], v[252:255], v[140:143]\n"
      "v_mfma_f32_16x16x32_bf16 v[152:155], v[232:235], v[248:251], v[152:155]\n"
      "v_mfma_f32_16x16x32_bf16 v[156:159], v[232:235], v[252:255], v[156:159]\n"
      "v_mfma_f32_16x16x32_bf16 v[168:171], v[236:239], v[248:251], v[168:171]\n"
      "v_mfma_f32_16x16x32_bf16 v[172:175], v[236:239], v[252:255], v[172:175]\n"
      "s_sub_u32 %[cnt], %[cnt], 1\n"
      "s_cbranch_scc1 2f\n"
      "s_waitcnt lgkmcnt(0)\n"
      "v_mfma_f32_16x16x32_bf16 v[48:51], v[176:179], v[240:243], v[48:51]\n"
      "v_mfma_f32_16x16x32_bf16 v[52:55], v[176:179], v[244:247], v[52:55]\n"
      "ds_read_b128 v[248:251], v46 offset:41984\n"
      "v_mfma_f32_16x16x32_bf16 v[64:67], v[180:183], v[240:243], v[64:67]\n"
      "ds_read_b128 v[252:255], v46 offset:62976\n"
      "s_barrier\n"
      "v_mfma_f32_16x16x32_bf16 v[68:71], v[180:183], v[244:247], v[68:71]\n"
      "ds_read_b64_tr_b16 v[208:209], v38 offset:29696\n"
      "v_mfma_f32_16x16x32_bf16 v[80:83], v[184:187], v[240:243], v[80:83]\n"
      "ds_read_b64_tr_b16 v[220:221], v41 offset:29696\n"
      "v_mfma_f32_16x16x32_bf16 v[84:87], v[184:187], v[244:247], v[84:87]\n"
      "ds_read_b64_tr_b16 v[212:213], v39 offset:29696\n"
      "v_mfma_f32_16x16x32_bf16 v[96:99], v[188:191], v[240:243], v[96:99]\n"
      "ds_read_b64_tr_b16 v[224:225], v42 offset:29696\n"
      "v_mfma_f32_16x16x32_bf16 v[100:103], v[188:191], v[244:247], v[100:103]\n"
      "ds_read_b64_tr_b16 v[216:217], v40 offset:29696\n"
      "v_mfma_f32_16x16x32_bf16 v[112:115], v[192:195], v[240:243], v[112:115]\n"
      "ds_read_b64_tr_b16 v[228:229], v43 offset:29696\n"
      "v_mfma_f32_16x16x32_bf16 v[116:119], v[192:195], v[244:247], v[116:119]\n"
      "ds_read_b64_tr_b16 v[232:233], v44 offset:29696\n"
      "v_mfma_f32_16x16x32_bf16 v[128:131], v[196:199], v[240:243], v[128:131]\n"
      "ds_read_b64_tr_b16 v[236:237], v44 offset:29728\n"
      "v_mfma_f32_16x16x32_bf16 v[132:135], v[196:199], v[244:247], v[132:135]\n"
      "v_mfma_f32_16x16x32_bf16 v[144:147], v[200:203], v[240:243], v[144:147]\n"
      "v_mfma_f32_16x16x32_bf16 v[148:151], v[200:203], v[244:247], v[148:151]\n"
      "v_mfma_f32_16x16x32_bf16 v[160:163], v[204:207], v[240:243], v[160:163]\n"
      "v_mfma_f32_16x16x32_bf16 v[164:167], v[204:207], v[244:247], v[164:167]\n"
      "s_waitcnt lgkmcnt(0)\n"
      "v_mfma_f32_16x16x32_bf16 v[56:59], v[176:179], v[248:251], v[56:59]\n"
      "s_add_u32 %[kx], %[kx], 64\n"
      "s_cmp_lg_u32 %[kx], 1280\n"
      "s_cselect_b32 %[kx], %[kx], 0\n"
      "v_add_u32 v46, %[kx], v37\n"
      "v_mfma_f32_16x16x32_bf16 v[60:63], v[176:179], v[252:255], v[60:63]\n"
      "ds_read_b128 v[240:243], v46 offset:0\n"
      "v_mfma_f32_16x16x32_bf16 v[72:75], v[180:183], v[248:251], v[72:75]\n"
      "ds_read_b128 v[244:247], v46 offset:20992\n"
      "s_barrier\n"
      "v_mfma_f32_16x16x32_bf16 v[76:79], v[180:183], v[252:255], v[76:79]\n"
      "ds_read_b64_tr_b16 v[210:211], v38 offset:44544\n"
      "v_mfma_f32_16x16x32_bf16 v[88:91], v[184:187], v[248:251], v[88:91]\n"
      "ds_read_b64_tr_b16 v[222:223], v41 offset:44544\n"
      "v_mfma_f32_16x16x32_bf16 v[92:95], v[184:187], v[252:255], v[92:95]\n"
      "ds_read_b64_tr_b16 v[214:215], v39 offset:44544\n"
      "v_mfma_f32_16x16x32_bf16 v[104:107], v[188:191], v[248:251], v[104:107]\n"
      "ds_read_b64_tr_b16 v[226:227], v42 offset:44544\n"
      "v_mfma_f32_16x16x32_bf16 v[108:111], v[188:191], v[252:255], v[108:111]\n"
      "ds_read_b64_tr_b16 v[218:219], v40 offset:44544\n"
      "v_mfma_f32_16x16x32_bf16 v[120:123], v[192:195], v[248:251], v[120:123]\n"
      "ds_read_b64_tr_b16 v[230:231], v43 offset:44544\n"
      "v_mfma_f32_16x16x32_bf16 v[124:127], v[192:195], v[252:255], v[124:127]\n"
      "ds_read_b64_tr_b16 v[234:235], v44 offset:44544\n"
      "v_mfma_f32_16x16x32_bf16 v[136:139], v[196:199], v[248:251], v[136:139]\n"
      "ds_read_b64_tr_b16 v[238:239], v44 offset:44576\n"
      "v_mfma_f32_16x16x32_bf16 v[140:143], v[196:199], v[252:255], v[140:143]\n"
      "v_mfma_f32_16x16x32_bf16 v[152:155], v[200:203], v[248:251], v[152:155]\n"
      "v_mfma_f32_16x16x32_bf16 v[156:159], v[200:203], v[252:255], v[156:159]\n"
      "v_mfma_f32_16x16x32_bf16 v[168:171], v[204:207], v[248:251], v[168:171]\n"
      "v_mfma_f32_16x16x32_bf16 v[172:175], v[204:207], v[252:255], v[172:175]\n"
      "s_branch 1b\n"
      "2:\n"
      "s_waitcnt lgkmcnt(0)\n"
      "s_nop 4\n"
      "v_cvt_pk_bf16_f32 v176, v144, v145\n"
      "v_cvt_pk_bf16_f32 v177, v146, v147\n"
      "v_cvt_pk_bf16_f32 v184, v160, v161\n"
      "v_cvt_pk_bf16_f32 v185, v162, v163\n"
      "ds_write_b64 v45, v[176:177] offset:0\n"
      "v_cvt_pk_bf16_f32 v178, v148, v149\n"
      "v_cvt_pk_bf16_f32 v179, v150, v151\n"
      "v_cvt_pk_bf16_f32 v186, v164, v165\n"
      "v_cvt_pk_bf16_f32 v187, v166, v167\n"
      "ds_write_b64 v45, v[178:179] offset:512\n"
      "v_cvt_pk_bf16_f32 v180, v152, v153\n"
      "v_cvt_pk_bf16_f32 v181, v154, v155\n"
      "v_cvt_pk_bf16_f32 v188, v168, v169\n"
      "v_cvt_pk_bf16_f32 v189, v170, v171\n"
      "ds_write_b64 v45, v[180:181] offset:1024\n"
      "v_cvt_pk_bf16_f32 v182, v156, v157\n"
      "v_cvt_pk_bf16_f32 v183, v158, v159\n"
      "v_cvt_pk_bf16_f32 v190, v172, v173\n"
      "v_cvt_pk_bf16_f32 v191, v174, v175\n"
      "ds_write_b64 v45, v[182:183] offset:1536\n"
      "s_mov_b64 exec, 0xffff\n"
      "ds_write_b64 v47, v[184:185] offset:0\n"
      "ds_write_b64 v47, v[186:187] offset:128\n"
      "ds_write_b64 v47, v[188:189] offset:256\n"
      "ds_write_b64 v47, v[190:191] offset:384\n"
      "s_mov_b64 exec, -1\n"
      "s_waitcnt lgkmcnt(0)\n"
      "s_barrier\n"
      "s_setprio 3\n"
          : "=&{v[48:79]}"(C0), "=&{v[80:111]}"(C1), "=&{v[112:143]}"(C2), "=&{v[144:175]}"(C3), [cnt] "=&s"(cnt), [kx] "+s"(kx), GPAR
          :
          : "memory", "scc", "v176", "v177", "v178", "v179", "v180", "v181", "v182", "v183", "v184", "v185", "v186", "v187", "v188", "v189", "v190", "v191", "v192", "v193", "v194", "v195", "v196", "v197", "v198", "v199", "v200", "v201", "v202", "v203", "v204", "v205", "v206", "v207", "v208", "v209", "v210", "v211", "v212", "v213", "v214", "v215", "v216", "v217", "v218", "v219", "v220", "v221", "v222", "v223", "v224", "v225", "v226", "v227", "v228", "v229", "v230", "v231", "v232", "v233", "v234", "v235", "v236", "v237", "v238", "v239", "v240", "v241", "v242", "v243", "v244", "v245", "v246", "v247", "v248", "v249", "v250", "v251", "v252", "v253", "v254", "v255");
    }
    asm volatile("" : "+s"(bid), "+s"(w));
    {
      IDX_DEFS;
      const int v = w - 4, bks = loc < 17 ? 4 * loc + v : 0;
      const u32 rot = (3 * (loc >> 3) + ((3 * j3) >> 1)) % 12;
      const u32 wo0 = (48 + 5 * (j3 & 3)) * 5120;
      SA = u32x16{v * 61440 + rot * 5120, v * 49152 + rot * 4096, (u32)v * 61440, (u32)v * 49152, (u32)v * 49152 + 49152,
                  wo0 + v * 1024, ldsb + v * 1024, 19u - ((v + 1) >> 2), wo0 + (75 + v) * 1024, ldsb + L_RT + v * 1024, 7u - ((v + 3) >> 2),
                  (u32)(4 * (loc % 10) + v) * 1024, 11u - ((v + 3) >> 2), (u32)(loc - 17) >> 31, k0 * 32, ldsb + L_RT + v * 5120};
      const u64 hd = (u64)(ws + WS_ACT + (u32)(g1 * ACT_G + (6 * j1 + 3 * (v >> 1)) * 4096)), hp = hd + (v & 1) * 8192, hl = hd + 4096;
      const u64 us = (u64)(bks < 48 ? wd : wo), ud = (u64)(ws + WS_WD + (u32)(xcd * WD_J + bks * 5120));
      const u64 fo = (u64)(ws + WS_HFLAG + (u32)bid * 64), fb = (u64)(ws + WS_HFLAG);
      SB = u32x8{(u32)hp, (u32)(hp >> 32), (u32)hl, (u32)(hl >> 32), (u32)us, (u32)(us >> 32), (u32)ud, (u32)(ud >> 32)};
      SC = u32x4{(u32)fo, (u32)(fo >> 32), (u32)fb, (u32)(fb >> 32)};
      SW = mk_srd(ws + WS_WD + (u32)(j3 * WD_J), WD_J); SH = mk_srd(ws + WS_ACT + (u32)(g3 * ACT_G), ACT_G); SK = mk_srd(ws + WS_KV, 4 * CHUNK);
      asm volatile(
      "s_nop 2\n"
      "s_mov_b32 s92, s59\n"
      "s_mov_b32 s93, s60\n"
      "123:\n"
      "s_add_u32 m0, s92, 0\n"
      "s_add_u32 s90, s92, 41984\n"
      "buffer_load_dwordx4 v8, s[84:87], s90 offen sc1 lds\n"
      "s_add_u32 s92, s92, 4096\n"
      "s_cmp_ge_u32 s92, 41984\n"
      "s_cselect_b32 s90, 41984, 0\n"
      "s_sub_u32 s92, s92, s90\n"
      "s_sub_u32 s93, s93, 1\n"
      "s_cmp_lg_u32 s93, 0\n"
      "s_cbranch_scc1 123b\n"
      "s_cmp_eq_u32 s61, 0\n"
      "s_cbranch_scc1 60f\n"
      "s_add_u32 m0, s63, 0\n"
      "s_nop 0\n"
      "global_load_lds_dwordx4 v32, s[68:69]\n"
      "s_add_u32 m0, s63, 1024\n"
      "s_nop 0\n"
      "global_load_lds_dwordx4 v33, s[68:69]\n"
      "s_add_u32 m0, s63, 2048\n"
      "s_nop 0\n"
      "global_load_lds_dwordx4 v34, s[68:69]\n"
      "s_add_u32 m0, s63, 3072\n"
      "s_nop 0\n"
      "global_load_lds_dwordx4 v35, s[68:69]\n"
      "s_add_u32 m0, s63, 4096\n"
      "s_nop 0\n"
      "global_load_lds_dwordx4 v36, s[68:69]\n"
      "60:\n"
      "v_mul_f32 v48, v48, v96\n"
      "v_mul_f32 v49, v49, v97\n"
      "v_mul_f32 v50, v50, v98\n"
      "v_mul_f32 v51, v51, v99\n"
      "v_mul_f32 v64, v64, v112\n"
      "v_mul_f32 v65, v65, v113\n"
      "v_mul_f32 v66, v66, v114\n"
      "v_mul_f32 v67, v67, v115\n"
      "v_cvt_pk_bf16_f32 v176, v48, v49\n"
      "v_cvt_pk_bf16_f32 v177, v50, v51\n"
      "v_cvt_pk_bf16_f32 v178, v64, v65\n"
      "v_cvt_pk_bf16_f32 v179, v66, v67\n"
      "global_store_dwordx4 v8, v[176:179], s[64:65] offset:0 sc1\n"
      "v_mul_f32 v52, v52, v100\n"
      "v_mul_f32 v53, v53, v101\n"
      "v_mul_f32 v54, v54, v102\n"
      "v_mul_f32 v55, v55, v103\n"
      "v_mul_f32 v68, v68, v116\n"
      "v_mul_f32 v69, v69, v117\n"
      "v_mul_f32 v70, v70, v118\n"
      "v_mul_f32 v71, v71, v119\n"
      "v_cvt_pk_bf16_f32 v180, v52, v53\n"
      "v_cvt_pk_bf16_f32 v181, v54, v55\n"
      "v_cvt_pk_bf16_f32 v182, v68, v69\n"
      "v_cvt_pk_bf16_f32 v183, v70, v71\n"
      "global_store_dwordx4 v8, v[180:183], s[64:65] offset:1024 sc1\n"
      "v_mul_f32 v56, v56, v104\n"
      "v_mul_f32 v57, v57, v105\n"
      "v_mul_f32 v58, v58, v106\n"
      "v_mul_f32 v59, v59, v107\n"
      "v_mul_f32 v72, v72, v120\n"
      "v_mul_f32 v73, v73, v121\n"
      "v_mul_f32 v74, v74, v122\n"
      "v_mul_f32 v75, v75, v123\n"
      "v_cvt_pk_bf16_f32 v184, v56, v57\n"
      "v_cvt_pk_bf16_f32 v185, v58, v59\n"
      "v_cvt_pk_bf16_f32 v186, v72, v73\n"
      "v_cvt_pk_bf16_f32 v187, v74, v75\n"
      "global_store_dwordx4 v8, v[184:187], s[64:65] offset:2048 sc1\n"
      "v_mul_f32 v60, v60, v108\n"
      "v_mul_f32 v61, v61, v109\n"
      "v_mul_f32 v62, v62, v110\n"
      "v_mul_f32 v63, v63, v111\n"
      "v_mul_f32 v76, v76, v124\n"
      "v_mul_f32 v77, v77, v125\n"
      "v_mul_f32 v78, v78, v126\n"
      "v_mul_f32 v79, v79, v127\n"
      "v_cvt_pk_bf16_f32 v188, v60, v61\n"
      "v_cvt_pk_bf16_f32 v189, v62, v63\n"
      "v_cvt_pk_bf16_f32 v190, v76, v77\n"
      "v_cvt_pk_bf16_f32 v191, v78, v79\n"
      "global_store_dwordx4 v8, v[188:191], s[64:65] offset:3072 sc1\n"
      "v_mul_f32 v80, v80, v128\n"
      "v_mul_f32 v81, v81, v129\n"
      "v_mul_f32 v82, v82, v130\n"
      "v_mul_f32 v83, v83, v131\n"
      "v_mul_f32 v84, v84, v132\n"
      "v_mul_f32 v85, v85, v133\n"
      "v_mul_f32 v86, v86, v134\n"
      "v_mul_f32 v87, v87, v135\n"
      "v_mul_f32 v88, v88, v136\n"
      "v_mul_f32 v89, v89, v137\n"
      "v_mul_f32 v90, v90, v138\n"
      "v_mul_f32 v91, v91, v139\n"
      "v_mul_f32 v92, v92, v140\n"
      "v_mul_f32 v93, v93, v141\n"
      "v_mul_f32 v94, v94, v142\n"
      "v_mul_f32 v95, v95, v143\n"
      "v_cvt_pk_bf16_f32 v192, v80, v81\n"
      "v_cvt_pk_bf16_f32 v193, v82, v83\n"
      "v_cvt_pk_bf16_f32 v194, v84, v85\n"
      "v_cvt_pk_bf16_f32 v195, v86, v87\n"
      "v_cvt_pk_bf16_f32 v196, v88, v89\n"
      "v_cvt_pk_bf16_f32 v197, v90, v91\n"
      "v_cvt_pk_bf16_f32 v198, v92, v93\n"
      "v_cvt_pk_bf16_f32 v199, v94, v95\n"
      "s_nop 0\n"
      "v_permlane16_swap_b32 v192, v194\n"
      "v_permlane16_swap_b32 v193, v195\n"
      "global_store_dwordx4 v24, v[192:195], s[66:67] offset:0 sc1\n"
      "v_permlane16_swap_b32 v196, v198\n"
      "v_permlane16_swap_b32 v197, v199\n"
      "global_store_dwordx4 v24, v[196:199], s[66:67] offset:2048 sc1\n"
      "s_setprio 3\n"
      "s_waitcnt vmcnt(11)\n"
      "s_cmp_lg_u32 s61, 0\n"
      "s_cbranch_scc1 61f\n"
      "s_waitcnt vmcnt(6)\n"
      "61:\n"
      "s_mov_b64 exec, 1\n"
      "ds_add_u32 v20, v21 offset:64\n"
      "s_mov_b64 exec, -1\n"
      "s_cbranch_scc0 62f\n"
      "s_waitcnt vmcnt(6)\n"
      "ds_read_b64_tr_b16 v[200:201], v25 offset:0\n"
      "ds_read_b64_tr_b16 v[202:203], v25 offset:640\n"
      "ds_read_b64_tr_b16 v[204:205], v25 offset:8\n"
      "ds_read_b64_tr_b16 v[206:207], v25 offset:648\n"
      "ds_read_b64_tr_b16 v[208:209], v25 offset:16\n"
      "ds_read_b64_tr_b16 v[210:211], v25 offset:656\n"
      "ds_read_b64_tr_b16 v[212:213], v25 offset:24\n"
      "ds_read_b64_tr_b16 v[214:215], v25 offset:664\n"
      "ds_read_b64_tr_b16 v[216:217], v25 offset:32\n"
      "ds_read_b64_tr_b16 v[218:219], v25 offset:672\n"
      "s_waitcnt lgkmcnt(8)\n"
      "global_store_dwordx4 v8, v[200:203], s[70:71] offset:0 sc1\n"
      "s_waitcnt lgkmcnt(6)\n"
      "global_store_dwordx4 v8, v[204:207], s[70:71] offset:1024 sc1\n"
      "s_waitcnt lgkmcnt(4)\n"
      "global_store_dwordx4 v8, v[208:211], s[70:71] offset:2048 sc1\n"
      "s_waitcnt lgkmcnt(2)\n"
      "global_store_dwordx4 v8, v[212:215], s[70:71] offset:3072 sc1\n"
      "s_waitcnt lgkmcnt(0)\n"
      "global_store_dwordx4 v9, v[216:219], s[70:71] offset:0 sc1\n"
      "62:\n"
      "s_mov_b32 s92, s59\n"
      "s_mov_b32 s93, s60\n"
      "124:\n"
      "s_add_u32 m0, s92, 41984\n"
      "s_add_u32 s90, s92, 83968\n"
      "buffer_load_dwordx4 v8, s[84:87], s90 offen sc1 lds\n"
      "s_add_u32 s92, s92, 4096\n"
      "s_cmp_ge_u32 s92, 41984\n"
      "s_cselect_b32 s90, 41984, 0\n"
      "s_sub_u32 s92, s92, s90\n"
      "s_sub_u32 s93, s93, 1\n"
      "s_cmp_lg_u32 s93, 0\n"
      "s_cbranch_scc1 124b\n"
      "ds_read_b32 v236, v20 offset:100\n"
      "v_mul_f32 v240, v144, v144\n"
      "v_mul_f32 v241, v148, v148\n"
      "v_mul_f32 v242, v152, v152\n"
      "v_mul_f32 v243, v156, v156\n"
      "v_fmac_f32 v240, v145, v145\n"
      "v_fmac_f32 v241, v149, v149\n"
      "v_fmac_f32 v242, v153, v153\n"
      "v_fmac_f32 v243, v157, v157\n"
      "v_fmac_f32 v240, v146, v146\n"
      "v_fmac_f32 v241, v150, v150\n"
      "v_fmac_f32 v242, v154, v154\n"
      "v_fmac_f32 v243, v158, v158\n"
      "v_fmac_f32 v240, v147, v147\n"
      "v_fmac_f32 v241, v151, v151\n"
      "v_fmac_f32 v242, v155, v155\n"
      "v_fmac_f32 v243, v159, v159\n"
      "s_mov_b64 exec, 0xffff\n"
      "v_fmac_f32 v240, v160, v160\n"
      "v_fmac_f32 v241, v164, v164\n"
      "v_fmac_f32 v242, v168, v168\n"
      "v_fmac_f32 v243, v172, v172\n"
      "v_fmac_f32 v240, v161, v161\n"
      "v_fmac_f32 v241, v165, v165\n"
      "v_fmac_f32 v242, v169, v169\n"
      "v_fmac_f32 v243, v173, v173\n"
      "v_fmac_f32 v240, v162, v162\n"
      "v_fmac_f32 v241, v166, v166\n"
      "v_fmac_f32 v242, v170, v170\n"
      "v_fmac_f32 v243, v174, v174\n"
      "v_fmac_f32 v240, v163, v163\n"
      "v_fmac_f32 v241, v167, v167\n"
      "v_fmac_f32 v242, v171, v171\n"
      "v_fmac_f32 v243, v175, v175\n"
      "s_mov_b64 exec, -1\n"
      "s_nop 1\n"
      "v_permlane16_swap_b32 v240, v241\n"
      "v_permlane16_swap_b32 v242, v243\n"
      "v_add_f32 v240, v240, v241\n"
      "v_add_f32 v242, v242, v243\n"
      "s_nop 1\n"
      "v_permlane32_swap_b32 v240, v242\n"
      "v_add_f32 v240, v240, v242\n"
      "s_waitcnt lgkmcnt(0)\n"
      "v_cmp_nle_f32 vcc, v240, v236\n"
      "s_nop 4\n"
      "s_cbranch_vccz 63f\n"
      "ds_write_b32 v20, v21 offset:96\n"
      "63:\n"
      "s_waitcnt vmcnt(10)\n"
      "s_mov_b64 exec, 1\n"
      "ds_add_rtn_u32 v237, v20, v21 offset:8\n"
      "s_mov_b64 exec, -1\n"
      "s_waitcnt lgkmcnt(0)\n"
      "v_readfirstlane_b32 s90, v237\n"
      "s_cmp_lg_u32 s90, 3\n"
      "s_cbranch_scc1 64f\n"
      "global_store_dword v28, v22, s[72:73] sc1\n"
      "64:\n"
      "s_waitcnt vmcnt(0) lgkmcnt(0)\n"
      "s_mov_b64 exec, 1\n"
      "ds_add_u32 v20, v21 offset:68\n"
      "s_mov_b64 exec, -1\n"
      "s_mov_b32 s91, 0x100000\n"
      "s_branch 125f\n"
      "126:\n"
      "s_sleep 6\n"
      "125:\n"
      "ds_read_b32 v31, v20 offset:80\n"
      "s_sub_u32 s91, s91, 1\n"
      "s_cmp_eq_u32 s91, 0\n"
      "s_cbranch_scc1 127f\n"
      "s_waitcnt lgkmcnt(0)\n"
      "v_readfirstlane_b32 s90, v31\n"
      "s_cmp_lt_u32 s90, 4\n"
      "s_cbranch_scc1 126b\n"
      "127:\n"
      "s_mov_b32 s92, s59\n"
      "s_mov_b32 s93, s60\n"
      "128:\n"
      "s_add_u32 m0, s92, 83968\n"
      "s_add_u32 s90, s92, 125952\n"
      "buffer_load_dwordx4 v8, s[84:87], s90 offen sc1 lds\n"
      "s_add_u32 s92, s92, 4096\n"
      "s_cmp_ge_u32 s92, 41984\n"
      "s_cselect_b32 s90, 41984, 0\n"
      "s_sub_u32 s92, s92, s90\n"
      "s_sub_u32 s93, s93, 1\n"
      "s_cmp_lg_u32 s93, 0\n"
      "s_cbranch_scc1 128b\n"
      "s_mov_b32 s91, 0x20000\n"
      "65:\n"
      "global_load_dword v237, v27, s[74:75] sc1\n"
      "s_sub_u32 s91, s91, 1\n"
      "s_waitcnt vmcnt(0)\n"
      "v_cmp_ne_u32 vcc, v237, v22\n"
      "s_cmp_eq_u32 s91, 0\n"
      "s_cbranch_scc1 66f\n"
      "s_nop 2\n"
      "s_cbranch_vccz 66f\n"
      "s_sleep 8\n"
      "s_branch 65b\n"
      "66:\n"
      "s_mov_b64 exec, 1\n"
      "ds_add_u32 v20, v21 offset:72\n"
      "s_mov_b64 exec, -1\n"
      "s_mov_b32 s91, 0x100000\n"
      "s_branch 129f\n"
      "130:\n"
      "s_sleep 2\n"
      "129:\n"
      "ds_read_b32 v31, v20 offset:8\n"
      "s_sub_u32 s91, s91, 1\n"
      "s_cmp_eq_u32 s91, 0\n"
      "s_cbranch_scc1 131f\n"
      "s_waitcnt lgkmcnt(0)\n"
      "v_readfirstlane_b32 s90, v31\n"
      "s_cmp_lt_u32 s90, 4\n"
      "s_cbranch_scc1 130b\n"
      "131:\n"
      "s_setprio 0\n"
      "s_mov_b32 s95, 4\n"
      "buffer_load_dwordx4 v[120:123], v8, s[76:79], s48 offen sc1\n"
      "buffer_load_dwordx4 v[124:127], v8, s[76:79], s48 offen offset:1024 sc1\n"
      "buffer_load_dwordx4 v[128:131], v8, s[76:79], s48 offen offset:2048 sc1\n"
      "buffer_load_dwordx4 v[132:135], v8, s[76:79], s48 offen offset:3072 sc1\n"
      "buffer_load_dwordx4 v[136:139], v9, s[76:79], s48 offen sc1\n"
      "buffer_load_dwordx4 v[140:143], v10, s[80:83], s49 offen sc1\n"
      "buffer_load_dwordx4 v[144:147], v11, s[80:83], s49 offen sc1\n"
      "buffer_load_dwordx4 v[148:151], v12, s[80:83], s49 offen sc1\n"
      "buffer_load_dwordx4 v[152:155], v13, s[80:83], s49 offen sc1\n"
      "s_add_u32 s48, s48, 5120\n"
      "s_add_u32 s49, s49, 4096\n"
      "s_cmp_lg_u32 s49, s52\n"
      "s_cselect_b32 s48, s48, s50\n"
      "s_cselect_b32 s49, s49, s51\n"
      "buffer_load_dwordx4 v[156:159], v8, s[76:79], s48 offen sc1\n"
      "buffer_load_dwordx4 v[160:163], v8, s[76:79], s48 offen offset:1024 sc1\n"
      "buffer_load_dwordx4 v[164:167], v8, s[76:79], s48 offen offset:2048 sc1\n"
      "buffer_load_dwordx4 v[168:171], v8, s[76:79], s48 offen offset:3072 sc1\n"
      "buffer_load_dwordx4 v[172:175], v9, s[76:79], s48 offen sc1\n"
      "buffer_load_dwordx4 v[176:179], v10, s[80:83], s49 offen sc1\n"
      "buffer_load_dwordx4 v[180:183], v11, s[80:83], s49 offen sc1\n"
      "buffer_load_dwordx4 v[184:187], v12, s[80:83], s49 offen sc1\n"
      "buffer_load_dwordx4 v[188:191], v13, s[80:83], s49 offen sc1\n"
      "s_add_u32 s48, s48, 5120\n"
      "s_add_u32 s49, s49, 4096\n"
      "s_cmp_lg_u32 s49, s52\n"
      "s_cselect_b32 s48, s48, s50\n"
      "s_cselect_b32 s49, s49, s51\n"
      "buffer_load_dwordx4 v[192:195], v8, s[76:79], s48 offen sc1\n"
      "buffer_load_dwordx4 v[196:199], v8, s[76:79], s48 offen offset:1024 sc1\n"
      "buffer_load_dwordx4 v[200:203], v8, s[76:79], s48 offen offset:2048 sc1\n"
      "buffer_load_dwordx4 v[204:207], v8, s[76:79], s48 offen offset:3072 sc1\n"
      "buffer_load_dwordx4 v[208:211], v9, s[76:79], s48 offen sc1\n"
      "buffer_load_dwordx4 v[212:215], v10, s[80:83], s49 offen sc1\n"
      "buffer_load_dwordx4 v[216:219], v11, s[80:83], s49 offen sc1\n"
      "buffer_load_dwordx4 v[220:223], v12, s[80:83], s49 offen sc1\n"
      "buffer_load_dwordx4 v[224:227], v13, s[80:83], s49 offen sc1\n"
      "s_add_u32 s48, s48, 5120\n"
      "s_add_u32 s49, s49, 4096\n"
      "s_cmp_lg_u32 s49, s52\n"
      "s_cselect_b32 s48, s48, s50\n"
      "s_cselect_b32 s49, s49, s51\n"
      "s_waitcnt vmcnt(18)\n"
      "v_mfma_f32_16x16x32_bf16 v[40:43], v[120:123], v[140:143], 0\n"
      "v_mfma_f32_16x16x32_bf16 v[60:63], v[120:123], v[144:147], 0\n"
      "v_mfma_f32_16x16x32_bf16 v[80:83], v[120:123], v[148:151], 0\n"
      "v_mfma_f32_16x16x32_bf16 v[100:103], v[120:123], v[152:155], 0\n"
      "buffer_load_dwordx4 v[120:123], v8, s[76:79], s48 offen sc1\n"
      "v_mfma_f32_16x16x32_bf16 v[44:47], v[124:127], v[140:143], 0\n"
      "v_mfma_f32_16x16x32_bf16 v[64:67], v[124:127], v[144:147], 0\n"
      "v_mfma_f32_16x16x32_bf16 v[84:87], v[124:127], v[148:151], 0\n"
      "v_mfma_f32_16x16x32_bf16 v[104:107], v[124:127], v[152:155], 0\n"
      "buffer_load_dwordx4 v[124:127], v8, s[76:79], s48 offen offset:1024 sc1\n"
      "v_mfma_f32_16x16x32_bf16 v[48:51], v[128:131], v[140:143], 0\n"
      "v_mfma_f32_16x16x32_bf16 v[68:71], v[128:131], v[144:147], 0\n"
      "v_mfma_f32_16x16x32_bf16 v[88:91], v[128:131], v[148:151], 0\n"
      "v_mfma_f32_16x16x32_bf16 v[108:111], v[128:131], v[152:155], 0\n"
      "buffer_load_dwordx4 v[128:131], v8, s[76:79], s48 offen offset:2048 sc1\n"
      "v_mfma_f32_16x16x32_bf16 v[52:55], v[132:135], v[140:143], 0\n"
      "v_mfma_f32_16x16x32_bf16 v[72:75], v[132:135], v[144:147], 0\n"
      "v_mfma_f32_16x16x32_bf16 v[92:95], v[132:135], v[148:151], 0\n"
      "v_mfma_f32_16x16x32_bf16 v[112:115], v[132:135], v[152:155], 0\n"
      "buffer_load_dwordx4 v[132:135], v8, s[76:79], s48 offen offset:3072 sc1\n"
      "v_mfma_f32_16x16x32_bf16 v[56:59], v[136:139], v[140:143], 0\n"
      "v_mfma_f32_16x16x32_bf16 v[76:79], v[136:139], v[144:147], 0\n"
      "v_mfma_f32_16x16x32_bf16 v[96:99], v[136:139], v[148:151], 0\n"
      "v_mfma_f32_16x16x32_bf16 v[116:119], v[136:139], v[152:155], 0\n"
      "buffer_load_dwordx4 v[136:139], v9, s[76:79], s48 offen sc1\n"
      "buffer_load_dwordx4 v[140:143], v10, s[80:83], s49 offen sc1\n"
      "buffer_load_dwordx4 v[144:147], v11, s[80:83], s49 offen sc1\n"
      "buffer_load_dwordx4 v[148:151], v12, s[80:83], s49 offen sc1\n"
      "buffer_load_dwordx4 v[152:155], v13, s[80:83], s49 offen sc1\n"
      "s_add_u32 s48, s48, 5120\n"
      "s_add_u32 s49, s49, 4096\n"
      "s_cmp_lg_u32 s49, s52\n"
      "s_cselect_b32 s48, s48, s50\n"
      "s_cselect_b32 s49, s49, s51\n"
      "s_branch 2f\n"
      "1:\n"
      "s_cmp_lg_u32 s95, 3\n"
      "s_cbranch_scc1 3f\n"
      "132:\n"
      "s_mov_b32 m0, s57\n"
      "s_cmp_ge_u32 s56, 348160\n"
      "s_cselect_b32 s90, 102400, 0\n"
      "s_sub_u32 s56, s56, s90\n"
      "buffer_load_dwordx4 v8, s[76:79], s56 offen sc1 lds\n"
      "s_add_u32 s56, s56, 4096\n"
      "s_add_u32 s57, s57, 4096\n"
      "s_sub_u32 s58, s58, 1\n"
      "s_cmp_lg_u32 s58, 0\n"
      "s_cbranch_scc1 132b\n"
      "s_mov_b32 m0, 163328\n"
      "s_nop 0\n"
      "buffer_load_dword v23, s[76:79], 0 offen sc1 lds\n"
      "global_load_lds_dword v26, s[88:89]\n"
      "3:\n"
      "s_waitcnt vmcnt(18)\n"
      "v_mfma_f32_16x16x32_bf16 v[40:43], v[120:123], v[140:143], v[40:43]\n"
      "v_mfma_f32_16x16x32_bf16 v[60:63], v[120:123], v[144:147], v[60:63]\n"
      "v_mfma_f32_16x16x32_bf16 v[80:83], v[120:123], v[148:151], v[80:83]\n"
      "v_mfma_f32_16x16x32_bf16 v[100:103], v[120:123], v[152:155], v[100:103]\n"
      "buffer_load_dwordx4 v[120:123], v8, s[76:79], s48 offen sc1\n"
      "v_mfma_f32_16x16x32_bf16 v[44:47], v[124:127], v[140:143], v[44:47]\n"
      "v_mfma_f32_16x16x32_bf16 v[64:67], v[124:127], v[144:147], v[64:67]\n"
      "v_mfma_f32_16x16x32_bf16 v[84:87], v[124:127], v[148:151], v[84:87]\n"
      "v_mfma_f32_16x16x32_bf16 v[104:107], v[124:127], v[152:155], v[104:107]\n"
      "buffer_load_dwordx4 v[124:127], v8, s[76:79], s48 offen offset:1024 sc1\n"
      "v_mfma_f32_16x16x32_bf16 v[48:51], v[128:131], v[140:143], v[48:51]\n"
      "v_mfma_f32_16x16x32_bf16 v[68:71], v[128:131], v[144:147], v[68:71]\n"
      "v_mfma_f32_16x16x32_bf16 v[88:91], v[128:131], v[148:151], v[88:91]\n"
      "v_mfma_f32_16x16x32_bf16 v[108:111], v[128:131], v[152:155], v[108:111]\n"
      "buffer_load_dwordx4 v[128:131], v8, s[76:79], s48 offen offset:2048 sc1\n"
      "v_mfma_f32_16x16x32_bf16 v[52:55], v[132:135], v[140:143], v[52:55]\n"
      "v_mfma_f32_16x16x32_bf16 v[72:75], v[132:135], v[144:147], v[72:75]\n"
      "v_mfma_f32_16x16x32_bf16 v[92:95], v[132:135], v[148:151], v[92:95]\n"
      "v_mfma_f32_16x16x32_bf16 v[112:115], v[132:135], v[152:155], v[112:115]\n"
      "buffer_load_dwordx4 v[132:135], v8, s[76:79], s48 offen offset:3072 sc1\n"
      "v_mfma_f32_16x16x32_bf16 v[56:59], v[136:139], v[140:143], v[56:59]\n"
      "v_mfma_f32_16x16x32_bf16 v[76:79], v[136:139], v[144:147], v[76:79]\n"
      "v_mfma_f32_16x16x32_bf16 v[96:99], v[136:139], v[148:151], v[96:99]\n"
      "v_mfma_f32_16x16x32_bf16 v[116:119], v[136:139], v[152:155], v[116:119]\n"
      "buffer_load_dwordx4 v[136:139], v9, s[76:79], s48 offen sc1\n"
      "buffer_load_dwordx4 v[140:143], v10, s[80:83], s49 offen sc1\n"
      "buffer_load_dwordx4 v[144:147], v11, s[80:83], s49 offen sc1\n"
      "buffer_load_dwordx4 v[148:151], v12, s[80:83], s49 offen sc1\n"
      "buffer_load_dwordx4 v[152:155], v13, s[80:83], s49 offen sc1\n"
      "s_add_u32 s48, s48, 5120\n"
      "s_add_u32 s49, s49, 4096\n"
      "s_cmp_lg_u32 s49, s52\n"
      "s_cselect_b32 s48, s48, s50\n"
      "s_cselect_b32 s49, s49, s51\n"
      "2:\n"
      "s_waitcnt vmcnt(18)\n"
      "v_mfma_f32_16x16x32_bf16 v[40:43], v[156:159], v[176:179], v[40:43]\n"
      "v_mfma_f32_16x16x32_bf16 v[60:63], v[156:159], v[180:183], v[60:63]\n"
      "v_mfma_f32_16x16x32_bf16 v[80:83], v[156:159], v[184:187], v[80:83]\n"
      "v_mfma_f32_16x16x32_bf16 v[100:103], v[156:159], v[188:191], v[100:103]\n"
      "buffer_load_dwordx4 v[156:159], v8, s[76:79], s48 offen sc1\n"
      "v_mfma_f32_16x16x32_bf16 v[44:47], v[160:163], v[176:179], v[44:47]\n"
      "v_mfma_f32_16x16x32_bf16 v[64:67], v[160:163], v[180:183], v[64:67]\n"
      "v_mfma_f32_16x16x32_bf16 v[84:87], v[160:163], v[184:187], v[84:87]\n"
      "v_mfma_f32_16x16x32_bf16 v[104:107], v[160:163], v[188:191], v[104:107]\n"
      "buffer_load_dwordx4 v[160:163], v8, s[76:79], s48 offen offset:1024 sc1\n"
      "v_mfma_f32_16x16x32_bf16 v[48:51], v[164:167], v[176:179], v[48:51]\n"
      "v_mfma_f32_16x16x32_bf16 v[68:71], v[164:167], v[180:183], v[68:71]\n"
      "v_mfma_f32_16x16x32_bf16 v[88:91], v[164:167], v[184:187], v[88:91]\n"
      "v_mfma_f32_16x16x32_bf16 v[108:111], v[164:167], v[188:191], v[108:111]\n"
      "buffer_load_dwordx4 v[164:167], v8, s[76:79], s48 offen offset:2048 sc1\n"
      "v_mfma_f32_16x16x32_bf16 v[52:55], v[168:171], v[176:179], v[52:55]\n"
      "v_mfma_f32_16x16x32_bf16 v[72:75], v[168:171], v[180:183], v[72:75]\n"
      "v_mfma_f32_16x16x32_bf16 v[92:95], v[168:171], v[184:187], v[92:95]\n"
      "v_mfma_f32_16x16x32_bf16 v[112:115], v[168:171], v[188:191], v[112:115]\n"
      "buffer_load_dwordx4 v[168:171], v8, s[76:79], s48 offen offset:3072 sc1\n"
      "v_mfma_f32_16x16x32_bf16 v[56:59], v[172:175], v[176:179], v[56:59]\n"
      "v_mfma_f32_16x16x32_bf16 v[76:79], v[172:175], v[180:183], v[76:79]\n"
      "v_mfma_f32_16x16x32_bf16 v[96:99], v[172:175], v[184:187], v[96:99]\n"
      "v_mfma_f32_16x16x32_bf16 v[116:119], v[172:175], v[188:191], v[116:119]\n"
      "buffer_load_dwordx4 v[172:175], v9, s[76:79], s48 offen sc1\n"
      "buffer_load_dwordx4 v[176:179], v10, s[80:83], s49 offen sc1\n"
      "buffer_load_dwordx4 v[180:183], v11, s[80:83], s49 offen sc1\n"
      "buffer_load_dwordx4 v[184:187], v12, s[80:83], s49 offen sc1\n"
      "buffer_load_dwordx4 v[188:191], v13, s[80:83], s49 offen sc1\n"
      "s_add_u32 s48, s48, 5120\n"
      "s_add_u32 s49, s49, 4096\n"
      "s_cmp_lg_u32 s49, s52\n"
      "s_cselect_b32 s48, s48, s50\n"
      "s_cselect_b32 s49, s49, s51\n"
      "s_waitcnt vmcnt(18)\n"
      "v_mfma_f32_16x16x32_bf16 v[40:43], v[192:195], v[212:215], v[40:43]\n"
      "v_mfma_f32_16x16x32_bf16 v[60:63], v[192:195], v[216:219], v[60:63]\n"
      "v_mfma_f32_16x16x32_bf16 v[80:83], v[192:195], v[220:223], v[80:83]\n"
      "v_mfma_f32_16x16x32_bf16 v[100:103], v[192:195], v[224:227], v[100:103]\n"
      "buffer_load_dwordx4 v[192:195], v8, s[76:79], s48 offen sc1\n"
      "v_mfma_f32_16x16x32_bf16 v[44:47], v[196:199], v[212:215], v[44:47]\n"
      "v_mfma_f32_16x16x32_bf16 v[64:67], v[196:199], v[216:219], v[64:67]\n"
      "v_mfma_f32_16x16x32_bf16 v[84:87], v[196:199], v[220:223], v[84:87]\n"
      "v_mfma_f32_16x16x32_bf16 v[104:107], v[196:199], v[224:227], v[104:107]\n"
      "buffer_load_dwordx4 v[196:199], v8, s[76:79], s48 offen offset:1024 sc1\n"
      "v_mfma_f32_16x16x32_bf16 v[48:51], v[200:203], v[212:215], v[48:51]\n"
      "v_mfma_f32_16x16x32_bf16 v[68:71], v[200:203], v[216:219], v[68:71]\n"
      "v_mfma_f32_16x16x32_bf16 v[88:91], v[200:203], v[220:223], v[88:91]\n"
      "v_mfma_f32_16x16x32_bf16 v[108:111], v[200:203], v[224:227], v[108:111]\n"
      "buffer_load_dwordx4 v[200:203], v8, s[76:79], s48 offen offset:2048 sc1\n"
      "v_mfma_f32_16x16x32_bf16 v[52:55], v[204:207], v[212:215], v[52:55]\n"
      "v_mfma_f32_16x16x32_bf16 v[72:75], v[204:207], v[216:219], v[72:75]\n"
      "v_mfma_f32_16x16x32_bf16 v[92:95], v[204:207], v[220:223], v[92:95]\n"
      "v_mfma_f32_16x16x32_bf16 v[112:115], v[204:207], v[224:227], v[112:115]\n"
      "buffer_load_dwordx4 v[204:207], v8, s[76:79], s48 offen offset:3072 sc1\n"
      "v_mfma_f32_16x16x32_bf16 v[56:59], v[208:211], v[212:215], v[56:59]\n"
      "v_mfma_f32_16x16x32_bf16 v[76:79], v[208:211], v[216:219], v[76:79]\n"
      "v_mfma_f32_16x16x32_bf16 v[96:99], v[208:211], v[220:223], v[96:99]\n"
      "v_mfma_f32_16x16x32_bf16 v[116:119], v[208:211], v[224:227], v[116:119]\n"
      "buffer_load_dwordx4 v[208:211], v9, s[76:79], s48 offen sc1\n"
      "buffer_load_dwordx4 v[212:215], v10, s[80:83], s49 offen sc1\n"
      "buffer_load_dwordx4 v[216:219], v11, s[80:83], s49 offen sc1\n"
      "buffer_load_dwordx4 v[220:223], v12, s[80:83], s49 offen sc1\n"
      "buffer_load_dwordx4 v[224:227], v13, s[80:83], s49 offen sc1\n"
      "s_add_u32 s48, s48, 5120\n"
      "s_add_u32 s49, s49, 4096\n"
      "s_cmp_lg_u32 s49, s52\n"
      "s_cselect_b32 s48, s48, s50\n"
      "s_cselect_b32 s49, s49, s51\n"
      "s_sub_u32 s95, s95, 1\n"
      "s_cmp_lg_u32 s95, 1\n"
      "s_cbranch_scc1 1b\n"
      "s_mov_b32 s91, 0x100000\n"
      "s_branch 133f\n"
      "134:\n"
      "s_sleep 6\n"
      "133:\n"
      "ds_read_b32 v31, v20 offset:88\n"
      "s_sub_u32 s91, s91, 1\n"
      "s_cmp_eq_u32 s91, 0\n"
      "s_cbranch_scc1 135f\n"
      "s_waitcnt lgkmcnt(0)\n"
      "v_readfirstlane_b32 s90, v31\n"
      "s_cmp_lt_u32 s90, 4\n"
      "s_cbranch_scc1 134b\n"
      "135:\n"
      "136:\n"
      "s_mov_b32 m0, s54\n"
      "s_cmp_ge_u32 s53, 348160\n"
      "s_cselect_b32 s90, 102400, 0\n"
      "s_sub_u32 s53, s53, s90\n"
      "buffer_load_dwordx4 v8, s[76:79], s53 offen sc1 lds\n"
      "s_add_u32 s53, s53, 4096\n"
      "s_add_u32 s54, s54, 4096\n"
      "s_sub_u32 s55, s55, 1\n"
      "s_cmp_lg_u32 s55, 0\n"
      "s_cbranch_scc1 136b\n"
      "s_waitcnt vmcnt(0)\n"
      "s_mov_b64 exec, 1\n"
      "ds_add_u32 v20, v21 offset:12\n"
      "s_mov_b64 exec, -1\n"
      "s_mov_b32 s91, 0x100000\n"
      "s_branch 137f\n"
      "138:\n"
      "s_sleep 3\n"
      "137:\n"
      "ds_read_b32 v31, v20 offset:16\n"
      "s_sub_u32 s91, s91, 1\n"
      "s_cmp_eq_u32 s91, 0\n"
      "s_cbranch_scc1 139f\n"
      "s_waitcnt lgkmcnt(0)\n"
      "v_readfirstlane_b32 s90, v31\n"
      "s_cmp_lt_u32 s90, 1\n"
      "s_cbranch_scc1 138b\n"
      "139:\n"
      "s_setprio 2\n"
      "s_waitcnt vmcnt(18)\n"
      "v_mfma_f32_16x16x32_bf16 v[40:43], v[120:123], v[140:143], v[40:43]\n"
      "v_mfma_f32_16x16x32_bf16 v[60:63], v[120:123], v[144:147], v[60:63]\n"
      "v_mfma_f32_16x16x32_bf16 v[80:83], v[120:123], v[148:151], v[80:83]\n"
      "v_mfma_f32_16x16x32_bf16 v[100:103], v[120:123], v[152:155], v[100:103]\n"
      "v_mfma_f32_16x16x32_bf16 v[44:47], v[124:127], v[140:143], v[44:47]\n"
      "v_mfma_f32_16x16x32_bf16 v[64:67], v[124:127], v[144:147], v[64:67]\n"
      "v_mfma_f32_16x16x32_bf16 v[84:87], v[124:127], v[148:151], v[84:87]\n"
      "v_mfma_f32_16x16x32_bf16 v[104:107], v[124:127], v[152:155], v[104:107]\n"
      "v_mfma_f32_16x16x32_bf16 v[48:51], v[128:131], v[140:143], v[48:51]\n"
      "v_mfma_f32_16x16x32_bf16 v[68:71], v[128:131], v[144:147], v[68:71]\n"
      "v_mfma_f32_16x16x32_bf16 v[88:91], v[128:131], v[148:151], v[88:91]\n"
      "v_mfma_f32_16x16x32_bf16 v[108:111], v[128:131], v[152:155], v[108:111]\n"
      "v_mfma_f32_16x16x32_bf16 v[52:55], v[132:135], v[140:143], v[52:55]\n"
      "v_mfma_f32_16x16x32_bf16 v[72:75], v[132:135], v[144:147], v[72:75]\n"
      "v_mfma_f32_16x16x32_bf16 v[92:95], v[132:135], v[148:151], v[92:95]\n"
      "v_mfma_f32_16x16x32_bf16 v[112:115], v[132:135], v[152:155], v[112:115]\n"
      "v_mfma_f32_16x16x32_bf16 v[56:59], v[136:139], v[140:143], v[56:59]\n"
      "v_mfma_f32_16x16x32_bf16 v[76:79], v[136:139], v[144:147], v[76:79]\n"
      "v_mfma_f32_16x16x32_bf16 v[96:99], v[136:139], v[148:151], v[96:99]\n"
      "v_mfma_f32_16x16x32_bf16 v[116:119], v[136:139], v[152:155], v[116:119]\n"
      "s_waitcnt vmcnt(9)\n"
      "v_mfma_f32_16x16x32_bf16 v[40:43], v[156:159], v[176:179], v[40:43]\n"
      "v_mfma_f32_16x16x32_bf16 v[60:63], v[156:159], v[180:183], v[60:63]\n"
      "v_mfma_f32_16x16x32_bf16 v[80:83], v[156:159], v[184:187], v[80:83]\n"
      "v_mfma_f32_16x16x32_bf16 v[100:103], v[156:159], v[188:191], v[100:103]\n"
      "v_mfma_f32_16x16x32_bf16 v[44:47], v[160:163], v[176:179], v[44:47]\n"
      "v_mfma_f32_16x16x32_bf16 v[64:67], v[160:163], v[180:183], v[64:67]\n"
      "v_mfma_f32_16x16x32_bf16 v[84:87], v[160:163], v[184:187], v[84:87]\n"
      "v_mfma_f32_16x16x32_bf16 v[104:107], v[160:163], v[188:191], v[104:107]\n"
      "v_mfma_f32_16x16x32_bf16 v[48:51], v[164:167], v[176:179], v[48:51]\n"
      "v_mfma_f32_16x16x32_bf16 v[68:71], v[164:167], v[180:183], v[68:71]\n"
      "v_mfma_f32_16x16x32_bf16 v[88:91], v[164:167], v[184:187], v[88:91]\n"
      "v_mfma_f32_16x16x32_bf16 v[108:111], v[164:167], v[188:191], v[108:111]\n"
      "v_mfma_f32_16x16x32_bf16 v[52:55], v[168:171], v[176:179], v[52:55]\n"
      "v_mfma_f32_16x16x32_bf16 v[72:75], v[168:171], v[180:183], v[72:75]\n"
      "v_mfma_f32_16x16x32_bf16 v[92:95], v[168:171], v[184:187], v[92:95]\n"
      "v_mfma_f32_16x16x32_bf16 v[112:115], v[168:171], v[188:191], v[112:115]\n"
      "v_mfma_f32_16x16x32_bf16 v[56:59], v[172:175], v[176:179], v[56:59]\n"
      "v_mfma_f32_16x16x32_bf16 v[76:79], v[172:175], v[180:183], v[76:79]\n"
      "v_mfma_f32_16x16x32_bf16 v[96:99], v[172:175], v[184:187], v[96:99]\n"
      "v_mfma_f32_16x16x32_bf16 v[116:119], v[172:175], v[188:191], v[116:119]\n"
      "s_waitcnt vmcnt(0)\n"
      "v_mfma_f32_16x16x32_bf16 v[40:43], v[192:195], v[212:215], v[40:43]\n"
      "v_mfma_f32_16x16x32_bf16 v[60:63], v[192:195], v[216:219], v[60:63]\n"
      "v_mfma_f32_16x16x32_bf16 v[80:83], v[192:195], v[220:223], v[80:83]\n"
      "v_mfma_f32_16x16x32_bf16 v[100:103], v[192:195], v[224:227], v[100:103]\n"
      "v_mfma_f32_16x16x32_bf16 v[44:47], v[196:199], v[212:215], v[44:47]\n"
      "v_mfma_f32_16x16x32_bf16 v[64:67], v[196:199], v[216:219], v[64:67]\n"
      "v_mfma_f32_16x16x32_bf16 v[84:87], v[196:199], v[220:223], v[84:87]\n"
      "v_mfma_f32_16x16x32_bf16 v[104:107], v[196:199], v[224:227], v[104:107]\n"
      "v_mfma_f32_16x16x32_bf16 v[48:51], v[200:203], v[212:215], v[48:51]\n"
      "v_mfma_f32_16x16x32_bf16 v[68:71], v[200:203], v[216:219], v[68:71]\n"
      "v_mfma_f32_16x16x32_bf16 v[88:91], v[200:203], v[220:223], v[88:91]\n"
      "v_mfma_f32_16x16x32_bf16 v[108:111], v[200:203], v[224:227], v[108:111]\n"
      "v_mfma_f32_16x16x32_bf16 v[52:55], v[204:207], v[212:215], v[52:55]\n"
      "v_mfma_f32_16x16x32_bf16 v[72:75], v[204:207], v[216:219], v[72:75]\n"
      "v_mfma_f32_16x16x32_bf16 v[92:95], v[204:207], v[220:223], v[92:95]\n"
      "v_mfma_f32_16x16x32_bf16 v[112:115], v[204:207], v[224:227], v[112:115]\n"
      "v_mfma_f32_16x16x32_bf16 v[56:59], v[208:211], v[212:215], v[56:59]\n"
      "v_mfma_f32_16x16x32_bf16 v[76:79], v[208:211], v[216:219], v[76:79]\n"
      "v_mfma_f32_16x16x32_bf16 v[96:99], v[208:211], v[220:223], v[96:99]\n"
      "v_mfma_f32_16x16x32_bf16 v[116:119], v[208:211], v[224:227], v[116:119]\n"
      "s_setprio 2\n"
      "s_mov_b32 s91, 0x100000\n"
      "s_branch 140f\n"
      "141:\n"
      "s_sleep 1\n"
      "140:\n"
      "ds_read_b32 v31, v20 offset:120\n"
      "s_sub_u32 s91, s91, 1\n"
      "s_cmp_eq_u32 s91, 0\n"
      "s_cbranch_scc1 142f\n"
      "s_waitcnt lgkmcnt(0)\n"
      "v_readfirstlane_b32 s90, v31\n"
      "s_cmp_lt_u32 s90, 4\n"
      "s_cbranch_scc1 141b\n"
      "142:\n"
      "ds_write_b128 v14, v[80:83] offset:0\n"
      "ds_write_b128 v14, v[84:87] offset:1024\n"
      "ds_write_b128 v14, v[88:91] offset:2048\n"
      "ds_write_b128 v14, v[92:95] offset:3072\n"
      "ds_write_b128 v14, v[96:99] offset:4096\n"
      "ds_write_b128 v14, v[100:103] offset:5120\n"
      "ds_write_b128 v14, v[104:107] offset:6144\n"
      "ds_write_b128 v14, v[108:111] offset:7168\n"
      "ds_write_b128 v14, v[112:115] offset:8192\n"
      "ds_write_b128 v14, v[116:119] offset:9216\n"
      "s_waitcnt lgkmcnt(0)\n"
      "ds_write_b32 v17, v21\n"
      "s_mov_b32 s91, 0x100000\n"
      "144:\n"
      "ds_read_b32 v31, v18 offset:0\n"
      "s_sub_u32 s91, s91, 1\n"
      "s_cmp_eq_u32 s91, 0\n"
      "s_cbranch_scc1 145f\n"
      "s_waitcnt lgkmcnt(0)\n"
      "v_readfirstlane_b32 s90, v31\n"
      "s_cmp_lt_u32 s90, 1\n"
      "s_cbranch_scc1 144b\n"
      "145:\n"
      "ds_read_b128 v[160:163], v15 offset:5120\n"
      "ds_read_b128 v[164:167], v15 offset:6144\n"
      "ds_read_b128 v[168:171], v15 offset:7168\n"
      "ds_read_b128 v[172:175], v15 offset:8192\n"
      "ds_read_b128 v[176:179], v15 offset:9216\n"
      "ds_read_b128 v[140:143], v15 offset:0\n"
      "ds_read_b128 v[144:147], v15 offset:1024\n"
      "ds_read_b128 v[148:151], v15 offset:2048\n"
      "ds_read_b128 v[152:155], v15 offset:3072\n"
      "ds_read_b128 v[156:159], v15 offset:4096\n"
      "s_waitcnt lgkmcnt(5)\n"
      "v_add_f32 v160, v60, v160\n"
      "v_add_f32 v161, v61, v161\n"
      "v_add_f32 v162, v62, v162\n"
      "v_add_f32 v163, v63, v163\n"
      "v_add_f32 v164, v64, v164\n"
      "v_add_f32 v165, v65, v165\n"
      "v_add_f32 v166, v66, v166\n"
      "v_add_f32 v167, v67, v167\n"
      "v_add_f32 v168, v68, v168\n"
      "v_add_f32 v169, v69, v169\n"
      "v_add_f32 v170, v70, v170\n"
      "v_add_f32 v171, v71, v171\n"
      "v_add_f32 v172, v72, v172\n"
      "v_add_f32 v173, v73, v173\n"
      "v_add_f32 v174, v74, v174\n"
      "v_add_f32 v175, v75, v175\n"
      "v_add_f32 v176, v76, v176\n"
      "v_add_f32 v177, v77, v177\n"
      "v_add_f32 v178, v78, v178\n"
      "v_add_f32 v179, v79, v179\n"
      "s_waitcnt lgkmcnt(0)\n"
      "ds_write_b128 v15, v[160:163] offset:0\n"
      "ds_write_b128 v15, v[164:167] offset:1024\n"
      "ds_write_b128 v15, v[168:171] offset:2048\n"
      "ds_write_b128 v15, v[172:175] offset:3072\n"
      "ds_write_b128 v15, v[176:179] offset:4096\n"
      "s_waitcnt lgkmcnt(0)\n"
      "ds_write_b32 v17, v21 offset:16\n"
      "v_add_f32 v40, v40, v140\n"
      "v_add_f32 v41, v41, v141\n"
      "v_add_f32 v42, v42, v142\n"
      "v_add_f32 v43, v43, v143\n"
      "v_add_f32 v44, v44, v144\n"
      "v_add_f32 v45, v45, v145\n"
      "v_add_f32 v46, v46, v146\n"
      "v_add_f32 v47, v47, v147\n"
      "v_add_f32 v48, v48, v148\n"
      "v_add_f32 v49, v49, v149\n"
      "v_add_f32 v50, v50, v150\n"
      "v_add_f32 v51, v51, v151\n"
      "v_add_f32 v52, v52, v152\n"
      "v_add_f32 v53, v53, v153\n"
      "v_add_f32 v54, v54, v154\n"
      "v_add_f32 v55, v55, v155\n"
      "v_add_f32 v56, v56, v156\n"
      "v_add_f32 v57, v57, v157\n"
      "v_add_f32 v58, v58, v158\n"
      "v_add_f32 v59, v59, v159\n"
      "s_mov_b32 s91, 0x100000\n"
      "147:\n"
      "ds_read_b32 v31, v19 offset:0\n"
      "s_sub_u32 s91, s91, 1\n"
      "s_cmp_eq_u32 s91, 0\n"
      "s_cbranch_scc1 148f\n"
      "s_waitcnt lgkmcnt(0)\n"
      "v_readfirstlane_b32 s90, v31\n"
      "s_cmp_lt_u32 s90, 1\n"
      "s_cbranch_scc1 147b\n"
      "148:\n"
      "ds_read_b128 v[220:223], v16 offset:0\n"
      "ds_read_b128 v[224:227], v16 offset:1024\n"
      "ds_read_b128 v[228:231], v16 offset:2048\n"
      "ds_read_b128 v[232:235], v16 offset:3072\n"
      "ds_read_b128 v[236:239], v16 offset:4096\n"
      "s_waitcnt lgkmcnt(0)\n"
      "v_add_f32 v40, v40, v220\n"
      "v_add_f32 v41, v41, v221\n"
      "v_add_f32 v42, v42, v222\n"
      "v_add_f32 v43, v43, v223\n"
      "v_add_f32 v44, v44, v224\n"
      "v_add_f32 v45, v45, v225\n"
      "v_add_f32 v46, v46, v226\n"
      "v_add_f32 v47, v47, v227\n"
      "v_add_f32 v48, v48, v228\n"
      "v_add_f32 v49, v49, v229\n"
      "v_add_f32 v50, v50, v230\n"
      "v_add_f32 v51, v51, v231\n"
      "v_add_f32 v52, v52, v232\n"
      "v_add_f32 v53, v53, v233\n"
      "v_add_f32 v54, v54, v234\n"
      "v_add_f32 v55, v55, v235\n"
      "v_add_f32 v56, v56, v236\n"
      "v_add_f32 v57, v57, v237\n"
      "v_add_f32 v58, v58, v238\n"
      "v_add_f32 v59, v59, v239\n"
      "ds_write_b128 v16, v[40:43] offset:0\n"
      "ds_write_b128 v16, v[44:47] offset:1024\n"
      "ds_write_b128 v16, v[48:51] offset:2048\n"
      "ds_write_b128 v16, v[52:55] offset:3072\n"
      "ds_write_b128 v16, v[56:59] offset:4096\n"
      "s_waitcnt lgkmcnt(0)\n"
      "ds_write_b32 v17, v21 offset:32\n"
      "s_waitcnt lgkmcnt(0)\n"
          : "+{v[48:79]}"(C0), "+{v[80:111]}"(C1), "+{v[112:143]}"(C2), "+{v[144:175]}"(C3), GPAR, "+{s[48:63]}"(SA)
          : "{s[64:71]}"(SB), "{s[72:75]}"(SC), "{s[76:79]}"(SW), "{s[80:83]}"(SH), "{s[84:87]}"(SK), "{s[88:89]}"(x)
          : "memory", "scc", "vcc", "s90", "s91", "s92", "s93", "s94", "s95", "v176", "v177", "v178", "v179", "v180", "v181", "v182", "v183", "v184", "v185", "v186", "v187", "v188", "v189", "v190", "v191", "v192", "v193", "v194", "v195", "v196", "v197", "v198", "v199", "v200", "v201", "v202", "v203", "v204", "v205", "v206", "v207", "v208", "v209", "v210", "v211", "v212", "v213", "v214", "v215", "v216", "v217", "v218", "v219", "v220", "v221", "v222", "v223", "v224", "v225", "v226", "v227", "v228", "v229", "v230", "v231", "v232", "v233", "v234", "v235", "v236", "v237", "v238", "v239", "v240", "v241", "v242", "v243", "v244", "v245", "v246", "v247", "v248", "v249", "v250", "v251", "v252", "v253", "v254", "v255");
    }
    asm volatile("s_endpgm" : "={v[192:223]}"(xa), "={v[224:231]}"(xa2)::"memory");
    asm volatile("" : "=s"(w), "=s"(bid), "=s"(ra), "=s"(x), "=s"(wk), "=s"(wv), "=s"(wq), "=s"(wo), "=s"(wg), "=s"(wu), "=s"(wd), "=s"(out), "=s"(ws));
  }
  {
    IDX_DEFS;
    LANE_DEFS;
    *((LAS u32*)(lds + L_MISC) + lane) = lane == C_RDY + 3 ? 4u : 0u;
    u32 gv;
    asm volatile("global_load_dword %0, %1, %2 sc1" : "=v"(gv) : "v"(0), "s"(ws + WS_G));
    {
      const int n = loc * 7 + (lane & 3) + 4 * (w & 1);
      touch((w & 2 ? wv : wk) + (n < 200 ? n : 199) * 128, junk);
    }
    DMA_SETUP;
    const int ta = w & 1, kb = w >> 1;
    const u32 sp = ldsb + L_SPARE, mi = ldsb + L_MISC;
    u32x16 P, P2, S;
    u32x8 T;
    f32x16 O0, O1, O2, O3;
    {
      const bool bval = (lane & (kind ? 12 : 48)) == 0;
      const u32 bo = 1024 + (((lane >> (2 * kind)) & 12) + trp) * 16;
      P[0] = od_[0]; P[1] = od_[1]; P[2] = od_[2]; P[3] = oq;
      P[4] = ldsb + L_AR + w * AR_SZ + (4 * gq + trq) * 40 + trp * 8;
      P[5] = ldsb + L_XN + (16 * tq + i16) * XS + gq * 16 + w * 320;
      P[6] = sp + w * 1280 + vo; P[7] = bval ? sp + w * 1280 + bo : junk;
      P[8] = mi + C_RDY * 4; P[9] = XN_DST; P[11] = vo; P[12] = sp + bo;
      const int kt = 2 * g1 + (tq >> 1);
      const u64 rec = (u64)(ws + WS_KV + (u32)((((kt >> 3) & 3) * 16 + (kt >> 5) * 8 + (kt & 7)) * REC)), fb = (u64)(ws + WS_KVFLAG);
      const u64 ob = (u64)(ws + WS_ACT + (u32)(g1 * ACT_G + 48 * 4096)), fl = (u64)(ws + WS_OFLAG + (u32)((g1 * 8 + j1) * 64 + w * 4));
      const u64 m0 = kind ? 0x000f000f000f000full : 0xffffull, m1 = kind ? 0x003f003f003f003full : 0xffffull;
      S = u32x16{(u32)rec, (u32)(rec >> 32), (u32)fb, (u32)(fb >> 32), (u32)ob, (u32)(ob >> 32), (u32)fl, (u32)(fl >> 32),
                 (u32)m0, (u32)(m0 >> 32), (u32)m1, (u32)(m1 >> 32), dw, dq, qhi, (u32)kind};
      T = u32x8{(k0 + 1) * (16 * 3072), (k0 + 1) * (16 * 1280), ringb + w * 1024, (u32)w * 1024, (u32)kb, 0u, 0u, 0u};
    }
    P[10] = vo;
    asm volatile("s_waitcnt lgkmcnt(0)\n\ts_barrier" : "+{v[8:23]}"(P)::"memory");
    {
      const int rp = (l31 & 3) + 4 * (l31 >> 3), dd = (rp & 7) + ((rp >> 3) == kb ? 0 : 6), vd = 10 * ((l31 >> 2) & 1) + dd;
      const int lim = (rp >> 3) == kb ? 6 : 4;
      const u32 vz = 41 - ((rp & 7) == 6), vsl = vz + ((rp & 7) < lim) * (h * 20 + vd - vz);
      const u32 kvb = ldsb + kb * 8 * REC;
      const u32 ov = (2 * ta + (l31 >> 4)) * 1024 + h * 256 + i16 * 16, c0 = 5 * j1 + 3 * kb;
      P[13] = (u32)((128 * (loc < 12 ? loc : loc - 12) + 32 * w + (lane >> 1)) * 1280 + xcd * 160 + (lane & 1) * 156);
      P[14] = (kind * 128 + 4 * g1 + tq) * 8;
      P[15] = kind ? 0x3f800000u : __builtin_bit_cast(u32, 0.22360679774997896f * 1.4426950408889634f);
      const u32 c0_ = kind ? 1280 + (tq & 1) * 672 : (tq & 1) * 256, c1_ = kind ? 8 : 512, c2_ = kind ? 320 : 8;
      P2[0] = c0_ + i16 * 16 + (gq & 1) * c1_ + h * c2_;
      const u32 km = 0u - kind;
      P2[1] = ((P2[0] + 256 + (i16 > 3) * (320 - h * 320)) & km) + ((1024 + (tq & 1) * 128 + i16 * 8) & ~km);
      P2[2] = i16 == 4 ? 0x3f803f80u : 0u;
      P2[3] = kvb + vo; P2[4] = kvb + 1024 + l31 * 8; P2[5] = kvb + 1280 + vsl * 16;
      P2[6] = ldsb + L_QX + (32 * ta + l31) * 32 + h * 16; P2[7] = ldsb + L_QX + 8192 + (32 * ta + l31) * 8;
      P2[8] = ldsb + L_PAIR + w * 8192 + vo; P2[9] = ldsb + L_PAIR + (w ^ 2) * 8192 + vo;
      P2[10] = mi + (C_PAIR + w) * 4; P2[11] = mi + (C_PAIR + (w ^ 2)) * 4;
      P2[12] = ov + (c0 >> 1) * 4096 + (c0 & 1) * 512; P2[13] = ov + ((c0 + 1) >> 1) * 4096 + ((c0 + 1) & 1) * 512; P2[14] = P2[12] + 4096;
      P2[15] = sp + vo;
    }
    asm volatile(
      "s_setprio 2\n"
      "v_mov_b32 v253, 1\n"
      "v_mov_b32 v247, 0x3f803f80\n"
      "v_mov_b64 v[64:65], 0\n"
      "v_mov_b64 v[66:67], 0\n"
      "v_mov_b64 v[68:69], 0\n"
      "v_mov_b64 v[70:71], 0\n"
      "v_mov_b64 v[72:73], 0\n"
      "v_mov_b64 v[74:75], 0\n"
      "v_mov_b64 v[76:77], 0\n"
      "v_mov_b64 v[78:79], 0\n"
      "v_mov_b64 v[80:81], 0\n"
      "v_mov_b64 v[82:83], 0\n"
      "v_mov_b64 v[84:85], 0\n"
      "v_mov_b64 v[86:87], 0\n"
      "v_mov_b64 v[88:89], 0\n"
      "v_mov_b64 v[90:91], 0\n"
      "v_mov_b64 v[92:93], 0\n"
      "v_mov_b64 v[94:95], 0\n"
      "v_mov_b64 v[96:97], 0\n"
      "v_mov_b64 v[98:99], 0\n"
      "v_mov_b64 v[100:101], 0\n"
      "v_mov_b64 v[102:103], 0\n"
      "v_mov_b64 v[104:105], 0\n"
      "v_mov_b64 v[106:107], 0\n"
      "v_mov_b64 v[108:109], 0\n"
      "v_mov_b64 v[110:111], 0\n"
      "v_mov_b64 v[112:113], 0\n"
      "v_mov_b64 v[114:115], 0\n"
      "v_mov_b64 v[116:117], 0\n"
      "v_mov_b64 v[118:119], 0\n"
      "v_mov_b64 v[120:121], 0\n"
      "v_mov_b64 v[122:123], 0\n"
      "v_mov_b64 v[124:125], 0\n"
      "v_mov_b64 v[126:127], 0\n"
      "v_mov_b64 v[176:177], 0\n"
      "v_mov_b64 v[178:179], 0\n"
      "v_mov_b64 v[180:181], 0\n"
      "v_mov_b64 v[182:183], 0\n"
      "v_mov_b64 v[184:185], 0\n"
      "v_mov_b64 v[186:187], 0\n"
      "v_mov_b64 v[188:189], 0\n"
      "v_mov_b64 v[190:191], 0\n"
      "v_mov_b64 v[238:239], 0\n"
      "v_mov_b64 v[240:241], 0\n"
      "v_mov_b64 v[242:243], 0\n"
      "v_mov_b64 v[244:245], 0\n"
      "v_mov_b64 v[56:57], 0\n"
      "v_mov_b64 v[58:59], 0\n"
      "v_mov_b64 v[60:61], 0\n"
      "v_mov_b64 v[62:63], 0\n"
      "s_mov_b32 s94, 0xffff0000\n"
      "s_waitcnt vmcnt(10)\n"
      "v_dot2_f32_bf16 v128, v192, v247, 0\n"
      "v_dot2_f32_bf16 v132, v192, v192, 0\n"
      "v_dot2_f32_bf16 v129, v193, v247, 0\n"
      "v_dot2_f32_bf16 v133, v193, v193, 0\n"
      "v_dot2_f32_bf16 v130, v194, v247, 0\n"
      "v_dot2_f32_bf16 v134, v194, v194, 0\n"
      "v_dot2_f32_bf16 v131, v195, v247, 0\n"
      "v_dot2_f32_bf16 v135, v195, v195, 0\n"
      "s_nop 1\n"
      "v_dot2c_f32_bf16 v128, v196, v247\n"
      "v_dot2c_f32_bf16 v132, v196, v196\n"
      "v_dot2c_f32_bf16 v129, v197, v247\n"
      "v_dot2c_f32_bf16 v133, v197, v197\n"
      "v_dot2c_f32_bf16 v130, v198, v247\n"
      "v_dot2c_f32_bf16 v134, v198, v198\n"
      "v_dot2c_f32_bf16 v131, v199, v247\n"
      "v_dot2c_f32_bf16 v135, v199, v199\n"
      "s_waitcnt vmcnt(8)\n"
      "v_dot2c_f32_bf16 v128, v200, v247\n"
      "v_dot2c_f32_bf16 v132, v200, v200\n"
      "v_dot2c_f32_bf16 v129, v201, v247\n"
      "v_dot2c_f32_bf16 v133, v201, v201\n"
      "v_dot2c_f32_bf16 v130, v202, v247\n"
      "v_dot2c_f32_bf16 v134, v202, v202\n"
      "v_dot2c_f32_bf16 v131, v203, v247\n"
      "v_dot2c_f32_bf16 v135, v203, v203\n"
      "v_dot2c_f32_bf16 v128, v204, v247\n"
      "v_dot2c_f32_bf16 v132, v204, v204\n"
      "v_dot2c_f32_bf16 v129, v205, v247\n"
      "v_dot2c_f32_bf16 v133, v205, v205\n"
      "v_dot2c_f32_bf16 v130, v206, v247\n"
      "v_dot2c_f32_bf16 v134, v206, v206\n"
      "v_dot2c_f32_bf16 v131, v207, v247\n"
      "v_dot2c_f32_bf16 v135, v207, v207\n"
      "s_waitcnt vmcnt(6)\n"
      "v_dot2c_f32_bf16 v128, v208, v247\n"
      "v_dot2c_f32_bf16 v132, v208, v208\n"
      "v_dot2c_f32_bf16 v129, v209, v247\n"
      "v_dot2c_f32_bf16 v133, v209, v209\n"
      "v_dot2c_f32_bf16 v130, v210, v247\n"
      "v_dot2c_f32_bf16 v134, v210, v210\n"
      "v_dot2c_f32_bf16 v131, v211, v247\n"
      "v_dot2c_f32_bf16 v135, v211, v211\n"
      "v_dot2c_f32_bf16 v128, v212, v247\n"
      "v_dot2c_f32_bf16 v132, v212, v212\n"
      "v_dot2c_f32_bf16 v129, v213, v247\n"
      "v_dot2c_f32_bf16 v133, v213, v213\n"
      "v_dot2c_f32_bf16 v130, v214, v247\n"
      "v_dot2c_f32_bf16 v134, v214, v214\n"
      "v_dot2c_f32_bf16 v131, v215, v247\n"
      "v_dot2c_f32_bf16 v135, v215, v215\n"
      "s_waitcnt vmcnt(4)\n"
      "v_dot2c_f32_bf16 v128, v216, v247\n"
      "v_dot2c_f32_bf16 v132, v216, v216\n"
      "v_dot2c_f32_bf16 v129, v217, v247\n"
      "v_dot2c_f32_bf16 v133, v217, v217\n"
      "v_dot2c_f32_bf16 v130, v218, v247\n"
      "v_dot2c_f32_bf16 v134, v218, v218\n"
      "v_dot2c_f32_bf16 v131, v219, v247\n"
      "v_dot2c_f32_bf16 v135, v219, v219\n"
      "v_dot2c_f32_bf16 v128, v220, v247\n"
      "v_dot2c_f32_bf16 v132, v220, v220\n"
      "v_dot2c_f32_bf16 v129, v221, v247\n"
      "v_dot2c_f32_bf16 v133, v221, v221\n"
      "v_dot2c_f32_bf16 v130, v222, v247\n"
      "v_dot2c_f32_bf16 v134, v222, v222\n"
      "v_dot2c_f32_bf16 v131, v223, v247\n"
      "v_dot2c_f32_bf16 v135, v223, v223\n"
      "s_waitcnt vmcnt(2)\n"
      "v_dot2c_f32_bf16 v128, v224, v247\n"
      "v_dot2c_f32_bf16 v132, v224, v224\n"
      "v_dot2c_f32_bf16 v129, v225, v247\n"
      "v_dot2c_f32_bf16 v133, v225, v225\n"
      "v_dot2c_f32_bf16 v130, v226, v247\n"
      "v_dot2c_f32_bf16 v134, v226, v226\n"
      "v_dot2c_f32_bf16 v131, v227, v247\n"
      "v_dot2c_f32_bf16 v135, v227, v227\n"
      "v_dot2c_f32_bf16 v128, v228, v247\n"
      "v_dot2c_f32_bf16 v132, v228, v228\n"
      "v_dot2c_f32_bf16 v129, v229, v247\n"
      "v_dot2c_f32_bf16 v133, v229, v229\n"
      "v_dot2c_f32_bf16 v130, v230, v247\n"
      "v_dot2c_f32_bf16 v134, v230, v230\n"
      "v_dot2c_f32_bf16 v131, v231, v247\n"
      "v_dot2c_f32_bf16 v135, v231, v231\n"
      "v_lshlrev_b32 v144, 16, v192\n"
      "v_and_b32 v145, s94, v192\n"
      "v_lshlrev_b32 v146, 16, v193\n"
      "v_and_b32 v147, s94, v193\n"
      "v_add_f32 v128, v128, v129\n"
      "v_add_f32 v130, v130, v131\n"
      "v_add_f32 v132, v132, v133\n"
      "v_add_f32 v134, v134, v135\n"
      "v_lshlrev_b32 v148, 16, v194\n"
      "v_and_b32 v149, s94, v194\n"
      "v_add_f32 v128, v128, v130\n"
      "v_add_f32 v132, v132, v134\n"
      "v_lshlrev_b32 v150, 16, v195\n"
      "v_and_b32 v151, s94, v195\n"
      "v_add_f32_dpp v128, v128, v128 quad_perm:[1,0,3,2] row_mask:0xf bank_mask:0xf\n"
      "v_add_f32_dpp v132, v132, v132 quad_perm:[1,0,3,2] row_mask:0xf bank_mask:0xf\n"
      "v_lshlrev_b32 v152, 16, v196\n"
      "v_and_b32 v153, s94, v196\n"
      "v_add_f32_dpp v128, v128, v128 quad_perm:[2,3,0,1] row_mask:0xf bank_mask:0xf\n"
      "v_add_f32_dpp v132, v132, v132 quad_perm:[2,3,0,1] row_mask:0xf bank_mask:0xf\n"
      "v_lshlrev_b32 v154, 16, v197\n"
      "v_and_b32 v155, s94, v197\n"
      "v_add_f32_dpp v128, v128, v128 row_half_mirror row_mask:0xf bank_mask:0xf\n"
      "v_add_f32_dpp v132, v132, v132 row_half_mirror row_mask:0xf bank_mask:0xf\n"
      "v_lshlrev_b32 v156, 16, v198\n"
      "v_and_b32 v157, s94, v198\n"
      "v_mul_f32 v128, 0x3acccccd, v128\n"
      "v_mul_f32 v132, 0x3acccccd, v132\n"
      "v_lshlrev_b32 v158, 16, v199\n"
      "v_and_b32 v159, s94, v199\n"
      "v_fma_f32 v132, -v128, v128, v132\n"
      "v_lshlrev_b32 v160, 16, v200\n"
      "v_and_b32 v161, s94, v200\n"
      "v_max_f32 v132, 0, v132\n"
      "v_lshlrev_b32 v162, 16, v201\n"
      "v_and_b32 v163, s94, v201\n"
      "v_cvt_pk_bf16_f32 v132, v128, v132\n"
      "v_lshlrev_b32 v164, 16, v202\n"
      "v_and_b32 v165, s94, v202\n"
      "v_lshlrev_b32 v128, 16, v132\n"
      "v_and_b32 v132, s94, v132\n"
      "v_lshlrev_b32 v166, 16, v203\n"
      "v_and_b32 v167, s94, v203\n"
      "v_add_f32 v132, 0x3727c5ac, v132\n"
      "v_lshlrev_b32 v168, 16, v204\n"
      "v_and_b32 v169, s94, v204\n"
      "v_cvt_pk_bf16_f32 v132, 0, v132\n"
      "v_lshlrev_b32 v170, 16, v205\n"
      "v_and_b32 v171, s94, v205\n"
      "v_sqrt_f32 v132, v132\n"
      "v_lshlrev_b32 v172, 16, v206\n"
      "v_cvt_pk_bf16_f32 v132, 0, v132\n"
      "v_and_b32 v173, s94, v206\n"
      "v_rcp_f32 v136, v132\n"
      "v_lshlrev_b32 v174, 16, v207\n"
      "v_mul_f32_e64 v138, -v128, v136\n"
      "v_and_b32 v175, s94, v207\n"
      "v_pk_fma_f32 v[144:145], v[144:145], v[136:137], v[138:139] op_sel_hi:[1,0,0]\n"
      "v_pk_fma_f32 v[146:147], v[146:147], v[136:137], v[138:139] op_sel_hi:[1,0,0]\n"
      "v_pk_fma_f32 v[148:149], v[148:149], v[136:137], v[138:139] op_sel_hi:[1,0,0]\n"
      "v_pk_fma_f32 v[150:151], v[150:151], v[136:137], v[138:139] op_sel_hi:[1,0,0]\n"
      "v_pk_fma_f32 v[152:153], v[152:153], v[136:137], v[138:139] op_sel_hi:[1,0,0]\n"
      "v_pk_fma_f32 v[154:155], v[154:155], v[136:137], v[138:139] op_sel_hi:[1,0,0]\n"
      "v_pk_fma_f32 v[156:157], v[156:157], v[136:137], v[138:139] op_sel_hi:[1,0,0]\n"
      "v_pk_fma_f32 v[158:159], v[158:159], v[136:137], v[138:139] op_sel_hi:[1,0,0]\n"
      "v_cvt_pk_bf16_f32 v192, v144, v145\n"
      "v_cvt_pk_bf16_f32 v193, v146, v147\n"
      "v_cvt_pk_bf16_f32 v194, v148, v149\n"
      "v_cvt_pk_bf16_f32 v195, v150, v151\n"
      "v_cvt_pk_bf16_f32 v196, v152, v153\n"
      "v_cvt_pk_bf16_f32 v197, v154, v155\n"
      "v_cvt_pk_bf16_f32 v198, v156, v157\n"
      "v_cvt_pk_bf16_f32 v199, v158, v159\n"
      "ds_write2_b64 v17, v[192:193], v[194:195] offset0:0 offset1:2\n"
      "ds_write2_b64 v17, v[196:197], v[198:199] offset0:16 offset1:18\n"
      "v_lshlrev_b32 v144, 16, v208\n"
      "v_and_b32 v145, s94, v208\n"
      "v_lshlrev_b32 v146, 16, v209\n"
      "v_and_b32 v147, s94, v209\n"
      "v_lshlrev_b32 v148, 16, v210\n"
      "v_and_b32 v149, s94, v210\n"
      "v_lshlrev_b32 v150, 16, v211\n"
      "v_and_b32 v151, s94, v211\n"
      "v_lshlrev_b32 v152, 16, v212\n"
      "v_and_b32 v153, s94, v212\n"
      "v_lshlrev_b32 v154, 16, v213\n"
      "v_and_b32 v155, s94, v213\n"
      "v_lshlrev_b32 v156, 16, v214\n"
      "v_and_b32 v157, s94, v214\n"
      "v_lshlrev_b32 v158, 16, v215\n"
      "v_and_b32 v159, s94, v215\n"
      "v_pk_fma_f32 v[160:161], v[160:161], v[136:137], v[138:139] op_sel_hi:[1,0,0]\n"
      "v_pk_fma_f32 v[162:163], v[162:163], v[136:137], v[138:139] op_sel_hi:[1,0,0]\n"
      "v_pk_fma_f32 v[164:165], v[164:165], v[136:137], v[138:139] op_sel_hi:[1,0,0]\n"
      "v_pk_fma_f32 v[166:167], v[166:167], v[136:137], v[138:139] op_sel_hi:[1,0,0]\n"
      "v_pk_fma_f32 v[168:169], v[168:169], v[136:137], v[138:139] op_sel_hi:[1,0,0]\n"
      "v_pk_fma_f32 v[170:171], v[170:171], v[136:137], v[138:139] op_sel_hi:[1,0,0]\n"
      "v_pk_fma_f32 v[172:173], v[172:173], v[136:137], v[138:139] op_sel_hi:[1,0,0]\n"
      "v_pk_fma_f32 v[174:175], v[174:175], v[136:137], v[138:139] op_sel_hi:[1,0,0]\n"
      "v_cvt_pk_bf16_f32 v200, v160, v161\n"
      "v_cvt_pk_bf16_f32 v201, v162, v163\n"
      "v_cvt_pk_bf16_f32 v202, v164, v165\n"
      "v_cvt_pk_bf16_f32 v203, v166, v167\n"
      "v_cvt_pk_bf16_f32 v204, v168, v169\n"
      "v_cvt_pk_bf16_f32 v205, v170, v171\n"
      "v_cvt_pk_bf16_f32 v206, v172, v173\n"
      "v_cvt_pk_bf16_f32 v207, v174, v175\n"
      "ds_write2_b64 v17, v[200:201], v[202:203] offset0:32 offset1:34\n"
      "ds_write2_b64 v17, v[204:205], v[206:207] offset0:48 offset1:50\n"
      "v_lshlrev_b32 v160, 16, v216\n"
      "v_and_b32 v161, s94, v216\n"
      "v_lshlrev_b32 v162, 16, v217\n"
      "v_and_b32 v163, s94, v217\n"
      "v_lshlrev_b32 v164, 16, v218\n"
      "v_and_b32 v165, s94, v218\n"
      "v_lshlrev_b32 v166, 16, v219\n"
      "v_and_b32 v167, s94, v219\n"
      "v_lshlrev_b32 v168, 16, v220\n"
      "v_and_b32 v169, s94, v220\n"
      "v_lshlrev_b32 v170, 16, v221\n"
      "v_and_b32 v171, s94, v221\n"
      "v_lshlrev_b32 v172, 16, v222\n"
      "v_and_b32 v173, s94, v222\n"
      "v_lshlrev_b32 v174, 16, v223\n"
      "v_and_b32 v175, s94, v223\n"
      "s_waitcnt vmcnt(1)\n"
      "s_add_u32 m0, s64, 14848\n"
      "s_nop 0\n"
      "buffer_load_dwordx4 v8, s[40:43], s68 offen lds\n"
      "buffer_load_dwordx4 v9, s[40:43], s68 offen offset:1024 lds\n"
      "buffer_load_dwordx4 v10, s[40:43], s68 offen offset:2048 lds\n"
      "s_cmp_eq_u32 s66, 7\n"
      "s_cbranch_scc1 107f\n"
      "s_add_u32 m0, s65, 14848\n"
      "s_mov_b32 exec_hi, s66\n"
      "buffer_load_dwordx4 v11, s[44:47], s69 offen lds\n"
      "s_mov_b32 exec_hi, -1\n"
      "107:\n"
      "s_add_u32 s68, s68, 49152\n"
      "s_add_u32 s69, s69, 20480\n"
      "s_cmp_lg_u32 s69, 819200\n"
      "s_cselect_b32 s68, s68, 0\n"
      "s_cselect_b32 s69, s69, 0\n"
      "v_pk_fma_f32 v[144:145], v[144:145], v[136:137], v[138:139] op_sel_hi:[1,0,0]\n"
      "v_pk_fma_f32 v[146:147], v[146:147], v[136:137], v[138:139] op_sel_hi:[1,0,0]\n"
      "v_pk_fma_f32 v[148:149], v[148:149], v[136:137], v[138:139] op_sel_hi:[1,0,0]\n"
      "v_pk_fma_f32 v[150:151], v[150:151], v[136:137], v[138:139] op_sel_hi:[1,0,0]\n"
      "v_pk_fma_f32 v[152:153], v[152:153], v[136:137], v[138:139] op_sel_hi:[1,0,0]\n"
      "v_pk_fma_f32 v[154:155], v[154:155], v[136:137], v[138:139] op_sel_hi:[1,0,0]\n"
      "v_pk_fma_f32 v[156:157], v[156:157], v[136:137], v[138:139] op_sel_hi:[1,0,0]\n"
      "v_pk_fma_f32 v[158:159], v[158:159], v[136:137], v[138:139] op_sel_hi:[1,0,0]\n"
      "v_cvt_pk_bf16_f32 v208, v144, v145\n"
      "v_cvt_pk_bf16_f32 v209, v146, v147\n"
      "v_cvt_pk_bf16_f32 v210, v148, v149\n"
      "v_cvt_pk_bf16_f32 v211, v150, v151\n"
      "v_cvt_pk_bf16_f32 v212, v152, v153\n"
      "v_cvt_pk_bf16_f32 v213, v154, v155\n"
      "v_cvt_pk_bf16_f32 v214, v156, v157\n"
      "v_cvt_pk_bf16_f32 v215, v158, v159\n"
      "ds_write2_b64 v17, v[208:209], v[210:211] offset0:64 offset1:66\n"
      "ds_write2_b64 v17, v[212:213], v[214:215] offset0:80 offset1:82\n"
      "v_lshlrev_b32 v144, 16, v224\n"
      "v_and_b32 v145, s94, v224\n"
      "v_lshlrev_b32 v146, 16, v225\n"
      "v_and_b32 v147, s94, v225\n"
      "v_lshlrev_b32 v148, 16, v226\n"
      "v_and_b32 v149, s94, v226\n"
      "v_lshlrev_b32 v150, 16, v227\n"
      "v_and_b32 v151, s94, v227\n"
      "v_lshlrev_b32 v152, 16, v228\n"
      "v_and_b32 v153, s94, v228\n"
      "v_lshlrev_b32 v154, 16, v229\n"
      "v_and_b32 v155, s94, v229\n"
      "v_lshlrev_b32 v156, 16, v230\n"
      "v_and_b32 v157, s94, v230\n"
      "v_lshlrev_b32 v158, 16, v231\n"
      "v_and_b32 v159, s94, v231\n"
      "v_pk_fma_f32 v[160:161], v[160:161], v[136:137], v[138:139] op_sel_hi:[1,0,0]\n"
      "v_pk_fma_f32 v[162:163], v[162:163], v[136:137], v[138:139] op_sel_hi:[1,0,0]\n"
      "v_pk_fma_f32 v[164:165], v[164:165], v[136:137], v[138:139] op_sel_hi:[1,0,0]\n"
      "v_pk_fma_f32 v[166:167], v[166:167], v[136:137], v[138:139] op_sel_hi:[1,0,0]\n"
      "v_pk_fma_f32 v[168:169], v[168:169], v[136:137], v[138:139] op_sel_hi:[1,0,0]\n"
      "v_pk_fma_f32 v[170:171], v[170:171], v[136:137], v[138:139] op_sel_hi:[1,0,0]\n"
      "v_pk_fma_f32 v[172:173], v[172:173], v[136:137], v[138:139] op_sel_hi:[1,0,0]\n"
      "v_pk_fma_f32 v[174:175], v[174:175], v[136:137], v[138:139] op_sel_hi:[1,0,0]\n"
      "v_cvt_pk_bf16_f32 v216, v160, v161\n"
      "v_cvt_pk_bf16_f32 v217, v162, v163\n"
      "v_cvt_pk_bf16_f32 v218, v164, v165\n"
      "v_cvt_pk_bf16_f32 v219, v166, v167\n"
      "v_cvt_pk_bf16_f32 v220, v168, v169\n"
      "v_cvt_pk_bf16_f32 v221, v170, v171\n"
      "v_cvt_pk_bf16_f32 v222, v172, v173\n"
      "v_cvt_pk_bf16_f32 v223, v174, v175\n"
      "ds_write2_b64 v17, v[216:217], v[218:219] offset0:96 offset1:98\n"
      "ds_write2_b64 v17, v[220:221], v[222:223] offset0:112 offset1:114\n"
      "v_pk_fma_f32 v[144:145], v[144:145], v[136:137], v[138:139] op_sel_hi:[1,0,0]\n"
      "v_pk_fma_f32 v[146:147], v[146:147], v[136:137], v[138:139] op_sel_hi:[1,0,0]\n"
      "v_pk_fma_f32 v[148:149], v[148:149], v[136:137], v[138:139] op_sel_hi:[1,0,0]\n"
      "v_pk_fma_f32 v[150:151], v[150:151], v[136:137], v[138:139] op_sel_hi:[1,0,0]\n"
      "v_pk_fma_f32 v[152:153], v[152:153], v[136:137], v[138:139] op_sel_hi:[1,0,0]\n"
      "v_pk_fma_f32 v[154:155], v[154:155], v[136:137], v[138:139] op_sel_hi:[1,0,0]\n"
      "v_pk_fma_f32 v[156:157], v[156:157], v[136:137], v[138:139] op_sel_hi:[1,0,0]\n"
      "v_pk_fma_f32 v[158:159], v[158:159], v[136:137], v[138:139] op_sel_hi:[1,0,0]\n"
      "v_cvt_pk_bf16_f32 v224, v144, v145\n"
      "v_cvt_pk_bf16_f32 v225, v146, v147\n"
      "v_cvt_pk_bf16_f32 v226, v148, v149\n"
      "v_cvt_pk_bf16_f32 v227, v150, v151\n"
      "v_cvt_pk_bf16_f32 v228, v152, v153\n"
      "v_cvt_pk_bf16_f32 v229, v154, v155\n"
      "v_cvt_pk_bf16_f32 v230, v156, v157\n"
      "v_cvt_pk_bf16_f32 v231, v158, v159\n"
      "ds_write2_b64 v17, v[224:225], v[226:227] offset0:128 offset1:130\n"
      "ds_write2_b64 v17, v[228:229], v[230:231] offset0:144 offset1:146\n"
      "s_waitcnt lgkmcnt(0)\n"
      "s_mov_b64 exec, 1\n"
      "ds_add_u32 v16, v253 offset:44\n"
      "s_mov_b64 exec, -1\n"
      "s_setprio 0\n"
      "s_add_u32 m0, s64, 29696\n"
      "s_nop 0\n"
      "buffer_load_dwordx4 v8, s[40:43], s68 offen lds\n"
      "buffer_load_dwordx4 v9, s[40:43], s68 offen offset:1024 lds\n"
      "buffer_load_dwordx4 v10, s[40:43], s68 offen offset:2048 lds\n"
      "s_cmp_eq_u32 s66, 7\n"
      "s_cbranch_scc1 108f\n"
      "s_add_u32 m0, s65, 29696\n"
      "s_mov_b32 exec_hi, s66\n"
      "buffer_load_dwordx4 v11, s[44:47], s69 offen lds\n"
      "s_mov_b32 exec_hi, -1\n"
      "108:\n"
      "s_add_u32 s68, s68, 49152\n"
      "s_add_u32 s69, s69, 20480\n"
      "s_cmp_lg_u32 s69, 819200\n"
      "s_cselect_b32 s68, s68, 0\n"
      "s_cselect_b32 s69, s69, 0\n"
      "v_readfirstlane_b32 s90, %[gv]\n"
      "s_mul_i32 s91, s90, 0x9E3779B1\n"
      "s_add_u32 s91, s91, 0x7F4A7C15\n"
      "s_xor_b32 s92, s91, 16\n"
      "s_cmp_eq_u32 s90, s91\n"
      "s_cselect_b32 s91, s92, s91\n"
      "v_mov_b32 v18, s91\n"
      "s_mov_b32 s91, 0x100000\n"
      "110:\n"
      "ds_read_b32 v248, v16 offset:44\n"
      "s_sub_u32 s91, s91, 1\n"
      "s_cmp_eq_u32 s91, 0\n"
      "s_cbranch_scc1 111f\n"
      "s_waitcnt lgkmcnt(0)\n"
      "v_readfirstlane_b32 s90, v248\n"
      "s_cmp_lt_u32 s90, 8\n"
      "s_cbranch_scc1 110b\n"
      "111:\n"
      "ds_read_b64_tr_b16 v[128:129], v12 offset:0\n"
      "ds_read_b64_tr_b16 v[130:131], v12 offset:640\n"
      "ds_read_b64_tr_b16 v[132:133], v12 offset:32\n"
      "ds_read_b64_tr_b16 v[134:135], v12 offset:672\n"
      "ds_read_b64_tr_b16 v[136:137], v12 offset:1280\n"
      "ds_read_b64_tr_b16 v[138:139], v12 offset:1920\n"
      "ds_read_b64_tr_b16 v[140:141], v12 offset:1312\n"
      "ds_read_b64_tr_b16 v[142:143], v12 offset:1952\n"
      "ds_read_b64_tr_b16 v[144:145], v12 offset:2560\n"
      "ds_read_b64_tr_b16 v[146:147], v12 offset:3200\n"
      "ds_read_b64_tr_b16 v[148:149], v12 offset:2592\n"
      "ds_read_b64_tr_b16 v[150:151], v12 offset:3232\n"
      "ds_read_b64_tr_b16 v[152:153], v12 offset:3840\n"
      "ds_read_b64_tr_b16 v[154:155], v12 offset:4480\n"
      "ds_read_b64_tr_b16 v[156:157], v12 offset:3872\n"
      "ds_read_b64_tr_b16 v[158:159], v12 offset:4512\n"
      "ds_read_b64_tr_b16 v[160:161], v12 offset:5120\n"
      "ds_read_b64_tr_b16 v[162:163], v12 offset:5760\n"
      "ds_read_b64_tr_b16 v[164:165], v12 offset:5152\n"
      "ds_read_b64_tr_b16 v[166:167], v12 offset:5792\n"
      "ds_read_b128 v[192:195], v13 offset:0\n"
      "ds_read_b128 v[196:199], v13 offset:64\n"
      "ds_read_b128 v[200:203], v13 offset:128\n"
      "ds_read_b128 v[204:207], v13 offset:192\n"
      "ds_read_b128 v[208:211], v13 offset:256\n"
      "s_waitcnt lgkmcnt(0)\n"
      "s_mov_b64 exec, 1\n"
      "ds_add_u32 v16, v253 offset:52\n"
      "s_mov_b64 exec, -1\n"
      "s_cmp_lg_u32 s67, 0\n"
      "s_cbranch_scc1 6f\n"
      "v_mfma_f32_16x16x32_bf16 v[168:171], v[128:131], v[192:195], 0\n"
      "v_mfma_f32_16x16x32_bf16 v[172:175], v[132:135], v[192:195], 0\n"
      "v_mfma_f32_16x16x32_bf16 v[168:171], v[136:139], v[196:199], v[168:171]\n"
      "v_mfma_f32_16x16x32_bf16 v[172:175], v[140:143], v[196:199], v[172:175]\n"
      "v_mfma_f32_16x16x32_bf16 v[168:171], v[144:147], v[200:203], v[168:171]\n"
      "v_mfma_f32_16x16x32_bf16 v[172:175], v[148:151], v[200:203], v[172:175]\n"
      "v_mfma_f32_16x16x32_bf16 v[168:171], v[152:155], v[204:207], v[168:171]\n"
      "v_mfma_f32_16x16x32_bf16 v[172:175], v[156:159], v[204:207], v[172:175]\n"
      "v_mfma_f32_16x16x32_bf16 v[168:171], v[160:163], v[208:211], v[168:171]\n"
      "v_mfma_f32_16x16x32_bf16 v[172:175], v[164:167], v[208:211], v[172:175]\n"
      "s_branch 7f\n"
      "6:\n"
      "v_mfma_f32_16x16x32_bf16 v[168:171], v[192:195], v[128:131], 0\n"
      "v_mfma_f32_16x16x32_bf16 v[172:175], v[192:195], v[132:135], 0\n"
      "v_mfma_f32_16x16x32_bf16 v[168:171], v[196:199], v[136:139], v[168:171]\n"
      "v_mfma_f32_16x16x32_bf16 v[172:175], v[196:199], v[140:143], v[172:175]\n"
      "v_mfma_f32_16x16x32_bf16 v[168:171], v[200:203], v[144:147], v[168:171]\n"
      "v_mfma_f32_16x16x32_bf16 v[172:175], v[200:203], v[148:151], v[172:175]\n"
      "v_mfma_f32_16x16x32_bf16 v[168:171], v[204:207], v[152:155], v[168:171]\n"
      "v_mfma_f32_16x16x32_bf16 v[172:175], v[204:207], v[156:159], v[172:175]\n"
      "v_mfma_f32_16x16x32_bf16 v[168:171], v[208:211], v[160:163], v[168:171]\n"
      "v_mfma_f32_16x16x32_bf16 v[172:175], v[208:211], v[164:167], v[172:175]\n"
      "7:\n"
      "s_mov_b32 s91, 0x100000\n"
      "114:\n"
      "ds_read_b32 v248, v16 offset:52\n"
      "s_sub_u32 s91, s91, 1\n"
      "s_cmp_eq_u32 s91, 0\n"
      "s_cbranch_scc1 115f\n"
      "s_waitcnt lgkmcnt(0)\n"
      "v_readfirstlane_b32 s90, v248\n"
      "s_cmp_lt_u32 s90, 4\n"
      "s_cbranch_scc1 114b\n"
      "115:\n"
      "ds_write_b128 v14, v[168:171]\n"
      "ds_write_b128 v15, v[172:175]\n"
      "s_add_u32 m0, s64, 44544\n"
      "s_nop 0\n"
      "buffer_load_dwordx4 v8, s[40:43], s68 offen lds\n"
      "buffer_load_dwordx4 v9, s[40:43], s68 offen offset:1024 lds\n"
      "buffer_load_dwordx4 v10, s[40:43], s68 offen offset:2048 lds\n"
      "s_cmp_eq_u32 s66, 7\n"
      "s_cbranch_scc1 116f\n"
      "s_add_u32 m0, s65, 44544\n"
      "s_mov_b32 exec_hi, s66\n"
      "buffer_load_dwordx4 v11, s[44:47], s69 offen lds\n"
      "s_mov_b32 exec_hi, -1\n"
      "116:\n"
      "s_add_u32 s68, s68, 49152\n"
      "s_add_u32 s69, s69, 20480\n"
      "s_cmp_lg_u32 s69, 819200\n"
      "s_cselect_b32 s68, s68, 0\n"
      "s_cselect_b32 s69, s69, 0\n"
      "s_add_u32 s68, s68, 49152\n"
      "s_add_u32 s69, s69, 20480\n"
      "s_cmp_lg_u32 s69, 819200\n"
      "s_cselect_b32 s68, s68, 0\n"
      "s_cselect_b32 s69, s69, 0\n"
      "s_cmp_eq_u32 s66, 7\n"
      "s_waitcnt lgkmcnt(0)\n"
      "s_cbranch_scc1 8f\n"
      "s_barrier\n"
      "s_waitcnt vmcnt(8)\n"
      "s_barrier\n"
      "s_add_u32 m0, s64, 0\n"
      "s_add_u32 s90, s68, 49152\n"
      "buffer_load_dwordx4 v8, s[40:43], s68 offen lds\n"
      "buffer_load_dwordx4 v9, s[40:43], s68 offen offset:1024 lds\n"
      "buffer_load_dwordx4 v10, s[40:43], s68 offen offset:2048 lds\n"
      "s_add_u32 m0, s65, 0\n"
      "s_mov_b32 exec_hi, s66\n"
      "buffer_load_dwordx4 v11, s[44:47], s69 offen lds\n"
      "s_mov_b32 exec_hi, -1\n"
      "s_add_u32 s69, s69, 20480\n"
      "s_cmp_lg_u32 s90, 1966080\n"
      "s_cselect_b32 s68, s90, 0\n"
      "s_cselect_b32 s69, s69, 0\n"
      "s_waitcnt vmcnt(8)\n"
      "s_barrier\n"
      "s_add_u32 m0, s64, 14848\n"
      "s_add_u32 s90, s68, 49152\n"
      "buffer_load_dwordx4 v8, s[40:43], s68 offen lds\n"
      "buffer_load_dwordx4 v9, s[40:43], s68 offen offset:1024 lds\n"
      "buffer_load_dwordx4 v10, s[40:43], s68 offen offset:2048 lds\n"
      "s_add_u32 m0, s65, 14848\n"
      "s_mov_b32 exec_hi, s66\n"
      "buffer_load_dwordx4 v11, s[44:47], s69 offen lds\n"
      "s_mov_b32 exec_hi, -1\n"
      "s_add_u32 s69, s69, 20480\n"
      "s_cmp_lg_u32 s90, 1966080\n"
      "s_cselect_b32 s68, s90, 0\n"
      "s_cselect_b32 s69, s69, 0\n"
      "s_waitcnt vmcnt(8)\n"
      "s_barrier\n"
      "s_add_u32 m0, s64, 29696\n"
      "s_add_u32 s90, s68, 49152\n"
      "buffer_load_dwordx4 v8, s[40:43], s68 offen lds\n"
      "buffer_load_dwordx4 v9, s[40:43], s68 offen offset:1024 lds\n"
      "buffer_load_dwordx4 v10, s[40:43], s68 offen offset:2048 lds\n"
      "s_add_u32 m0, s65, 29696\n"
      "s_mov_b32 exec_hi, s66\n"
      "buffer_load_dwordx4 v11, s[44:47], s69 offen lds\n"
      "s_mov_b32 exec_hi, -1\n"
      "s_add_u32 s69, s69, 20480\n"
      "s_cmp_lg_u32 s90, 1966080\n"
      "s_cselect_b32 s68, s90, 0\n"
      "s_cselect_b32 s69, s69, 0\n"
      "s_waitcnt vmcnt(12)\n"
      "s_barrier\n"
      "s_add_u32 m0, s64, 44544\n"
      "s_add_u32 s90, s68, 49152\n"
      "buffer_load_dwordx4 v8, s[40:43], s68 offen lds\n"
      "buffer_load_dwordx4 v9, s[40:43], s68 offen offset:1024 lds\n"
      "buffer_load_dwordx4 v10, s[40:43], s68 offen offset:2048 lds\n"
      "s_add_u32 m0, s65, 44544\n"
      "s_mov_b32 exec_hi, s66\n"
      "buffer_load_dwordx4 v11, s[44:47], s69 offen lds\n"
      "s_mov_b32 exec_hi, -1\n"
      "s_add_u32 s69, s69, 20480\n"
      "s_cmp_lg_u32 s90, 1966080\n"
      "s_cselect_b32 s68, s90, 0\n"
      "s_cselect_b32 s69, s69, 0\n"
      "s_waitcnt vmcnt(12)\n"
      "s_barrier\n"
      "s_add_u32 m0, s64, 59392\n"
      "s_add_u32 s90, s68, 49152\n"
      "buffer_load_dwordx4 v8, s[40:43], s68 offen lds\n"
      "buffer_load_dwordx4 v9, s[40:43], s68 offen offset:1024 lds\n"
      "buffer_load_dwordx4 v10, s[40:43], s68 offen offset:2048 lds\n"
      "s_add_u32 m0, s65, 59392\n"
      "s_mov_b32 exec_hi, s66\n"
      "buffer_load_dwordx4 v11, s[44:47], s69 offen lds\n"
      "s_mov_b32 exec_hi, -1\n"
      "s_add_u32 s69, s69, 20480\n"
      "s_cmp_lg_u32 s90, 1966080\n"
      "s_cselect_b32 s68, s90, 0\n"
      "s_cselect_b32 s69, s69, 0\n"
      "s_waitcnt vmcnt(12)\n"
      "s_barrier\n"
      "s_add_u32 m0, s64, 0\n"
      "s_add_u32 s90, s68, 49152\n"
      "buffer_load_dwordx4 v8, s[40:43], s68 offen lds\n"
      "buffer_load_dwordx4 v9, s[40:43], s68 offen offset:1024 lds\n"
      "buffer_load_dwordx4 v10, s[40:43], s68 offen offset:2048 lds\n"
      "s_add_u32 m0, s65, 0\n"
      "s_mov_b32 exec_hi, s66\n"
      "buffer_load_dwordx4 v11, s[44:47], s69 offen lds\n"
      "s_mov_b32 exec_hi, -1\n"
      "s_add_u32 s69, s69, 20480\n"
      "s_cmp_lg_u32 s90, 1966080\n"
      "s_cselect_b32 s68, s90, 0\n"
      "s_cselect_b32 s69, s69, 0\n"
      "s_waitcnt vmcnt(12)\n"
      "s_barrier\n"
      "s_add_u32 m0, s64, 14848\n"
      "s_add_u32 s90, s68, 49152\n"
      "buffer_load_dwordx4 v8, s[40:43], s68 offen lds\n"
      "buffer_load_dwordx4 v9, s[40:43], s68 offen offset:1024 lds\n"
      "buffer_load_dwordx4 v10, s[40:43], s68 offen offset:2048 lds\n"
      "s_add_u32 m0, s65, 14848\n"
      "s_mov_b32 exec_hi, s66\n"
      "buffer_load_dwordx4 v11, s[44:47], s69 offen lds\n"
      "s_mov_b32 exec_hi, -1\n"
      "s_add_u32 s69, s69, 20480\n"
      "s_cmp_lg_u32 s90, 1966080\n"
      "s_cselect_b32 s68, s90, 0\n"
      "s_cselect_b32 s69, s69, 0\n"
      "s_waitcnt vmcnt(12)\n"
      "s_barrier\n"
      "s_add_u32 m0, s64, 29696\n"
      "s_add_u32 s90, s68, 49152\n"
      "buffer_load_dwordx4 v8, s[40:43], s68 offen lds\n"
      "buffer_load_dwordx4 v9, s[40:43], s68 offen offset:1024 lds\n"
      "buffer_load_dwordx4 v10, s[40:43], s68 offen offset:2048 lds\n"
      "s_add_u32 m0, s65, 29696\n"
      "s_mov_b32 exec_hi, s66\n"
      "buffer_load_dwordx4 v11, s[44:47], s69 offen lds\n"
      "s_mov_b32 exec_hi, -1\n"
      "s_add_u32 s69, s69, 20480\n"
      "s_cmp_lg_u32 s90, 1966080\n"
      "s_cselect_b32 s68, s90, 0\n"
      "s_cselect_b32 s69, s69, 0\n"
      "s_waitcnt vmcnt(12)\n"
      "s_barrier\n"
      "s_add_u32 m0, s64, 44544\n"
      "s_add_u32 s90, s68, 49152\n"
      "buffer_load_dwordx4 v8, s[40:43], s68 offen lds\n"
      "buffer_load_dwordx4 v9, s[40:43], s68 offen offset:1024 lds\n"
      "buffer_load_dwordx4 v10, s[40:43], s68 offen offset:2048 lds\n"
      "s_add_u32 m0, s65, 44544\n"
      "s_mov_b32 exec_hi, s66\n"
      "buffer_load_dwordx4 v11, s[44:47], s69 offen lds\n"
      "s_mov_b32 exec_hi, -1\n"
      "s_add_u32 s69, s69, 20480\n"
      "s_cmp_lg_u32 s90, 1966080\n"
      "s_cselect_b32 s68, s90, 0\n"
      "s_cselect_b32 s69, s69, 0\n"
      "s_waitcnt vmcnt(12)\n"
      "s_barrier\n"
      "s_add_u32 m0, s64, 59392\n"
      "s_add_u32 s90, s68, 49152\n"
      "buffer_load_dwordx4 v8, s[40:43], s68 offen lds\n"
      "buffer_load_dwordx4 v9, s[40:43], s68 offen offset:1024 lds\n"
      "buffer_load_dwordx4 v10, s[40:43], s68 offen offset:2048 lds\n"
      "s_add_u32 m0, s65, 59392\n"
      "s_mov_b32 exec_hi, s66\n"
      "buffer_load_dwordx4 v11, s[44:47], s69 offen lds\n"
      "s_mov_b32 exec_hi, -1\n"
      "s_add_u32 s69, s69, 20480\n"
      "s_cmp_lg_u32 s90, 1966080\n"
      "s_cselect_b32 s68, s90, 0\n"
      "s_cselect_b32 s69, s69, 0\n"
      "s_waitcnt vmcnt(12)\n"
      "s_barrier\n"
      "s_add_u32 m0, s64, 0\n"
      "s_add_u32 s90, s68, 49152\n"
      "buffer_load_dwordx4 v8, s[40:43], s68 offen lds\n"
      "buffer_load_dwordx4 v9, s[40:43], s68 offen offset:1024 lds\n"
      "buffer_load_dwordx4 v10, s[40:43], s68 offen offset:2048 lds\n"
      "s_add_u32 m0, s65, 0\n"
      "s_mov_b32 exec_hi, s66\n"
      "buffer_load_dwordx4 v11, s[44:47], s69 offen lds\n"
      "s_mov_b32 exec_hi, -1\n"
      "s_add_u32 s69, s69, 20480\n"
      "s_cmp_lg_u32 s90, 1966080\n"
      "s_cselect_b32 s68, s90, 0\n"
      "s_cselect_b32 s69, s69, 0\n"
      "s_waitcnt vmcnt(12)\n"
      "s_barrier\n"
      "s_add_u32 m0, s64, 14848\n"
      "s_add_u32 s90, s68, 49152\n"
      "buffer_load_dwordx4 v8, s[40:43], s68 offen lds\n"
      "buffer_load_dwordx4 v9, s[40:43], s68 offen offset:1024 lds\n"
      "buffer_load_dwordx4 v10, s[40:43], s68 offen offset:2048 lds\n"
      "s_add_u32 m0, s65, 14848\n"
      "s_mov_b32 exec_hi, s66\n"
      "buffer_load_dwordx4 v11, s[44:47], s69 offen lds\n"
      "s_mov_b32 exec_hi, -1\n"
      "s_add_u32 s69, s69, 20480\n"
      "s_cmp_lg_u32 s90, 1966080\n"
      "s_cselect_b32 s68, s90, 0\n"
      "s_cselect_b32 s69, s69, 0\n"
      "s_waitcnt vmcnt(12)\n"
      "s_barrier\n"
      "s_add_u32 m0, s64, 29696\n"
      "s_add_u32 s90, s68, 49152\n"
      "buffer_load_dwordx4 v8, s[40:43], s68 offen lds\n"
      "buffer_load_dwordx4 v9, s[40:43], s68 offen offset:1024 lds\n"
      "buffer_load_dwordx4 v10, s[40:43], s68 offen offset:2048 lds\n"
      "s_add_u32 m0, s65, 29696\n"
      "s_mov_b32 exec_hi, s66\n"
      "buffer_load_dwordx4 v11, s[44:47], s69 offen lds\n"
      "s_mov_b32 exec_hi, -1\n"
      "s_add_u32 s69, s69, 20480\n"
      "s_cmp_lg_u32 s90, 1966080\n"
      "s_cselect_b32 s68, s90, 0\n"
      "s_cselect_b32 s69, s69, 0\n"
      "s_waitcnt vmcnt(12)\n"
      "s_barrier\n"
      "s_add_u32 m0, s64, 44544\n"
      "s_add_u32 s90, s68, 49152\n"
      "buffer_load_dwordx4 v8, s[40:43], s68 offen lds\n"
      "buffer_load_dwordx4 v9, s[40:43], s68 offen offset:1024 lds\n"
      "buffer_load_dwordx4 v10, s[40:43], s68 offen offset:2048 lds\n"
      "s_add_u32 m0, s65, 44544\n"
      "s_mov_b32 exec_hi, s66\n"
      "buffer_load_dwordx4 v11, s[44:47], s69 offen lds\n"
      "s_mov_b32 exec_hi, -1\n"
      "s_add_u32 s69, s69, 20480\n"
      "s_cmp_lg_u32 s90, 1966080\n"
      "s_cselect_b32 s68, s90, 0\n"
      "s_cselect_b32 s69, s69, 0\n"
      "s_waitcnt vmcnt(12)\n"
      "s_barrier\n"
      "s_add_u32 m0, s64, 59392\n"
      "s_add_u32 s90, s68, 49152\n"
      "buffer_load_dwordx4 v8, s[40:43], s68 offen lds\n"
      "buffer_load_dwordx4 v9, s[40:43], s68 offen offset:1024 lds\n"
      "buffer_load_dwordx4 v10, s[40:43], s68 offen offset:2048 lds\n"
      "s_add_u32 m0, s65, 59392\n"
      "s_mov_b32 exec_hi, s66\n"
      "buffer_load_dwordx4 v11, s[44:47], s69 offen lds\n"
      "s_mov_b32 exec_hi, -1\n"
      "s_add_u32 s69, s69, 20480\n"
      "s_cmp_lg_u32 s90, 1966080\n"
      "s_cselect_b32 s68, s90, 0\n"
      "s_cselect_b32 s69, s69, 0\n"
      "s_waitcnt vmcnt(12)\n"
      "s_barrier\n"
      "s_add_u32 m0, s64, 0\n"
      "s_add_u32 s90, s68, 49152\n"
      "buffer_load_dwordx4 v8, s[40:43], s68 offen lds\n"
      "buffer_load_dwordx4 v9, s[40:43], s68 offen offset:1024 lds\n"
      "buffer_load_dwordx4 v10, s[40:43], s68 offen offset:2048 lds\n"
      "s_add_u32 m0, s65, 0\n"
      "s_mov_b32 exec_hi, s66\n"
      "buffer_load_dwordx4 v11, s[44:47], s69 offen lds\n"
      "s_mov_b32 exec_hi, -1\n"
      "s_add_u32 s69, s69, 20480\n"
      "s_cmp_lg_u32 s90, 1966080\n"
      "s_cselect_b32 s68, s90, 0\n"
      "s_cselect_b32 s69, s69, 0\n"
      "s_waitcnt vmcnt(12)\n"
      "s_barrier\n"
      "s_add_u32 m0, s64, 14848\n"
      "s_add_u32 s90, s68, 49152\n"
      "buffer_load_dwordx4 v8, s[40:43], s68 offen lds\n"
      "buffer_load_dwordx4 v9, s[40:43], s68 offen offset:1024 lds\n"
      "buffer_load_dwordx4 v10, s[40:43], s68 offen offset:2048 lds\n"
      "s_add_u32 m0, s65, 14848\n"
      "s_mov_b32 exec_hi, s66\n"
      "buffer_load_dwordx4 v11, s[44:47], s69 offen lds\n"
      "s_mov_b32 exec_hi, -1\n"
      "s_add_u32 s69, s69, 20480\n"
      "s_cmp_lg_u32 s90, 1966080\n"
      "s_cselect_b32 s68, s90, 0\n"
      "s_cselect_b32 s69, s69, 0\n"
      "s_waitcnt vmcnt(12)\n"
      "s_barrier\n"
      "s_add_u32 m0, s64, 29696\n"
      "s_add_u32 s90, s68, 49152\n"
      "buffer_load_dwordx4 v8, s[40:43], s68 offen lds\n"
      "buffer_load_dwordx4 v9, s[40:43], s68 offen offset:1024 lds\n"
      "buffer_load_dwordx4 v10, s[40:43], s68 offen offset:2048 lds\n"
      "s_add_u32 m0, s65, 29696\n"
      "s_mov_b32 exec_hi, s66\n"
      "buffer_load_dwordx4 v11, s[44:47], s69 offen lds\n"
      "s_mov_b32 exec_hi, -1\n"
      "s_add_u32 s69, s69, 20480\n"
      "s_cmp_lg_u32 s90, 1966080\n"
      "s_cselect_b32 s68, s90, 0\n"
      "s_cselect_b32 s69, s69, 0\n"
      "s_waitcnt vmcnt(12)\n"
      "s_barrier\n"
      "s_add_u32 m0, s64, 44544\n"
      "s_add_u32 s90, s68, 49152\n"
      "buffer_load_dwordx4 v8, s[40:43], s68 offen lds\n"
      "buffer_load_dwordx4 v9, s[40:43], s68 offen offset:1024 lds\n"
      "buffer_load_dwordx4 v10, s[40:43], s68 offen offset:2048 lds\n"
      "s_add_u32 m0, s65, 44544\n"
      "s_mov_b32 exec_hi, s66\n"
      "buffer_load_dwordx4 v11, s[44:47], s69 offen lds\n"
      "s_mov_b32 exec_hi, -1\n"
      "s_add_u32 s69, s69, 20480\n"
      "s_cmp_lg_u32 s90, 1966080\n"
      "s_cselect_b32 s68, s90, 0\n"
      "s_cselect_b32 s69, s69, 0\n"
      "s_waitcnt vmcnt(12)\n"
      "s_barrier\n"
      "s_add_u32 m0, s64, 59392\n"
      "s_add_u32 s90, s68, 49152\n"
      "buffer_load_dwordx4 v8, s[40:43], s68 offen lds\n"
      "buffer_load_dwordx4 v9, s[40:43], s68 offen offset:1024 lds\n"
      "buffer_load_dwordx4 v10, s[40:43], s68 offen offset:2048 lds\n"
      "s_add_u32 m0, s65, 59392\n"
      "s_mov_b32 exec_hi, s66\n"
      "buffer_load_dwordx4 v11, s[44:47], s69 offen lds\n"
      "s_mov_b32 exec_hi, -1\n"
      "s_add_u32 s69, s69, 20480\n"
      "s_cmp_lg_u32 s90, 1966080\n"
      "s_cselect_b32 s68, s90, 0\n"
      "s_cselect_b32 s69, s69, 0\n"
      "s_waitcnt vmcnt(12)\n"
      "s_barrier\n"
      "s_add_u32 m0, s64, 0\n"
      "s_add_u32 s90, s68, 49152\n"
      "buffer_load_dwordx4 v8, s[40:43], s68 offen lds\n"
      "buffer_load_dwordx4 v9, s[40:43], s68 offen offset:1024 lds\n"
      "buffer_load_dwordx4 v10, s[40:43], s68 offen offset:2048 lds\n"
      "s_add_u32 m0, s65, 0\n"
      "s_mov_b32 exec_hi, s66\n"
      "buffer_load_dwordx4 v11, s[44:47], s69 offen lds\n"
      "s_mov_b32 exec_hi, -1\n"
      "s_add_u32 s69, s69, 20480\n"
      "s_cmp_lg_u32 s90, 1966080\n"
      "s_cselect_b32 s68, s90, 0\n"
      "s_cselect_b32 s69, s69, 0\n"
      "s_waitcnt vmcnt(12)\n"
      "s_barrier\n"
      "s_add_u32 m0, s64, 14848\n"
      "s_add_u32 s90, s68, 49152\n"
      "buffer_load_dwordx4 v8, s[40:43], s68 offen lds\n"
      "buffer_load_dwordx4 v9, s[40:43], s68 offen offset:1024 lds\n"
      "buffer_load_dwordx4 v10, s[40:43], s68 offen offset:2048 lds\n"
      "s_add_u32 m0, s65, 14848\n"
      "s_mov_b32 exec_hi, s66\n"
      "buffer_load_dwordx4 v11, s[44:47], s69 offen lds\n"
      "s_mov_b32 exec_hi, -1\n"
      "s_add_u32 s69, s69, 20480\n"
      "s_cmp_lg_u32 s90, 1966080\n"
      "s_cselect_b32 s68, s90, 0\n"
      "s_cselect_b32 s69, s69, 0\n"
      "s_waitcnt vmcnt(12)\n"
      "s_barrier\n"
      "s_add_u32 m0, s64, 29696\n"
      "s_add_u32 s90, s68, 49152\n"
      "buffer_load_dwordx4 v8, s[40:43], s68 offen lds\n"
      "buffer_load_dwordx4 v9, s[40:43], s68 offen offset:1024 lds\n"
      "buffer_load_dwordx4 v10, s[40:43], s68 offen offset:2048 lds\n"
      "s_add_u32 m0, s65, 29696\n"
      "s_mov_b32 exec_hi, s66\n"
      "buffer_load_dwordx4 v11, s[44:47], s69 offen lds\n"
      "s_mov_b32 exec_hi, -1\n"
      "s_add_u32 s69, s69, 20480\n"
      "s_cmp_lg_u32 s90, 1966080\n"
      "s_cselect_b32 s68, s90, 0\n"
      "s_cselect_b32 s69, s69, 0\n"
      "s_waitcnt vmcnt(12)\n"
      "s_barrier\n"
      "s_add_u32 m0, s64, 44544\n"
      "s_add_u32 s90, s68, 49152\n"
      "buffer_load_dwordx4 v8, s[40:43], s68 offen lds\n"
      "buffer_load_dwordx4 v9, s[40:43], s68 offen offset:1024 lds\n"
      "buffer_load_dwordx4 v10, s[40:43], s68 offen offset:2048 lds\n"
      "s_add_u32 m0, s65, 44544\n"
      "s_mov_b32 exec_hi, s66\n"
      "buffer_load_dwordx4 v11, s[44:47], s69 offen lds\n"
      "s_mov_b32 exec_hi, -1\n"
      "s_add_u32 s69, s69, 20480\n"
      "s_cmp_lg_u32 s90, 1966080\n"
      "s_cselect_b32 s68, s90, 0\n"
      "s_cselect_b32 s69, s69, 0\n"
      "s_waitcnt vmcnt(12)\n"
      "s_barrier\n"
      "s_add_u32 m0, s64, 59392\n"
      "s_add_u32 s90, s68, 49152\n"
      "buffer_load_dwordx4 v8, s[40:43], s68 offen lds\n"
      "buffer_load_dwordx4 v9, s[40:43], s68 offen offset:1024 lds\n"
      "buffer_load_dwordx4 v10, s[40:43], s68 offen offset:2048 lds\n"
      "s_add_u32 m0, s65, 59392\n"
      "s_mov_b32 exec_hi, s66\n"
      "buffer_load_dwordx4 v11, s[44:47], s69 offen lds\n"
      "s_mov_b32 exec_hi, -1\n"
      "s_add_u32 s69, s69, 20480\n"
      "s_cmp_lg_u32 s90, 1966080\n"
      "s_cselect_b32 s68, s90, 0\n"
      "s_cselect_b32 s69, s69, 0\n"
      "s_waitcnt vmcnt(12)\n"
      "s_barrier\n"
      "s_add_u32 m0, s64, 0\n"
      "s_add_u32 s90, s68, 49152\n"
      "buffer_load_dwordx4 v8, s[40:43], s68 offen lds\n"
      "buffer_load_dwordx4 v9, s[40:43], s68 offen offset:1024 lds\n"
      "buffer_load_dwordx4 v10, s[40:43], s68 offen offset:2048 lds\n"
      "s_add_u32 m0, s65, 0\n"
      "s_mov_b32 exec_hi, s66\n"
      "buffer_load_dwordx4 v11, s[44:47], s69 offen lds\n"
      "s_mov_b32 exec_hi, -1\n"
      "s_add_u32 s69, s69, 20480\n"
      "s_cmp_lg_u32 s90, 1966080\n"
      "s_cselect_b32 s68, s90, 0\n"
      "s_cselect_b32 s69, s69, 0\n"
      "s_waitcnt vmcnt(12)\n"
      "s_barrier\n"
      "s_add_u32 m0, s64, 14848\n"
      "s_add_u32 s90, s68, 49152\n"
      "buffer_load_dwordx4 v8, s[40:43], s68 offen lds\n"
      "buffer_load_dwordx4 v9, s[40:43], s68 offen offset:1024 lds\n"
      "buffer_load_dwordx4 v10, s[40:43], s68 offen offset:2048 lds\n"
      "s_add_u32 m0, s65, 14848\n"
      "s_mov_b32 exec_hi, s66\n"
      "buffer_load_dwordx4 v11, s[44:47], s69 offen lds\n"
      "s_mov_b32 exec_hi, -1\n"
      "s_add_u32 s69, s69, 20480\n"
      "s_cmp_lg_u32 s90, 1966080\n"
      "s_cselect_b32 s68, s90, 0\n"
      "s_cselect_b32 s69, s69, 0\n"
      "s_waitcnt vmcnt(12)\n"
      "s_barrier\n"
      "s_add_u32 m0, s64, 29696\n"
      "s_add_u32 s90, s68, 49152\n"
      "buffer_load_dwordx4 v8, s[40:43], s68 offen lds\n"
      "buffer_load_dwordx4 v9, s[40:43], s68 offen offset:1024 lds\n"
      "buffer_load_dwordx4 v10, s[40:43], s68 offen offset:2048 lds\n"
      "s_add_u32 m0, s65, 29696\n"
      "s_mov_b32 exec_hi, s66\n"
      "buffer_load_dwordx4 v11, s[44:47], s69 offen lds\n"
      "s_mov_b32 exec_hi, -1\n"
      "s_add_u32 s69, s69, 20480\n"
      "s_cmp_lg_u32 s90, 1966080\n"
      "s_cselect_b32 s68, s90, 0\n"
      "s_cselect_b32 s69, s69, 0\n"
      "s_waitcnt vmcnt(12)\n"
      "s_barrier\n"
      "s_add_u32 m0, s64, 44544\n"
      "s_add_u32 s90, s68, 49152\n"
      "buffer_load_dwordx4 v8, s[40:43], s68 offen lds\n"
      "buffer_load_dwordx4 v9, s[40:43], s68 offen offset:1024 lds\n"
      "buffer_load_dwordx4 v10, s[40:43], s68 offen offset:2048 lds\n"
      "s_add_u32 m0, s65, 44544\n"
      "s_mov_b32 exec_hi, s66\n"
      "buffer_load_dwordx4 v11, s[44:47], s69 offen lds\n"
      "s_mov_b32 exec_hi, -1\n"
      "s_add_u32 s69, s69, 20480\n"
      "s_cmp_lg_u32 s90, 1966080\n"
      "s_cselect_b32 s68, s90, 0\n"
      "s_cselect_b32 s69, s69, 0\n"
      "s_waitcnt vmcnt(12)\n"
      "s_barrier\n"
      "s_add_u32 m0, s64, 59392\n"
      "s_add_u32 s90, s68, 49152\n"
      "buffer_load_dwordx4 v8, s[40:43], s68 offen lds\n"
      "buffer_load_dwordx4 v9, s[40:43], s68 offen offset:1024 lds\n"
      "buffer_load_dwordx4 v10, s[40:43], s68 offen offset:2048 lds\n"
      "s_add_u32 m0, s65, 59392\n"
      "s_mov_b32 exec_hi, s66\n"
      "buffer_load_dwordx4 v11, s[44:47], s69 offen lds\n"
      "s_mov_b32 exec_hi, -1\n"
      "s_add_u32 s69, s69, 20480\n"
      "s_cmp_lg_u32 s90, 1966080\n"
      "s_cselect_b32 s68, s90, 0\n"
      "s_cselect_b32 s69, s69, 0\n"
      "s_waitcnt vmcnt(12)\n"
      "s_barrier\n"
      "s_add_u32 m0, s64, 0\n"
      "s_add_u32 s90, s68, 49152\n"
      "buffer_load_dwordx4 v8, s[40:43], s68 offen lds\n"
      "buffer_load_dwordx4 v9, s[40:43], s68 offen offset:1024 lds\n"
      "buffer_load_dwordx4 v10, s[40:43], s68 offen offset:2048 lds\n"
      "s_add_u32 m0, s65, 0\n"
      "s_mov_b32 exec_hi, s66\n"
      "buffer_load_dwordx4 v11, s[44:47], s69 offen lds\n"
      "s_mov_b32 exec_hi, -1\n"
      "s_add_u32 s69, s69, 20480\n"
      "s_cmp_lg_u32 s90, 1966080\n"
      "s_cselect_b32 s68, s90, 0\n"
      "s_cselect_b32 s69, s69, 0\n"
      "s_waitcnt vmcnt(12)\n"
      "s_barrier\n"
      "s_add_u32 m0, s64, 14848\n"
      "s_add_u32 s90, s68, 49152\n"
      "buffer_load_dwordx4 v8, s[40:43], s68 offen lds\n"
      "buffer_load_dwordx4 v9, s[40:43], s68 offen offset:1024 lds\n"
      "buffer_load_dwordx4 v10, s[40:43], s68 offen offset:2048 lds\n"
      "s_add_u32 m0, s65, 14848\n"
      "s_mov_b32 exec_hi, s66\n"
      "buffer_load_dwordx4 v11, s[44:47], s69 offen lds\n"
      "s_mov_b32 exec_hi, -1\n"
      "s_add_u32 s69, s69, 20480\n"
      "s_cmp_lg_u32 s90, 1966080\n"
      "s_cselect_b32 s68, s90, 0\n"
      "s_cselect_b32 s69, s69, 0\n"
      "s_waitcnt vmcnt(12)\n"
      "s_barrier\n"
      "s_add_u32 m0, s64, 29696\n"
      "s_add_u32 s90, s68, 49152\n"
      "buffer_load_dwordx4 v8, s[40:43], s68 offen lds\n"
      "buffer_load_dwordx4 v9, s[40:43], s68 offen offset:1024 lds\n"
      "buffer_load_dwordx4 v10, s[40:43], s68 offen offset:2048 lds\n"
      "s_add_u32 m0, s65, 29696\n"
      "s_mov_b32 exec_hi, s66\n"
      "buffer_load_dwordx4 v11, s[44:47], s69 offen lds\n"
      "s_mov_b32 exec_hi, -1\n"
      "s_add_u32 s69, s69, 20480\n"
      "s_cmp_lg_u32 s90, 1966080\n"
      "s_cselect_b32 s68, s90, 0\n"
      "s_cselect_b32 s69, s69, 0\n"
      "s_waitcnt vmcnt(12)\n"
      "s_barrier\n"
      "s_add_u32 m0, s64, 44544\n"
      "s_add_u32 s90, s68, 49152\n"
      "buffer_load_dwordx4 v8, s[40:43], s68 offen lds\n"
      "buffer_load_dwordx4 v9, s[40:43], s68 offen offset:1024 lds\n"
      "buffer_load_dwordx4 v10, s[40:43], s68 offen offset:2048 lds\n"
      "s_add_u32 m0, s65, 44544\n"
      "s_mov_b32 exec_hi, s66\n"
      "buffer_load_dwordx4 v11, s[44:47], s69 offen lds\n"
      "s_mov_b32 exec_hi, -1\n"
      "s_add_u32 s69, s69, 20480\n"
      "s_cmp_lg_u32 s90, 1966080\n"
      "s_cselect_b32 s68, s90, 0\n"
      "s_cselect_b32 s69, s69, 0\n"
      "s_waitcnt vmcnt(12)\n"
      "s_barrier\n"
      "s_add_u32 m0, s64, 59392\n"
      "s_add_u32 s90, s68, 49152\n"
      "buffer_load_dwordx4 v8, s[40:43], s68 offen lds\n"
      "buffer_load_dwordx4 v9, s[40:43], s68 offen offset:1024 lds\n"
      "buffer_load_dwordx4 v10, s[40:43], s68 offen offset:2048 lds\n"
      "s_add_u32 m0, s65, 59392\n"
      "s_mov_b32 exec_hi, s66\n"
      "buffer_load_dwordx4 v11, s[44:47], s69 offen lds\n"
      "s_mov_b32 exec_hi, -1\n"
      "s_add_u32 s69, s69, 20480\n"
      "s_cmp_lg_u32 s90, 1966080\n"
      "s_cselect_b32 s68, s90, 0\n"
      "s_cselect_b32 s69, s69, 0\n"
      "s_branch 10f\n"
      "8:\n"
      "s_barrier\n"
      "ds_read_b128 v[212:215], v39 offset:0\n"
      "ds_read_b128 v[216:219], v20 offset:0\n"
      "ds_read_b128 v[220:223], v39 offset:1280\n"
      "ds_read_b128 v[224:227], v20 offset:1280\n"
      "ds_read_b128 v[228:231], v39 offset:2560\n"
      "ds_read_b128 v[232:235], v20 offset:2560\n"
      "s_waitcnt vmcnt(6)\n"
      "s_barrier\n"
      "s_add_u32 m0, s64, 0\n"
      "s_add_u32 s90, s68, 49152\n"
      "buffer_load_dwordx4 v8, s[40:43], s68 offen lds\n"
      "buffer_load_dwordx4 v9, s[40:43], s68 offen offset:1024 lds\n"
      "buffer_load_dwordx4 v10, s[40:43], s68 offen offset:2048 lds\n"
      "s_cmp_lg_u32 s90, 1966080\n"
      "s_cselect_b32 s68, s90, 0\n"
      "s_waitcnt lgkmcnt(0)\n"
      "v_add_f32 v168, v168, v212\n"
      "v_add_f32 v169, v169, v213\n"
      "v_add_f32 v170, v170, v214\n"
      "v_add_f32 v171, v171, v215\n"
      "v_add_f32 v172, v172, v216\n"
      "v_add_f32 v173, v173, v217\n"
      "v_add_f32 v174, v174, v218\n"
      "v_add_f32 v175, v175, v219\n"
      "s_waitcnt vmcnt(6)\n"
      "s_barrier\n"
      "s_add_u32 m0, s64, 14848\n"
      "s_add_u32 s90, s68, 49152\n"
      "buffer_load_dwordx4 v8, s[40:43], s68 offen lds\n"
      "buffer_load_dwordx4 v9, s[40:43], s68 offen offset:1024 lds\n"
      "buffer_load_dwordx4 v10, s[40:43], s68 offen offset:2048 lds\n"
      "s_cmp_lg_u32 s90, 1966080\n"
      "s_cselect_b32 s68, s90, 0\n"
      "s_waitcnt lgkmcnt(0)\n"
      "v_add_f32 v168, v168, v220\n"
      "v_add_f32 v169, v169, v221\n"
      "v_add_f32 v170, v170, v222\n"
      "v_add_f32 v171, v171, v223\n"
      "v_add_f32 v172, v172, v224\n"
      "v_add_f32 v173, v173, v225\n"
      "v_add_f32 v174, v174, v226\n"
      "v_add_f32 v175, v175, v227\n"
      "s_waitcnt vmcnt(6)\n"
      "s_barrier\n"
      "s_add_u32 m0, s64, 29696\n"
      "s_add_u32 s90, s68, 49152\n"
      "buffer_load_dwordx4 v8, s[40:43], s68 offen lds\n"
      "buffer_load_dwordx4 v9, s[40:43], s68 offen offset:1024 lds\n"
      "buffer_load_dwordx4 v10, s[40:43], s68 offen offset:2048 lds\n"
      "s_cmp_lg_u32 s90, 1966080\n"
      "s_cselect_b32 s68, s90, 0\n"
      "s_waitcnt lgkmcnt(0)\n"
      "v_add_f32 v168, v168, v228\n"
      "v_add_f32 v169, v169, v229\n"
      "v_add_f32 v170, v170, v230\n"
      "v_add_f32 v171, v171, v231\n"
      "v_add_f32 v172, v172, v232\n"
      "v_add_f32 v173, v173, v233\n"
      "v_add_f32 v174, v174, v234\n"
      "v_add_f32 v175, v175, v235\n"
      "s_waitcnt vmcnt(9)\n"
      "s_barrier\n"
      "s_add_u32 m0, s64, 44544\n"
      "s_add_u32 s90, s68, 49152\n"
      "buffer_load_dwordx4 v8, s[40:43], s68 offen lds\n"
      "buffer_load_dwordx4 v9, s[40:43], s68 offen offset:1024 lds\n"
      "buffer_load_dwordx4 v10, s[40:43], s68 offen offset:2048 lds\n"
      "s_cmp_lg_u32 s90, 1966080\n"
      "s_cselect_b32 s68, s90, 0\n"
      "v_mul_f32 v168, v23, v168\n"
      "v_mul_f32 v169, v23, v169\n"
      "v_mul_f32 v170, v23, v170\n"
      "v_mul_f32 v171, v23, v171\n"
      "v_mul_f32 v172, v23, v172\n"
      "v_mul_f32 v173, v23, v173\n"
      "v_mul_f32 v174, v23, v174\n"
      "v_mul_f32 v175, v23, v175\n"
      "s_waitcnt vmcnt(9)\n"
      "s_barrier\n"
      "s_add_u32 m0, s64, 59392\n"
      "s_add_u32 s90, s68, 49152\n"
      "buffer_load_dwordx4 v8, s[40:43], s68 offen lds\n"
      "buffer_load_dwordx4 v9, s[40:43], s68 offen offset:1024 lds\n"
      "buffer_load_dwordx4 v10, s[40:43], s68 offen offset:2048 lds\n"
      "s_cmp_lg_u32 s90, 1966080\n"
      "s_cselect_b32 s68, s90, 0\n"
      "v_cvt_pk_bf16_f32 v212, v168, v169\n"
      "v_cvt_pk_bf16_f32 v213, v170, v171\n"
      "v_cvt_pk_bf16_f32 v214, v172, v173\n"
      "v_cvt_pk_bf16_f32 v215, v174, v175\n"
      "v_cndmask_b32 v214, v26, v214, s[60:61]\n"
      "v_cndmask_b32 v215, v26, v215, s[60:61]\n"
      "s_waitcnt vmcnt(9)\n"
      "s_barrier\n"
      "s_add_u32 m0, s64, 0\n"
      "s_add_u32 s90, s68, 49152\n"
      "buffer_load_dwordx4 v8, s[40:43], s68 offen lds\n"
      "buffer_load_dwordx4 v9, s[40:43], s68 offen offset:1024 lds\n"
      "buffer_load_dwordx4 v10, s[40:43], s68 offen offset:2048 lds\n"
      "s_cmp_lg_u32 s90, 1966080\n"
      "s_cselect_b32 s68, s90, 0\n"
      "global_store_dwordx2 v24, v[212:213], s[52:53] sc1\n"
      "s_waitcnt vmcnt(10)\n"
      "s_barrier\n"
      "s_add_u32 m0, s64, 14848\n"
      "s_add_u32 s90, s68, 49152\n"
      "buffer_load_dwordx4 v8, s[40:43], s68 offen lds\n"
      "buffer_load_dwordx4 v9, s[40:43], s68 offen offset:1024 lds\n"
      "buffer_load_dwordx4 v10, s[40:43], s68 offen offset:2048 lds\n"
      "s_cmp_lg_u32 s90, 1966080\n"
      "s_cselect_b32 s68, s90, 0\n"
      "s_mov_b64 exec, s[62:63]\n"
      "global_store_dwordx2 v25, v[214:215], s[52:53] sc1\n"
      "s_mov_b64 exec, -1\n"
      "s_waitcnt vmcnt(11)\n"
      "s_barrier\n"
      "s_add_u32 m0, s64, 29696\n"
      "s_add_u32 s90, s68, 49152\n"
      "buffer_load_dwordx4 v8, s[40:43], s68 offen lds\n"
      "buffer_load_dwordx4 v9, s[40:43], s68 offen offset:1024 lds\n"
      "buffer_load_dwordx4 v10, s[40:43], s68 offen offset:2048 lds\n"
      "s_cmp_lg_u32 s90, 1966080\n"
      "s_cselect_b32 s68, s90, 0\n"
      "v_cndmask_b32 v172, 0, v172, s[60:61]\n"
      "v_cndmask_b32 v173, 0, v173, s[60:61]\n"
      "v_cndmask_b32 v174, 0, v174, s[60:61]\n"
      "v_cndmask_b32 v175, 0, v175, s[60:61]\n"
      "v_mul_f32 v41, v168, v168\n"
      "v_fmac_f32 v41, v169, v169\n"
      "v_fmac_f32 v41, v170, v170\n"
      "v_fmac_f32 v41, v171, v171\n"
      "s_waitcnt vmcnt(11)\n"
      "s_barrier\n"
      "s_add_u32 m0, s64, 44544\n"
      "s_add_u32 s90, s68, 49152\n"
      "buffer_load_dwordx4 v8, s[40:43], s68 offen lds\n"
      "buffer_load_dwordx4 v9, s[40:43], s68 offen offset:1024 lds\n"
      "buffer_load_dwordx4 v10, s[40:43], s68 offen offset:2048 lds\n"
      "s_cmp_lg_u32 s90, 1966080\n"
      "s_cselect_b32 s68, s90, 0\n"
      "v_fmac_f32 v41, v172, v172\n"
      "v_fmac_f32 v41, v173, v173\n"
      "v_fmac_f32 v41, v174, v174\n"
      "v_fmac_f32 v41, v175, v175\n"
      "v_mov_b32 v42, v41\n"
      "s_nop 1\n"
      "v_permlane16_swap_b32 v41, v42\n"
      "v_add_f32 v41, v41, v42\n"
      "s_waitcnt vmcnt(11)\n"
      "s_barrier\n"
      "s_add_u32 m0, s64, 59392\n"
      "s_add_u32 s90, s68, 49152\n"
      "buffer_load_dwordx4 v8, s[40:43], s68 offen lds\n"
      "buffer_load_dwordx4 v9, s[40:43], s68 offen offset:1024 lds\n"
      "buffer_load_dwordx4 v10, s[40:43], s68 offen offset:2048 lds\n"
      "s_cmp_lg_u32 s90, 1966080\n"
      "s_cselect_b32 s68, s90, 0\n"
      "v_mov_b32 v42, v41\n"
      "s_nop 1\n"
      "v_permlane32_swap_b32 v41, v42\n"
      "v_add_f32 v41, v41, v42\n"
      "v_mov_b32 v42, 0x7f61b1e6\n"
      "v_cmp_le_f32 vcc, v41, v42\n"
      "v_mov_b32 v40, v18\n"
      "s_nop 0\n"
      "v_cndmask_b32 v41, v42, v41, vcc\n"
      "s_waitcnt vmcnt(10)\n"
      "s_barrier\n"
      "s_add_u32 m0, s64, 0\n"
      "s_add_u32 s90, s68, 49152\n"
      "buffer_load_dwordx4 v8, s[40:43], s68 offen lds\n"
      "buffer_load_dwordx4 v9, s[40:43], s68 offen offset:1024 lds\n"
      "buffer_load_dwordx4 v10, s[40:43], s68 offen offset:2048 lds\n"
      "s_cmp_lg_u32 s90, 1966080\n"
      "s_cselect_b32 s68, s90, 0\n"
      "s_nop 1\n"
      "v_max_f32_dpp v41, v41, v41 quad_perm:[1,0,3,2] row_mask:0xf bank_mask:0xf\n"
      "s_nop 1\n"
      "v_max_f32_dpp v41, v41, v41 quad_perm:[2,3,0,1] row_mask:0xf bank_mask:0xf\n"
      "s_nop 1\n"
      "v_max_f32_dpp v41, v41, v41 row_half_mirror row_mask:0xf bank_mask:0xf\n"
      "s_nop 1\n"
      "v_max_f32_dpp v41, v41, v41 row_mirror row_mask:0xf bank_mask:0xf\n"
      "s_waitcnt vmcnt(9)\n"
      "s_barrier\n"
      "s_add_u32 m0, s64, 14848\n"
      "s_add_u32 s90, s68, 49152\n"
      "buffer_load_dwordx4 v8, s[40:43], s68 offen lds\n"
      "buffer_load_dwordx4 v9, s[40:43], s68 offen offset:1024 lds\n"
      "buffer_load_dwordx4 v10, s[40:43], s68 offen offset:2048 lds\n"
      "s_cmp_lg_u32 s90, 1966080\n"
      "s_cselect_b32 s68, s90, 0\n"
      "s_waitcnt vmcnt(9)\n"
      "s_barrier\n"
      "s_add_u32 m0, s64, 29696\n"
      "s_add_u32 s90, s68, 49152\n"
      "buffer_load_dwordx4 v8, s[40:43], s68 offen lds\n"
      "buffer_load_dwordx4 v9, s[40:43], s68 offen offset:1024 lds\n"
      "buffer_load_dwordx4 v10, s[40:43], s68 offen offset:2048 lds\n"
      "s_cmp_lg_u32 s90, 1966080\n"
      "s_cselect_b32 s68, s90, 0\n"
      "global_store_dwordx2 v22, v[40:41], s[54:55] sc1\n"
      "s_waitcnt vmcnt(10)\n"
      "s_barrier\n"
      "s_add_u32 m0, s64, 44544\n"
      "s_add_u32 s90, s68, 49152\n"
      "buffer_load_dwordx4 v8, s[40:43], s68 offen lds\n"
      "buffer_load_dwordx4 v9, s[40:43], s68 offen offset:1024 lds\n"
      "buffer_load_dwordx4 v10, s[40:43], s68 offen offset:2048 lds\n"
      "s_cmp_lg_u32 s90, 1966080\n"
      "s_cselect_b32 s68, s90, 0\n"
      "s_waitcnt vmcnt(10)\n"
      "s_barrier\n"
      "s_add_u32 m0, s64, 59392\n"
      "s_add_u32 s90, s68, 49152\n"
      "buffer_load_dwordx4 v8, s[40:43], s68 offen lds\n"
      "buffer_load_dwordx4 v9, s[40:43], s68 offen offset:1024 lds\n"
      "buffer_load_dwordx4 v10, s[40:43], s68 offen offset:2048 lds\n"
      "s_cmp_lg_u32 s90, 1966080\n"
      "s_cselect_b32 s68, s90, 0\n"
      "s_waitcnt vmcnt(10)\n"
      "s_barrier\n"
      "s_add_u32 m0, s64, 0\n"
      "s_add_u32 s90, s68, 49152\n"
      "buffer_load_dwordx4 v8, s[40:43], s68 offen lds\n"
      "buffer_load_dwordx4 v9, s[40:43], s68 offen offset:1024 lds\n"
      "buffer_load_dwordx4 v10, s[40:43], s68 offen offset:2048 lds\n"
      "s_cmp_lg_u32 s90, 1966080\n"
      "s_cselect_b32 s68, s90, 0\n"
      "s_waitcnt vmcnt(10)\n"
      "s_barrier\n"
      "s_add_u32 m0, s64, 14848\n"
      "s_add_u32 s90, s68, 49152\n"
      "buffer_load_dwordx4 v8, s[40:43], s68 offen lds\n"
      "buffer_load_dwordx4 v9, s[40:43], s68 offen offset:1024 lds\n"
      "buffer_load_dwordx4 v10, s[40:43], s68 offen offset:2048 lds\n"
      "s_cmp_lg_u32 s90, 1966080\n"
      "s_cselect_b32 s68, s90, 0\n"
      "s_waitcnt vmcnt(9)\n"
      "s_barrier\n"
      "s_add_u32 m0, s64, 29696\n"
      "s_add_u32 s90, s68, 49152\n"
      "buffer_load_dwordx4 v8, s[40:43], s68 offen lds\n"
      "buffer_load_dwordx4 v9, s[40:43], s68 offen offset:1024 lds\n"
      "buffer_load_dwordx4 v10, s[40:43], s68 offen offset:2048 lds\n"
      "s_cmp_lg_u32 s90, 1966080\n"
      "s_cselect_b32 s68, s90, 0\n"
      "s_waitcnt vmcnt(9)\n"
      "s_barrier\n"
      "s_add_u32 m0, s64, 44544\n"
      "s_add_u32 s90, s68, 49152\n"
      "buffer_load_dwordx4 v8, s[40:43], s68 offen lds\n"
      "buffer_load_dwordx4 v9, s[40:43], s68 offen offset:1024 lds\n"
      "buffer_load_dwordx4 v10, s[40:43], s68 offen offset:2048 lds\n"
      "s_cmp_lg_u32 s90, 1966080\n"
      "s_cselect_b32 s68, s90, 0\n"
      "s_waitcnt vmcnt(9)\n"
      "s_barrier\n"
      "s_add_u32 m0, s64, 59392\n"
      "s_add_u32 s90, s68, 49152\n"
      "buffer_load_dwordx4 v8, s[40:43], s68 offen lds\n"
      "buffer_load_dwordx4 v9, s[40:43], s68 offen offset:1024 lds\n"
      "buffer_load_dwordx4 v10, s[40:43], s68 offen offset:2048 lds\n"
      "s_cmp_lg_u32 s90, 1966080\n"
      "s_cselect_b32 s68, s90, 0\n"
      "s_waitcnt vmcnt(9)\n"
      "s_barrier\n"
      "s_add_u32 m0, s64, 0\n"
      "s_add_u32 s90, s68, 49152\n"
      "buffer_load_dwordx4 v8, s[40:43], s68 offen lds\n"
      "buffer_load_dwordx4 v9, s[40:43], s68 offen offset:1024 lds\n"
      "buffer_load_dwordx4 v10, s[40:43], s68 offen offset:2048 lds\n"
      "s_cmp_lg_u32 s90, 1966080\n"
      "s_cselect_b32 s68, s90, 0\n"
      "s_waitcnt vmcnt(9)\n"
      "s_barrier\n"
      "s_add_u32 m0, s64, 14848\n"
      "s_add_u32 s90, s68, 49152\n"
      "buffer_load_dwordx4 v8, s[40:43], s68 offen lds\n"
      "buffer_load_dwordx4 v9, s[40:43], s68 offen offset:1024 lds\n"
      "buffer_load_dwordx4 v10, s[40:43], s68 offen offset:2048 lds\n"
      "s_cmp_lg_u32 s90, 1966080\n"
      "s_cselect_b32 s68, s90, 0\n"
      "s_waitcnt vmcnt(9)\n"
      "s_barrier\n"
      "s_add_u32 m0, s64, 29696\n"
      "s_add_u32 s90, s68, 49152\n"
      "buffer_load_dwordx4 v8, s[40:43], s68 offen lds\n"
      "buffer_load_dwordx4 v9, s[40:43], s68 offen offset:1024 lds\n"
      "buffer_load_dwordx4 v10, s[40:43], s68 offen offset:2048 lds\n"
      "s_cmp_lg_u32 s90, 1966080\n"
      "s_cselect_b32 s68, s90, 0\n"
      "s_waitcnt vmcnt(9)\n"
      "s_barrier\n"
      "s_add_u32 m0, s64, 44544\n"
      "s_add_u32 s90, s68, 49152\n"
      "buffer_load_dwordx4 v8, s[40:43], s68 offen lds\n"
      "buffer_load_dwordx4 v9, s[40:43], s68 offen offset:1024 lds\n"
      "buffer_load_dwordx4 v10, s[40:43], s68 offen offset:2048 lds\n"
      "s_cmp_lg_u32 s90, 1966080\n"
      "s_cselect_b32 s68, s90, 0\n"
      "s_waitcnt vmcnt(9)\n"
      "s_barrier\n"
      "s_add_u32 m0, s64, 59392\n"
      "s_add_u32 s90, s68, 49152\n"
      "buffer_load_dwordx4 v8, s[40:43], s68 offen lds\n"
      "buffer_load_dwordx4 v9, s[40:43], s68 offen offset:1024 lds\n"
      "buffer_load_dwordx4 v10, s[40:43], s68 offen offset:2048 lds\n"
      "s_cmp_lg_u32 s90, 1966080\n"
      "s_cselect_b32 s68, s90, 0\n"
      "s_waitcnt vmcnt(9)\n"
      "s_barrier\n"
      "s_add_u32 m0, s64, 0\n"
      "s_add_u32 s90, s68, 49152\n"
      "buffer_load_dwordx4 v8, s[40:43], s68 offen lds\n"
      "buffer_load_dwordx4 v9, s[40:43], s68 offen offset:1024 lds\n"
      "buffer_load_dwordx4 v10, s[40:43], s68 offen offset:2048 lds\n"
      "s_cmp_lg_u32 s90, 1966080\n"
      "s_cselect_b32 s68, s90, 0\n"
      "global_load_dwordx4 v[224:227], v19, s[54:55] sc1\n"
      "s_waitcnt vmcnt(10)\n"
      "s_barrier\n"
      "s_add_u32 m0, s64, 14848\n"
      "s_add_u32 s90, s68, 49152\n"
      "buffer_load_dwordx4 v8, s[40:43], s68 offen lds\n"
      "buffer_load_dwordx4 v9, s[40:43], s68 offen offset:1024 lds\n"
      "buffer_load_dwordx4 v10, s[40:43], s68 offen offset:2048 lds\n"
      "s_cmp_lg_u32 s90, 1966080\n"
      "s_cselect_b32 s68, s90, 0\n"
      "global_load_dwordx4 v[228:231], v19, s[54:55] offset:1024 sc1\n"
      "s_waitcnt vmcnt(11)\n"
      "s_barrier\n"
      "s_add_u32 m0, s64, 29696\n"
      "s_add_u32 s90, s68, 49152\n"
      "buffer_load_dwordx4 v8, s[40:43], s68 offen lds\n"
      "buffer_load_dwordx4 v9, s[40:43], s68 offen offset:1024 lds\n"
      "buffer_load_dwordx4 v10, s[40:43], s68 offen offset:2048 lds\n"
      "s_cmp_lg_u32 s90, 1966080\n"
      "s_cselect_b32 s68, s90, 0\n"
      "s_waitcnt vmcnt(11)\n"
      "s_barrier\n"
      "s_add_u32 m0, s64, 44544\n"
      "s_add_u32 s90, s68, 49152\n"
      "buffer_load_dwordx4 v8, s[40:43], s68 offen lds\n"
      "buffer_load_dwordx4 v9, s[40:43], s68 offen offset:1024 lds\n"
      "buffer_load_dwordx4 v10, s[40:43], s68 offen offset:2048 lds\n"
      "s_cmp_lg_u32 s90, 1966080\n"
      "s_cselect_b32 s68, s90, 0\n"
      "s_waitcnt vmcnt(11)\n"
      "s_barrier\n"
      "s_add_u32 m0, s64, 59392\n"
      "s_add_u32 s90, s68, 49152\n"
      "buffer_load_dwordx4 v8, s[40:43], s68 offen lds\n"
      "buffer_load_dwordx4 v9, s[40:43], s68 offen offset:1024 lds\n"
      "buffer_load_dwordx4 v10, s[40:43], s68 offen offset:2048 lds\n"
      "s_cmp_lg_u32 s90, 1966080\n"
      "s_cselect_b32 s68, s90, 0\n"
      "s_waitcnt vmcnt(10)\n"
      "s_barrier\n"
      "s_add_u32 m0, s64, 0\n"
      "s_add_u32 s90, s68, 49152\n"
      "buffer_load_dwordx4 v8, s[40:43], s68 offen lds\n"
      "buffer_load_dwordx4 v9, s[40:43], s68 offen offset:1024 lds\n"
      "buffer_load_dwordx4 v10, s[40:43], s68 offen offset:2048 lds\n"
      "s_cmp_lg_u32 s90, 1966080\n"
      "s_cselect_b32 s68, s90, 0\n"
      "s_waitcnt vmcnt(9)\n"
      "s_barrier\n"
      "s_add_u32 m0, s64, 14848\n"
      "s_add_u32 s90, s68, 49152\n"
      "buffer_load_dwordx4 v8, s[40:43], s68 offen lds\n"
      "buffer_load_dwordx4 v9, s[40:43], s68 offen offset:1024 lds\n"
      "buffer_load_dwordx4 v10, s[40:43], s68 offen offset:2048 lds\n"
      "s_cmp_lg_u32 s90, 1966080\n"
      "s_cselect_b32 s68, s90, 0\n"
      "v_cmp_eq_u32 vcc, v224, v18\n"
      "v_cmp_eq_u32 s[92:93], v226, v18\n"
      "s_and_b64 vcc, vcc, s[92:93]\n"
      "s_waitcnt vmcnt(9)\n"
      "s_barrier\n"
      "s_add_u32 m0, s64, 29696\n"
      "s_add_u32 s90, s68, 49152\n"
      "buffer_load_dwordx4 v8, s[40:43], s68 offen lds\n"
      "buffer_load_dwordx4 v9, s[40:43], s68 offen offset:1024 lds\n"
      "buffer_load_dwordx4 v10, s[40:43], s68 offen offset:2048 lds\n"
      "s_cmp_lg_u32 s90, 1966080\n"
      "s_cselect_b32 s68, s90, 0\n"
      "v_cmp_eq_u32 s[92:93], v228, v18\n"
      "s_and_b64 vcc, vcc, s[92:93]\n"
      "v_cmp_eq_u32 s[92:93], v230, v18\n"
      "s_and_b64 vcc, vcc, s[92:93]\n"
      "s_waitcnt vmcnt(9)\n"
      "s_barrier\n"
      "s_add_u32 m0, s64, 44544\n"
      "s_add_u32 s90, s68, 49152\n"
      "buffer_load_dwordx4 v8, s[40:43], s68 offen lds\n"
      "buffer_load_dwordx4 v9, s[40:43], s68 offen offset:1024 lds\n"
      "buffer_load_dwordx4 v10, s[40:43], s68 offen offset:2048 lds\n"
      "s_cmp_lg_u32 s90, 1966080\n"
      "s_cselect_b32 s68, s90, 0\n"
      "s_cmp_eq_u64 vcc, -1\n"
      "s_cbranch_scc1 11f\n"
      "s_mov_b32 s91, 0x20000\n"
      "9:\n"
      "s_sleep 8\n"
      "global_load_dwordx4 v[224:227], v19, s[54:55] sc1\n"
      "global_load_dwordx4 v[228:231], v19, s[54:55] offset:1024 sc1\n"
      "s_waitcnt vmcnt(0)\n"
      "v_cmp_eq_u32 vcc, v224, v18\n"
      "v_cmp_eq_u32 s[92:93], v226, v18\n"
      "s_and_b64 vcc, vcc, s[92:93]\n"
      "v_cmp_eq_u32 s[92:93], v228, v18\n"
      "s_and_b64 vcc, vcc, s[92:93]\n"
      "v_cmp_eq_u32 s[92:93], v230, v18\n"
      "s_and_b64 vcc, vcc, s[92:93]\n"
      "s_cmp_eq_u64 vcc, -1\n"
      "s_cbranch_scc1 11f\n"
      "s_sub_u32 s91, s91, 1\n"
      "s_cmp_lg_u32 s91, 0\n"
      "s_cbranch_scc1 9b\n"
      "11:\n"
      "s_waitcnt vmcnt(9)\n"
      "s_barrier\n"
      "s_add_u32 m0, s64, 59392\n"
      "s_add_u32 s90, s68, 49152\n"
      "buffer_load_dwordx4 v8, s[40:43], s68 offen lds\n"
      "buffer_load_dwordx4 v9, s[40:43], s68 offen offset:1024 lds\n"
      "buffer_load_dwordx4 v10, s[40:43], s68 offen offset:2048 lds\n"
      "s_cmp_lg_u32 s90, 1966080\n"
      "s_cselect_b32 s68, s90, 0\n"
      "10:\n"
      "s_waitcnt vmcnt(9)\n"
      "s_barrier\n"
      "s_mov_b32 m0, s70\n"
      "s_add_u32 s70, s70, 4096\n"
      "buffer_load_dwordx4 v19, s[48:51], s71 offen sc1 lds\n"
      "s_add_u32 s71, s71, 4096\n"
      "s_mov_b32 m0, s70\n"
      "s_add_u32 s70, s70, 4096\n"
      "buffer_load_dwordx4 v19, s[48:51], s71 offen sc1 lds\n"
      "s_add_u32 s71, s71, 4096\n"
      "s_mov_b32 m0, s70\n"
      "s_add_u32 s70, s70, 4096\n"
      "buffer_load_dwordx4 v19, s[48:51], s71 offen sc1 lds\n"
      "s_add_u32 s71, s71, 4096\n"
      "s_waitcnt vmcnt(9)\n"
      "s_barrier\n"
      "s_mov_b32 m0, s70\n"
      "s_add_u32 s70, s70, 4096\n"
      "buffer_load_dwordx4 v19, s[48:51], s71 offen sc1 lds\n"
      "s_add_u32 s71, s71, 4096\n"
      "s_mov_b32 m0, s70\n"
      "s_add_u32 s70, s70, 4096\n"
      "buffer_load_dwordx4 v19, s[48:51], s71 offen sc1 lds\n"
      "s_add_u32 s71, s71, 4096\n"
      "s_mov_b32 m0, s70\n"
      "s_add_u32 s70, s70, 4096\n"
      "buffer_load_dwordx4 v19, s[48:51], s71 offen sc1 lds\n"
      "s_add_u32 s71, s71, 4096\n"
      "s_mov_b32 m0, s70\n"
      "s_add_u32 s70, s70, 4096\n"
      "buffer_load_dwordx4 v19, s[48:51], s71 offen sc1 lds\n"
      "s_add_u32 s71, s71, 4096\n"
      "s_waitcnt vmcnt(9)\n"
      "s_barrier\n"
      "s_mov_b32 m0, s70\n"
      "s_add_u32 s70, s70, 4096\n"
      "buffer_load_dwordx4 v19, s[48:51], s71 offen sc1 lds\n"
      "s_add_u32 s71, s71, 4096\n"
      "s_mov_b32 m0, s70\n"
      "s_add_u32 s70, s70, 4096\n"
      "buffer_load_dwordx4 v19, s[48:51], s71 offen sc1 lds\n"
      "s_add_u32 s71, s71, 4096\n"
      "s_mov_b32 m0, s70\n"
      "s_add_u32 s70, s70, 4096\n"
      "buffer_load_dwordx4 v19, s[48:51], s71 offen sc1 lds\n"
      "s_add_u32 s71, s71, 4096\n"
      "s_waitcnt vmcnt(9)\n"
      "s_barrier\n"
      "s_cmp_lg_u32 s71, 40960\n"
      "s_cbranch_scc1 5f\n"
      "s_mov_b32 m0, s70\n"
      "s_add_u32 s70, s70, 4096\n"
      "buffer_load_dwordx4 v19, s[48:51], s71 offen sc1 lds\n"
      "s_add_u32 s71, s71, 4096\n"
      "5:\n"
      "s_cmp_eq_u32 s78, 0\n"
      "s_cbranch_scc1 15f\n"
      "s_mov_b32 m0, s79\n"
      "s_nop 0\n"
      "global_load_lds_dword v21, s[76:77]\n"
      "15:\n"
      "s_barrier\n"
      "v_max_f32 v225, v225, v227\n"
      "v_max_f32 v229, v229, v231\n"
      "s_nop 0\n"
      "v_max_f32_dpp v225, v225, v225 quad_perm:[1,0,3,2] row_mask:0xf bank_mask:0xf\n"
      "v_max_f32_dpp v229, v229, v229 quad_perm:[1,0,3,2] row_mask:0xf bank_mask:0xf\n"
      "s_nop 0\n"
      "v_max_f32_dpp v225, v225, v225 quad_perm:[2,3,0,1] row_mask:0xf bank_mask:0xf\n"
      "v_max_f32_dpp v229, v229, v229 quad_perm:[2,3,0,1] row_mask:0xf bank_mask:0xf\n"
      "s_nop 0\n"
      "v_max_f32_dpp v225, v225, v225 row_half_mirror row_mask:0xf bank_mask:0xf\n"
      "v_max_f32_dpp v229, v229, v229 row_half_mirror row_mask:0xf bank_mask:0xf\n"
      "s_nop 0\n"
      "v_max_f32_dpp v225, v225, v225 row_mirror row_mask:0xf bank_mask:0xf\n"
      "v_max_f32_dpp v229, v229, v229 row_mirror row_mask:0xf bank_mask:0xf\n"
      "v_mov_b32 v224, v225\n"
      "v_mov_b32 v228, v229\n"
      "s_nop 1\n"
      "v_permlane16_swap_b32 v225, v224\n"
      "v_permlane16_swap_b32 v229, v228\n"
      "v_max_f32 v225, v225, v224\n"
      "v_max_f32 v229, v229, v228\n"
      "v_mov_b32 v224, v225\n"
      "v_mov_b32 v228, v229\n"
      "s_nop 1\n"
      "v_permlane32_swap_b32 v225, v224\n"
      "v_permlane32_swap_b32 v229, v228\n"
      "v_max_f32 v225, v225, v224\n"
      "v_max_f32 v229, v229, v228\n"
      "s_barrier\n"
      "v_max_f32 v225, 0x0da24260, v225\n"
      "v_rcp_f32 v225, v225\n"
      "v_cmp_ge_f32 vcc, 0x5368d4a5, v229\n"
      "v_mul_f32 v225, 0x45f0a000, v225\n"
      "s_mov_b32 s74, 83968\n"
      "v_cndmask_b32 v225, -1.0, v225, vcc\n"
      "s_cmp_lg_u32 s66, 7\n"
      "s_cbranch_scc1 19f\n"
      "ds_write_b32 v16, v225 offset:36\n"
      "19:\n"
      "v_add_u32 v249, s74, v27\n"
      "v_add_u32 v250, s74, v28\n"
      "v_add_u32 v251, s74, v29\n"
      "v_mov_b32 v252, 0xdddd\n"
      "s_mov_b32 s73, 4\n"
      "s_mov_b32 s75, 0\n"
      "s_cmp_lg_u32 s78, 0\n"
      "s_cbranch_scc1 16f\n"
      "s_waitcnt vmcnt(0)\n"
      "16:\n"
      "s_waitcnt vmcnt(1) lgkmcnt(0)\n"
      "s_barrier\n"
      "ds_read_b128 v[192:195], v249 offset:0\n"
      "ds_read_b64 v[204:205], v250 offset:0\n"
      "ds_read_b128 v[196:199], v251 offset:0\n"
      "ds_read_b128 v[200:203], v251 offset:672\n"
      "s_mov_b32 exec_hi, 0\n"
      "ds_read_b64 v[56:57], v31 offset:0\n"
      "ds_read_b64 v[58:59], v31 offset:512\n"
      "ds_read_b64 v[60:61], v31 offset:1024\n"
      "ds_read_b64 v[62:63], v31 offset:1536\n"
      "s_mov_b32 exec_hi, -1\n"
      "ds_read_b128 v[40:43], v30 offset:0\n"
      "ds_read_b128 v[44:47], v30 offset:2048\n"
      "ds_read_b128 v[48:51], v30 offset:4096\n"
      "ds_read_b128 v[52:55], v30 offset:6144\n"
      "s_waitcnt lgkmcnt(3)\n"
      "v_mfma_f32_32x32x16_bf16 v[128:143], v[192:195], v[40:43], 0\n"
      "v_mfma_f32_32x32x8_bf16 v[128:143], v[204:205], v[56:57], v[128:143]\n"
      "ds_read_b128 v[206:209], v249 offset:2624\n"
      "ds_read_b64 v[218:219], v250 offset:2624\n"
      "ds_read_b128 v[210:213], v251 offset:2624\n"
      "ds_read_b128 v[214:217], v251 offset:3296\n"
      "s_waitcnt lgkmcnt(4)\n"
      "s_nop 7\n"
      "1:\n"
      "v_mfma_f32_32x32x16_bf16 v[144:159], v[192:195], v[44:47], 0\n"
      "v_exp_f32 v128, v128\n"
      "v_exp_f32 v129, v129\n"
      "v_exp_f32 v130, v130\n"
      "v_exp_f32 v131, v131\n"
      "v_mfma_f32_32x32x8_bf16 v[144:159], v[204:205], v[58:59], v[144:159]\n"
      "v_exp_f32 v132, v132\n"
      "v_exp_f32 v133, v133\n"
      "v_exp_f32 v134, v134\n"
      "v_exp_f32 v135, v135\n"
      "v_smfmac_f32_32x32x32_bf16 v[112:127], v[238:241], v[176:183], v252\n"
      "v_exp_f32 v136, v136\n"
      "v_exp_f32 v137, v137\n"
      "v_exp_f32 v138, v138\n"
      "v_exp_f32 v139, v139\n"
      "v_smfmac_f32_32x32x32_bf16 v[112:127], v[242:245], v[184:191], v252\n"
      "s_waitcnt lgkmcnt(0)\n"
      "ds_read_b128 v[220:223], v249 offset:5248\n"
      "ds_read_b64 v[232:233], v250 offset:5248\n"
      "ds_read_b128 v[224:227], v251 offset:5248\n"
      "ds_read_b128 v[228:231], v251 offset:5920\n"
      "v_exp_f32 v140, v140\n"
      "v_exp_f32 v141, v141\n"
      "v_exp_f32 v142, v142\n"
      "v_exp_f32 v143, v143\n"
      "v_mfma_f32_32x32x16_bf16 v[160:175], v[192:195], v[48:51], 0\n"
      "v_exp_f32 v144, v144\n"
      "v_exp_f32 v145, v145\n"
      "v_exp_f32 v146, v146\n"
      "v_exp_f32 v147, v147\n"
      "v_mfma_f32_32x32x8_bf16 v[160:175], v[204:205], v[60:61], v[160:175]\n"
      "v_exp_f32 v148, v148\n"
      "v_exp_f32 v149, v149\n"
      "v_exp_f32 v150, v150\n"
      "v_exp_f32 v151, v151\n"
      "v_smfmac_f32_32x32x32_bf16 v[64:79], v[196:199], v[128:135], v252\n"
      "v_exp_f32 v152, v152\n"
      "v_exp_f32 v153, v153\n"
      "v_exp_f32 v154, v154\n"
      "v_exp_f32 v155, v155\n"
      "v_smfmac_f32_32x32x32_bf16 v[64:79], v[200:203], v[136:143], v252\n"
      "v_exp_f32 v156, v156\n"
      "v_exp_f32 v157, v157\n"
      "v_exp_f32 v158, v158\n"
      "v_exp_f32 v159, v159\n"
      "v_mfma_f32_32x32x16_bf16 v[176:191], v[192:195], v[52:55], 0\n"
      "v_exp_f32 v160, v160\n"
      "v_exp_f32 v161, v161\n"
      "v_exp_f32 v162, v162\n"
      "v_exp_f32 v163, v163\n"
      "v_mfma_f32_32x32x8_bf16 v[176:191], v[204:205], v[62:63], v[176:191]\n"
      "v_exp_f32 v164, v164\n"
      "v_exp_f32 v165, v165\n"
      "v_exp_f32 v166, v166\n"
      "v_exp_f32 v167, v167\n"
      "v_smfmac_f32_32x32x32_bf16 v[80:95], v[196:199], v[144:151], v252\n"
      "v_exp_f32 v168, v168\n"
      "v_exp_f32 v169, v169\n"
      "v_exp_f32 v170, v170\n"
      "v_exp_f32 v171, v171\n"
      "v_smfmac_f32_32x32x32_bf16 v[80:95], v[200:203], v[152:159], v252\n"
      "v_exp_f32 v172, v172\n"
      "v_exp_f32 v173, v173\n"
      "v_exp_f32 v174, v174\n"
      "v_exp_f32 v175, v175\n"
      "v_mfma_f32_32x32x16_bf16 v[128:143], v[206:209], v[40:43], 0\n"
      "v_exp_f32 v176, v176\n"
      "v_exp_f32 v177, v177\n"
      "v_exp_f32 v178, v178\n"
      "v_exp_f32 v179, v179\n"
      "v_mfma_f32_32x32x8_bf16 v[128:143], v[218:219], v[56:57], v[128:143]\n"
      "v_exp_f32 v180, v180\n"
      "v_exp_f32 v181, v181\n"
      "v_exp_f32 v182, v182\n"
      "v_exp_f32 v183, v183\n"
      "v_smfmac_f32_32x32x32_bf16 v[96:111], v[196:199], v[160:167], v252\n"
      "v_exp_f32 v184, v184\n"
      "v_exp_f32 v185, v185\n"
      "v_exp_f32 v186, v186\n"
      "v_exp_f32 v187, v187\n"
      "v_smfmac_f32_32x32x32_bf16 v[96:111], v[200:203], v[168:175], v252\n"
      "v_exp_f32 v188, v188\n"
      "v_exp_f32 v189, v189\n"
      "v_exp_f32 v190, v190\n"
      "v_exp_f32 v191, v191\n"
      "v_mfma_f32_32x32x16_bf16 v[144:159], v[206:209], v[44:47], 0\n"
      "v_exp_f32 v128, v128\n"
      "v_exp_f32 v129, v129\n"
      "v_exp_f32 v130, v130\n"
      "v_exp_f32 v131, v131\n"
      "v_mfma_f32_32x32x8_bf16 v[144:159], v[218:219], v[58:59], v[144:159]\n"
      "v_exp_f32 v132, v132\n"
      "v_exp_f32 v133, v133\n"
      "v_exp_f32 v134, v134\n"
      "v_exp_f32 v135, v135\n"
      "v_smfmac_f32_32x32x32_bf16 v[112:127], v[196:199], v[176:183], v252\n"
      "v_exp_f32 v136, v136\n"
      "v_exp_f32 v137, v137\n"
      "v_exp_f32 v138, v138\n"
      "v_exp_f32 v139, v139\n"
      "v_smfmac_f32_32x32x32_bf16 v[112:127], v[200:203], v[184:191], v252\n"
      "s_waitcnt lgkmcnt(0)\n"
      "ds_read_b128 v[234:237], v249 offset:7872\n"
      "ds_read_b64 v[246:247], v250 offset:7872\n"
      "ds_read_b128 v[238:241], v251 offset:7872\n"
      "ds_read_b128 v[242:245], v251 offset:8544\n"
      "v_exp_f32 v140, v140\n"
      "v_exp_f32 v141, v141\n"
      "v_exp_f32 v142, v142\n"
      "v_exp_f32 v143, v143\n"
      "v_mfma_f32_32x32x16_bf16 v[160:175], v[206:209], v[48:51], 0\n"
      "v_exp_f32 v144, v144\n"
      "v_exp_f32 v145, v145\n"
      "v_exp_f32 v146, v146\n"
      "v_exp_f32 v147, v147\n"
      "v_mfma_f32_32x32x8_bf16 v[160:175], v[218:219], v[60:61], v[160:175]\n"
      "v_exp_f32 v148, v148\n"
      "v_exp_f32 v149, v149\n"
      "v_exp_f32 v150, v150\n"
      "v_exp_f32 v151, v151\n"
      "v_smfmac_f32_32x32x32_bf16 v[64:79], v[210:213], v[128:135], v252\n"
      "v_exp_f32 v152, v152\n"
      "v_exp_f32 v153, v153\n"
      "v_exp_f32 v154, v154\n"
      "v_exp_f32 v155, v155\n"
      "v_smfmac_f32_32x32x32_bf16 v[64:79], v[214:217], v[136:143], v252\n"
      "v_exp_f32 v156, v156\n"
      "v_exp_f32 v157, v157\n"
      "v_exp_f32 v158, v158\n"
      "v_exp_f32 v159, v159\n"
      "v_mfma_f32_32x32x16_bf16 v[176:191], v[206:209], v[52:55], 0\n"
      "v_exp_f32 v160, v160\n"
      "v_exp_f32 v161, v161\n"
      "v_exp_f32 v162, v162\n"
      "v_exp_f32 v163, v163\n"
      "v_mfma_f32_32x32x8_bf16 v[176:191], v[218:219], v[62:63], v[176:191]\n"
      "v_exp_f32 v164, v164\n"
      "v_exp_f32 v165, v165\n"
      "v_exp_f32 v166, v166\n"
      "v_exp_f32 v167, v167\n"
      "v_smfmac_f32_32x32x32_bf16 v[80:95], v[210:213], v[144:151], v252\n"
      "v_exp_f32 v168, v168\n"
      "v_exp_f32 v169, v169\n"
      "v_exp_f32 v170, v170\n"
      "v_exp_f32 v171, v171\n"
      "v_smfmac_f32_32x32x32_bf16 v[80:95], v[214:217], v[152:159], v252\n"
      "v_exp_f32 v172, v172\n"
      "v_exp_f32 v173, v173\n"
      "v_exp_f32 v174, v174\n"
      "v_exp_f32 v175, v175\n"
      "v_mfma_f32_32x32x16_bf16 v[128:143], v[220:223], v[40:43], 0\n"
      "v_exp_f32 v176, v176\n"
      "v_exp_f32 v177, v177\n"
      "v_exp_f32 v178, v178\n"
      "v_exp_f32 v179, v179\n"
      "v_mfma_f32_32x32x8_bf16 v[128:143], v[232:233], v[56:57], v[128:143]\n"
      "v_exp_f32 v180, v180\n"
      "v_exp_f32 v181, v181\n"
      "v_exp_f32 v182, v182\n"
      "v_exp_f32 v183, v183\n"
      "v_smfmac_f32_32x32x32_bf16 v[96:111], v[210:213], v[160:167], v252\n"
      "v_exp_f32 v184, v184\n"
      "v_exp_f32 v185, v185\n"
      "v_exp_f32 v186, v186\n"
      "v_exp_f32 v187, v187\n"
      "v_smfmac_f32_32x32x32_bf16 v[96:111], v[214:217], v[168:175], v252\n"
      "v_exp_f32 v188, v188\n"
      "v_exp_f32 v189, v189\n"
      "v_exp_f32 v190, v190\n"
      "v_exp_f32 v191, v191\n"
      "v_mfma_f32_32x32x16_bf16 v[144:159], v[220:223], v[44:47], 0\n"
      "v_exp_f32 v128, v128\n"
      "v_exp_f32 v129, v129\n"
      "v_exp_f32 v130, v130\n"
      "v_exp_f32 v131, v131\n"
      "v_mfma_f32_32x32x8_bf16 v[144:159], v[232:233], v[58:59], v[144:159]\n"
      "v_exp_f32 v132, v132\n"
      "v_exp_f32 v133, v133\n"
      "v_exp_f32 v134, v134\n"
      "v_exp_f32 v135, v135\n"
      "v_smfmac_f32_32x32x32_bf16 v[112:127], v[210:213], v[176:183], v252\n"
      "v_exp_f32 v136, v136\n"
      "v_exp_f32 v137, v137\n"
      "v_exp_f32 v138, v138\n"
      "v_exp_f32 v139, v139\n"
      "v_smfmac_f32_32x32x32_bf16 v[112:127], v[214:217], v[184:191], v252\n"
      "s_waitcnt lgkmcnt(0)\n"
      "ds_read_b128 v[192:195], v249 offset:10496\n"
      "ds_read_b64 v[204:205], v250 offset:10496\n"
      "ds_read_b128 v[196:199], v251 offset:10496\n"
      "ds_read_b128 v[200:203], v251 offset:11168\n"
      "v_exp_f32 v140, v140\n"
      "v_exp_f32 v141, v141\n"
      "v_exp_f32 v142, v142\n"
      "v_exp_f32 v143, v143\n"
      "v_mfma_f32_32x32x16_bf16 v[160:175], v[220:223], v[48:51], 0\n"
      "v_exp_f32 v144, v144\n"
      "v_exp_f32 v145, v145\n"
      "v_exp_f32 v146, v146\n"
      "v_exp_f32 v147, v147\n"
      "v_mfma_f32_32x32x8_bf16 v[160:175], v[232:233], v[60:61], v[160:175]\n"
      "v_exp_f32 v148, v148\n"
      "v_exp_f32 v149, v149\n"
      "v_exp_f32 v150, v150\n"
      "v_exp_f32 v151, v151\n"
      "v_smfmac_f32_32x32x32_bf16 v[64:79], v[224:227], v[128:135], v252\n"
      "v_exp_f32 v152, v152\n"
      "v_exp_f32 v153, v153\n"
      "v_exp_f32 v154, v154\n"
      "v_exp_f32 v155, v155\n"
      "v_smfmac_f32_32x32x32_bf16 v[64:79], v[228:231], v[136:143], v252\n"
      "v_exp_f32 v156, v156\n"
      "v_exp_f32 v157, v157\n"
      "v_exp_f32 v158, v158\n"
      "v_exp_f32 v159, v159\n"
      "v_mfma_f32_32x32x16_bf16 v[176:191], v[220:223], v[52:55], 0\n"
      "v_exp_f32 v160, v160\n"
      "v_exp_f32 v161, v161\n"
      "v_exp_f32 v162, v162\n"
      "v_exp_f32 v163, v163\n"
      "v_mfma_f32_32x32x8_bf16 v[176:191], v[232:233], v[62:63], v[176:191]\n"
      "v_exp_f32 v164, v164\n"
      "v_exp_f32 v165, v165\n"
      "v_exp_f32 v166, v166\n"
      "v_exp_f32 v167, v167\n"
      "v_smfmac_f32_32x32x32_bf16 v[80:95], v[224:227], v[144:151], v252\n"
      "v_exp_f32 v168, v168\n"
      "v_exp_f32 v169, v169\n"
      "v_exp_f32 v170, v170\n"
      "v_exp_f32 v171, v171\n"
      "v_smfmac_f32_32x32x32_bf16 v[80:95], v[228:231], v[152:159], v252\n"
      "v_exp_f32 v172, v172\n"
      "v_exp_f32 v173, v173\n"
      "v_exp_f32 v174, v174\n"
      "v_exp_f32 v175, v175\n"
      "v_mfma_f32_32x32x16_bf16 v[128:143], v[234:237], v[40:43], 0\n"
      "v_exp_f32 v176, v176\n"
      "v_exp_f32 v177, v177\n"
      "v_exp_f32 v178, v178\n"
      "v_exp_f32 v179, v179\n"
      "v_mfma_f32_32x32x8_bf16 v[128:143], v[246:247], v[56:57], v[128:143]\n"
      "v_exp_f32 v180, v180\n"
      "v_exp_f32 v181, v181\n"
      "v_exp_f32 v182, v182\n"
      "v_exp_f32 v183, v183\n"
      "v_smfmac_f32_32x32x32_bf16 v[96:111], v[224:227], v[160:167], v252\n"
      "v_exp_f32 v184, v184\n"
      "v_exp_f32 v185, v185\n"
      "v_exp_f32 v186, v186\n"
      "v_exp_f32 v187, v187\n"
      "v_smfmac_f32_32x32x32_bf16 v[96:111], v[228:231], v[168:175], v252\n"
      "v_exp_f32 v188, v188\n"
      "v_exp_f32 v189, v189\n"
      "v_exp_f32 v190, v190\n"
      "v_exp_f32 v191, v191\n"
      "v_mfma_f32_32x32x16_bf16 v[144:159], v[234:237], v[44:47], 0\n"
      "v_exp_f32 v128, v128\n"
      "v_exp_f32 v129, v129\n"
      "v_exp_f32 v130, v130\n"
      "v_exp_f32 v131, v131\n"
      "v_mfma_f32_32x32x8_bf16 v[144:159], v[246:247], v[58:59], v[144:159]\n"
      "v_exp_f32 v132, v132\n"
      "v_exp_f32 v133, v133\n"
      "v_exp_f32 v134, v134\n"
      "v_exp_f32 v135, v135\n"
      "v_smfmac_f32_32x32x32_bf16 v[112:127], v[224:227], v[176:183], v252\n"
      "v_exp_f32 v136, v136\n"
      "v_exp_f32 v137, v137\n"
      "v_exp_f32 v138, v138\n"
      "v_exp_f32 v139, v139\n"
      "v_smfmac_f32_32x32x32_bf16 v[112:127], v[228:231], v[184:191], v252\n"
      "s_waitcnt lgkmcnt(0)\n"
      "ds_read_b128 v[206:209], v249 offset:13120\n"
      "ds_read_b64 v[218:219], v250 offset:13120\n"
      "ds_read_b128 v[210:213], v251 offset:13120\n"
      "ds_read_b128 v[214:217], v251 offset:13792\n"
      "v_exp_f32 v140, v140\n"
      "v_exp_f32 v141, v141\n"
      "v_exp_f32 v142, v142\n"
      "v_exp_f32 v143, v143\n"
      "v_mfma_f32_32x32x16_bf16 v[160:175], v[234:237], v[48:51], 0\n"
      "v_exp_f32 v144, v144\n"
      "v_exp_f32 v145, v145\n"
      "v_exp_f32 v146, v146\n"
      "v_exp_f32 v147, v147\n"
      "v_mfma_f32_32x32x8_bf16 v[160:175], v[246:247], v[60:61], v[160:175]\n"
      "v_exp_f32 v148, v148\n"
      "v_exp_f32 v149, v149\n"
      "v_exp_f32 v150, v150\n"
      "v_exp_f32 v151, v151\n"
      "v_smfmac_f32_32x32x32_bf16 v[64:79], v[238:241], v[128:135], v252\n"
      "v_exp_f32 v152, v152\n"
      "v_exp_f32 v153, v153\n"
      "v_exp_f32 v154, v154\n"
      "v_exp_f32 v155, v155\n"
      "v_smfmac_f32_32x32x32_bf16 v[64:79], v[242:245], v[136:143], v252\n"
      "v_exp_f32 v156, v156\n"
      "v_exp_f32 v157, v157\n"
      "v_exp_f32 v158, v158\n"
      "v_exp_f32 v159, v159\n"
      "v_mfma_f32_32x32x16_bf16 v[176:191], v[234:237], v[52:55], 0\n"
      "v_exp_f32 v160, v160\n"
      "v_exp_f32 v161, v161\n"
      "v_exp_f32 v162, v162\n"
      "v_exp_f32 v163, v163\n"
      "v_mfma_f32_32x32x8_bf16 v[176:191], v[246:247], v[62:63], v[176:191]\n"
      "v_exp_f32 v164, v164\n"
      "v_exp_f32 v165, v165\n"
      "v_exp_f32 v166, v166\n"
      "v_exp_f32 v167, v167\n"
      "v_smfmac_f32_32x32x32_bf16 v[80:95], v[238:241], v[144:151], v252\n"
      "v_exp_f32 v168, v168\n"
      "v_exp_f32 v169, v169\n"
      "v_exp_f32 v170, v170\n"
      "v_exp_f32 v171, v171\n"
      "v_smfmac_f32_32x32x32_bf16 v[80:95], v[242:245], v[152:159], v252\n"
      "v_exp_f32 v172, v172\n"
      "v_exp_f32 v173, v173\n"
      "v_exp_f32 v174, v174\n"
      "v_exp_f32 v175, v175\n"
      "v_mfma_f32_32x32x16_bf16 v[128:143], v[192:195], v[40:43], 0\n"
      "v_exp_f32 v176, v176\n"
      "v_exp_f32 v177, v177\n"
      "v_exp_f32 v178, v178\n"
      "v_exp_f32 v179, v179\n"
      "v_mfma_f32_32x32x8_bf16 v[128:143], v[204:205], v[56:57], v[128:143]\n"
      "v_exp_f32 v180, v180\n"
      "v_exp_f32 v181, v181\n"
      "v_exp_f32 v182, v182\n"
      "v_exp_f32 v183, v183\n"
      "v_smfmac_f32_32x32x32_bf16 v[96:111], v[238:241], v[160:167], v252\n"
      "v_exp_f32 v184, v184\n"
      "v_exp_f32 v185, v185\n"
      "v_exp_f32 v186, v186\n"
      "v_exp_f32 v187, v187\n"
      "v_smfmac_f32_32x32x32_bf16 v[96:111], v[242:245], v[168:175], v252\n"
      "v_exp_f32 v188, v188\n"
      "v_exp_f32 v189, v189\n"
      "v_exp_f32 v190, v190\n"
      "v_exp_f32 v191, v191\n"
      "v_mfma_f32_32x32x16_bf16 v[144:159], v[192:195], v[44:47], 0\n"
      "v_exp_f32 v128, v128\n"
      "v_exp_f32 v129, v129\n"
      "v_exp_f32 v130, v130\n"
      "v_exp_f32 v131, v131\n"
      "v_mfma_f32_32x32x8_bf16 v[144:159], v[204:205], v[58:59], v[144:159]\n"
      "v_exp_f32 v132, v132\n"
      "v_exp_f32 v133, v133\n"
      "v_exp_f32 v134, v134\n"
      "v_exp_f32 v135, v135\n"
      "v_smfmac_f32_32x32x32_bf16 v[112:127], v[238:241], v[176:183], v252\n"
      "v_exp_f32 v136, v136\n"
      "v_exp_f32 v137, v137\n"
      "v_exp_f32 v138, v138\n"
      "v_exp_f32 v139, v139\n"
      "v_smfmac_f32_32x32x32_bf16 v[112:127], v[242:245], v[184:191], v252\n"
      "s_waitcnt lgkmcnt(0)\n"
      "ds_read_b128 v[220:223], v249 offset:15744\n"
      "ds_read_b64 v[232:233], v250 offset:15744\n"
      "ds_read_b128 v[224:227], v251 offset:15744\n"
      "ds_read_b128 v[228:231], v251 offset:16416\n"
      "v_exp_f32 v140, v140\n"
      "v_exp_f32 v141, v141\n"
      "v_exp_f32 v142, v142\n"
      "v_exp_f32 v143, v143\n"
      "v_mfma_f32_32x32x16_bf16 v[160:175], v[192:195], v[48:51], 0\n"
      "v_exp_f32 v144, v144\n"
      "v_exp_f32 v145, v145\n"
      "v_exp_f32 v146, v146\n"
      "v_exp_f32 v147, v147\n"
      "v_mfma_f32_32x32x8_bf16 v[160:175], v[204:205], v[60:61], v[160:175]\n"
      "v_exp_f32 v148, v148\n"
      "v_exp_f32 v149, v149\n"
      "v_exp_f32 v150, v150\n"
      "v_exp_f32 v151, v151\n"
      "v_smfmac_f32_32x32x32_bf16 v[64:79], v[196:199], v[128:135], v252\n"
      "v_exp_f32 v152, v152\n"
      "v_exp_f32 v153, v153\n"
      "v_exp_f32 v154, v154\n"
      "v_exp_f32 v155, v155\n"
      "v_smfmac_f32_32x32x32_bf16 v[64:79], v[200:203], v[136:143], v252\n"
      "v_exp_f32 v156, v156\n"
      "v_exp_f32 v157, v157\n"
      "v_exp_f32 v158, v158\n"
      "v_exp_f32 v159, v159\n"
      "v_mfma_f32_32x32x16_bf16 v[176:191], v[192:195], v[52:55], 0\n"
      "v_exp_f32 v160, v160\n"
      "v_exp_f32 v161, v161\n"
      "v_exp_f32 v162, v162\n"
      "v_exp_f32 v163, v163\n"
      "v_mfma_f32_32x32x8_bf16 v[176:191], v[204:205], v[62:63], v[176:191]\n"
      "v_exp_f32 v164, v164\n"
      "v_exp_f32 v165, v165\n"
      "v_exp_f32 v166, v166\n"
      "v_exp_f32 v167, v167\n"
      "v_smfmac_f32_32x32x32_bf16 v[80:95], v[196:199], v[144:151], v252\n"
      "v_exp_f32 v168, v168\n"
      "v_exp_f32 v169, v169\n"
      "v_exp_f32 v170, v170\n"
      "v_exp_f32 v171, v171\n"
      "v_smfmac_f32_32x32x32_bf16 v[80:95], v[200:203], v[152:159], v252\n"
      "v_exp_f32 v172, v172\n"
      "v_exp_f32 v173, v173\n"
      "v_exp_f32 v174, v174\n"
      "v_exp_f32 v175, v175\n"
      "v_mfma_f32_32x32x16_bf16 v[128:143], v[206:209], v[40:43], 0\n"
      "v_exp_f32 v176, v176\n"
      "v_exp_f32 v177, v177\n"
      "v_exp_f32 v178, v178\n"
      "v_exp_f32 v179, v179\n"
      "v_mfma_f32_32x32x8_bf16 v[128:143], v[218:219], v[56:57], v[128:143]\n"
      "v_exp_f32 v180, v180\n"
      "v_exp_f32 v181, v181\n"
      "v_exp_f32 v182, v182\n"
      "v_exp_f32 v183, v183\n"
      "v_smfmac_f32_32x32x32_bf16 v[96:111], v[196:199], v[160:167], v252\n"
      "v_exp_f32 v184, v184\n"
      "v_exp_f32 v185, v185\n"
      "v_exp_f32 v186, v186\n"
      "v_exp_f32 v187, v187\n"
      "v_smfmac_f32_32x32x32_bf16 v[96:111], v[200:203], v[168:175], v252\n"
      "v_exp_f32 v188, v188\n"
      "v_exp_f32 v189, v189\n"
      "v_exp_f32 v190, v190\n"
      "v_exp_f32 v191, v191\n"
      "v_mfma_f32_32x32x16_bf16 v[144:159], v[206:209], v[44:47], 0\n"
      "v_exp_f32 v128, v128\n"
      "v_exp_f32 v129, v129\n"
      "v_exp_f32 v130, v130\n"
      "v_exp_f32 v131, v131\n"
      "v_mfma_f32_32x32x8_bf16 v[144:159], v[218:219], v[58:59], v[144:159]\n"
      "v_exp_f32 v132, v132\n"
      "v_exp_f32 v133, v133\n"
      "v_exp_f32 v134, v134\n"
      "v_exp_f32 v135, v135\n"
      "v_smfmac_f32_32x32x32_bf16 v[112:127], v[196:199], v[176:183], v252\n"
      "v_exp_f32 v136, v136\n"
      "v_exp_f32 v137, v137\n"
      "v_exp_f32 v138, v138\n"
      "v_exp_f32 v139, v139\n"
      "v_smfmac_f32_32x32x32_bf16 v[112:127], v[200:203], v[184:191], v252\n"
      "s_waitcnt lgkmcnt(0)\n"
      "ds_read_b128 v[234:237], v249 offset:18368\n"
      "ds_read_b64 v[246:247], v250 offset:18368\n"
      "ds_read_b128 v[238:241], v251 offset:18368\n"
      "ds_read_b128 v[242:245], v251 offset:19040\n"
      "ds_read_b32 v248, v16\n"
      "v_exp_f32 v140, v140\n"
      "v_exp_f32 v141, v141\n"
      "v_exp_f32 v142, v142\n"
      "v_exp_f32 v143, v143\n"
      "v_mfma_f32_32x32x16_bf16 v[160:175], v[206:209], v[48:51], 0\n"
      "v_exp_f32 v144, v144\n"
      "v_exp_f32 v145, v145\n"
      "v_exp_f32 v146, v146\n"
      "v_exp_f32 v147, v147\n"
      "v_mfma_f32_32x32x8_bf16 v[160:175], v[218:219], v[60:61], v[160:175]\n"
      "v_exp_f32 v148, v148\n"
      "v_exp_f32 v149, v149\n"
      "v_exp_f32 v150, v150\n"
      "v_exp_f32 v151, v151\n"
      "v_smfmac_f32_32x32x32_bf16 v[64:79], v[210:213], v[128:135], v252\n"
      "v_exp_f32 v152, v152\n"
      "v_exp_f32 v153, v153\n"
      "v_exp_f32 v154, v154\n"
      "v_exp_f32 v155, v155\n"
      "v_smfmac_f32_32x32x32_bf16 v[64:79], v[214:217], v[136:143], v252\n"
      "v_exp_f32 v156, v156\n"
      "v_exp_f32 v157, v157\n"
      "v_exp_f32 v158, v158\n"
      "v_exp_f32 v159, v159\n"
      "v_mfma_f32_32x32x16_bf16 v[176:191], v[206:209], v[52:55], 0\n"
      "v_exp_f32 v160, v160\n"
      "v_exp_f32 v161, v161\n"
      "v_exp_f32 v162, v162\n"
      "v_exp_f32 v163, v163\n"
      "v_mfma_f32_32x32x8_bf16 v[176:191], v[218:219], v[62:63], v[176:191]\n"
      "v_exp_f32 v164, v164\n"
      "v_exp_f32 v165, v165\n"
      "v_exp_f32 v166, v166\n"
      "v_exp_f32 v167, v167\n"
      "v_smfmac_f32_32x32x32_bf16 v[80:95], v[210:213], v[144:151], v252\n"
      "v_exp_f32 v168, v168\n"
      "v_exp_f32 v169, v169\n"
      "v_exp_f32 v170, v170\n"
      "v_exp_f32 v171, v171\n"
      "v_smfmac_f32_32x32x32_bf16 v[80:95], v[214:217], v[152:159], v252\n"
      "v_exp_f32 v172, v172\n"
      "v_exp_f32 v173, v173\n"
      "v_exp_f32 v174, v174\n"
      "v_exp_f32 v175, v175\n"
      "v_mfma_f32_32x32x16_bf16 v[128:143], v[220:223], v[40:43], 0\n"
      "v_exp_f32 v176, v176\n"
      "v_exp_f32 v177, v177\n"
      "v_exp_f32 v178, v178\n"
      "v_exp_f32 v179, v179\n"
      "v_mfma_f32_32x32x8_bf16 v[128:143], v[232:233], v[56:57], v[128:143]\n"
      "v_exp_f32 v180, v180\n"
      "v_exp_f32 v181, v181\n"
      "v_exp_f32 v182, v182\n"
      "v_exp_f32 v183, v183\n"
      "v_smfmac_f32_32x32x32_bf16 v[96:111], v[210:213], v[160:167], v252\n"
      "v_exp_f32 v184, v184\n"
      "v_exp_f32 v185, v185\n"
      "v_exp_f32 v186, v186\n"
      "v_exp_f32 v187, v187\n"
      "v_smfmac_f32_32x32x32_bf16 v[96:111], v[214:217], v[168:175], v252\n"
      "v_exp_f32 v188, v188\n"
      "v_exp_f32 v189, v189\n"
      "v_exp_f32 v190, v190\n"
      "v_exp_f32 v191, v191\n"
      "v_mfma_f32_32x32x16_bf16 v[144:159], v[220:223], v[44:47], 0\n"
      "v_exp_f32 v128, v128\n"
      "v_exp_f32 v129, v129\n"
      "v_exp_f32 v130, v130\n"
      "v_exp_f32 v131, v131\n"
      "v_mfma_f32_32x32x8_bf16 v[144:159], v[232:233], v[58:59], v[144:159]\n"
      "v_exp_f32 v132, v132\n"
      "v_exp_f32 v133, v133\n"
      "v_exp_f32 v134, v134\n"
      "v_exp_f32 v135, v135\n"
      "v_smfmac_f32_32x32x32_bf16 v[112:127], v[210:213], v[176:183], v252\n"
      "v_exp_f32 v136, v136\n"
      "v_exp_f32 v137, v137\n"
      "v_exp_f32 v138, v138\n"
      "v_exp_f32 v139, v139\n"
      "v_smfmac_f32_32x32x32_bf16 v[112:127], v[214:217], v[184:191], v252\n"
      "s_waitcnt lgkmcnt(0)\n"
      "s_mov_b64 exec, 1\n"
      "ds_add_u32 v16, v253 offset:16\n"
      "s_mov_b64 exec, -1\n"
      "v_readfirstlane_b32 s90, v248\n"
      "s_add_u32 s74, s74, 41984\n"
      "s_cmp_lg_u32 s74, 125952\n"
      "s_cselect_b32 s74, s74, 0\n"
      "v_add_u32 v249, s74, v27\n"
      "v_add_u32 v250, s74, v28\n"
      "v_add_u32 v251, s74, v29\n"
      "s_cmp_ge_u32 s90, 4\n"
      "s_cbranch_scc1 3f\n"
      "s_mov_b32 s91, 0x200000\n"
      "2:\n"
      "s_sleep 1\n"
      "ds_read_b32 v248, v16\n"
      "s_sub_u32 s91, s91, 1\n"
      "s_cmp_eq_u32 s91, 0\n"
      "s_cbranch_scc1 3f\n"
      "s_waitcnt lgkmcnt(0)\n"
      "v_readfirstlane_b32 s90, v248\n"
      "s_cmp_lt_u32 s90, 4\n"
      "s_cbranch_scc1 2b\n"
      "3:\n"
      "v_add_u32 v16, 4, v16\n"
      "ds_read_b128 v[192:195], v249 offset:0\n"
      "ds_read_b64 v[204:205], v250 offset:0\n"
      "ds_read_b128 v[196:199], v251 offset:0\n"
      "ds_read_b128 v[200:203], v251 offset:672\n"
      "v_exp_f32 v140, v140\n"
      "v_exp_f32 v141, v141\n"
      "v_exp_f32 v142, v142\n"
      "v_exp_f32 v143, v143\n"
      "v_mfma_f32_32x32x16_bf16 v[160:175], v[220:223], v[48:51], 0\n"
      "v_exp_f32 v144, v144\n"
      "v_exp_f32 v145, v145\n"
      "v_exp_f32 v146, v146\n"
      "v_exp_f32 v147, v147\n"
      "v_mfma_f32_32x32x8_bf16 v[160:175], v[232:233], v[60:61], v[160:175]\n"
      "v_exp_f32 v148, v148\n"
      "v_exp_f32 v149, v149\n"
      "v_exp_f32 v150, v150\n"
      "v_exp_f32 v151, v151\n"
      "v_smfmac_f32_32x32x32_bf16 v[64:79], v[224:227], v[128:135], v252\n"
      "v_exp_f32 v152, v152\n"
      "v_exp_f32 v153, v153\n"
      "v_exp_f32 v154, v154\n"
      "v_exp_f32 v155, v155\n"
      "v_smfmac_f32_32x32x32_bf16 v[64:79], v[228:231], v[136:143], v252\n"
      "v_exp_f32 v156, v156\n"
      "v_exp_f32 v157, v157\n"
      "v_exp_f32 v158, v158\n"
      "v_exp_f32 v159, v159\n"
      "v_mfma_f32_32x32x16_bf16 v[176:191], v[220:223], v[52:55], 0\n"
      "v_exp_f32 v160, v160\n"
      "v_exp_f32 v161, v161\n"
      "v_exp_f32 v162, v162\n"
      "v_exp_f32 v163, v163\n"
      "v_mfma_f32_32x32x8_bf16 v[176:191], v[232:233], v[62:63], v[176:191]\n"
      "v_exp_f32 v164, v164\n"
      "v_exp_f32 v165, v165\n"
      "v_exp_f32 v166, v166\n"
      "v_exp_f32 v167, v167\n"
      "v_smfmac_f32_32x32x32_bf16 v[80:95], v[224:227], v[144:151], v252\n"
      "v_exp_f32 v168, v168\n"
      "v_exp_f32 v169, v169\n"
      "v_exp_f32 v170, v170\n"
      "v_exp_f32 v171, v171\n"
      "v_smfmac_f32_32x32x32_bf16 v[80:95], v[228:231], v[152:159], v252\n"
      "v_exp_f32 v172, v172\n"
      "v_exp_f32 v173, v173\n"
      "v_exp_f32 v174, v174\n"
      "v_exp_f32 v175, v175\n"
      "v_mfma_f32_32x32x16_bf16 v[128:143], v[234:237], v[40:43], 0\n"
      "v_exp_f32 v176, v176\n"
      "v_exp_f32 v177, v177\n"
      "v_exp_f32 v178, v178\n"
      "v_exp_f32 v179, v179\n"
      "v_mfma_f32_32x32x8_bf16 v[128:143], v[246:247], v[56:57], v[128:143]\n"
      "v_exp_f32 v180, v180\n"
      "v_exp_f32 v181, v181\n"
      "v_exp_f32 v182, v182\n"
      "v_exp_f32 v183, v183\n"
      "v_smfmac_f32_32x32x32_bf16 v[96:111], v[224:227], v[160:167], v252\n"
      "v_exp_f32 v184, v184\n"
      "v_exp_f32 v185, v185\n"
      "v_exp_f32 v186, v186\n"
      "v_exp_f32 v187, v187\n"
      "v_smfmac_f32_32x32x32_bf16 v[96:111], v[228:231], v[168:175], v252\n"
      "v_exp_f32 v188, v188\n"
      "v_exp_f32 v189, v189\n"
      "v_exp_f32 v190, v190\n"
      "v_exp_f32 v191, v191\n"
      "v_mfma_f32_32x32x16_bf16 v[144:159], v[234:237], v[44:47], 0\n"
      "v_exp_f32 v128, v128\n"
      "v_exp_f32 v129, v129\n"
      "v_exp_f32 v130, v130\n"
      "v_exp_f32 v131, v131\n"
      "v_mfma_f32_32x32x8_bf16 v[144:159], v[246:247], v[58:59], v[144:159]\n"
      "v_exp_f32 v132, v132\n"
      "v_exp_f32 v133, v133\n"
      "v_exp_f32 v134, v134\n"
      "v_exp_f32 v135, v135\n"
      "v_smfmac_f32_32x32x32_bf16 v[112:127], v[224:227], v[176:183], v252\n"
      "v_exp_f32 v136, v136\n"
      "v_exp_f32 v137, v137\n"
      "v_exp_f32 v138, v138\n"
      "v_exp_f32 v139, v139\n"
      "v_smfmac_f32_32x32x32_bf16 v[112:127], v[228:231], v[184:191], v252\n"
      "s_waitcnt lgkmcnt(0)\n"
      "ds_read_b128 v[206:209], v249 offset:2624\n"
      "ds_read_b64 v[218:219], v250 offset:2624\n"
      "ds_read_b128 v[210:213], v251 offset:2624\n"
      "ds_read_b128 v[214:217], v251 offset:3296\n"
      "v_exp_f32 v140, v140\n"
      "v_exp_f32 v141, v141\n"
      "v_exp_f32 v142, v142\n"
      "v_exp_f32 v143, v143\n"
      "v_mfma_f32_32x32x16_bf16 v[160:175], v[234:237], v[48:51], 0\n"
      "v_exp_f32 v144, v144\n"
      "v_exp_f32 v145, v145\n"
      "v_exp_f32 v146, v146\n"
      "v_exp_f32 v147, v147\n"
      "v_mfma_f32_32x32x8_bf16 v[160:175], v[246:247], v[60:61], v[160:175]\n"
      "v_exp_f32 v148, v148\n"
      "v_exp_f32 v149, v149\n"
      "v_exp_f32 v150, v150\n"
      "v_exp_f32 v151, v151\n"
      "v_smfmac_f32_32x32x32_bf16 v[64:79], v[238:241], v[128:135], v252\n"
      "v_exp_f32 v152, v152\n"
      "v_exp_f32 v153, v153\n"
      "v_exp_f32 v154, v154\n"
      "v_exp_f32 v155, v155\n"
      "v_smfmac_f32_32x32x32_bf16 v[64:79], v[242:245], v[136:143], v252\n"
      "v_exp_f32 v156, v156\n"
      "v_exp_f32 v157, v157\n"
      "v_exp_f32 v158, v158\n"
      "v_exp_f32 v159, v159\n"
      "v_mfma_f32_32x32x16_bf16 v[176:191], v[234:237], v[52:55], 0\n"
      "v_exp_f32 v160, v160\n"
      "v_exp_f32 v161, v161\n"
      "v_exp_f32 v162, v162\n"
      "v_exp_f32 v163, v163\n"
      "v_mfma_f32_32x32x8_bf16 v[176:191], v[246:247], v[62:63], v[176:191]\n"
      "v_exp_f32 v164, v164\n"
      "v_exp_f32 v165, v165\n"
      "v_exp_f32 v166, v166\n"
      "v_exp_f32 v167, v167\n"
      "v_smfmac_f32_32x32x32_bf16 v[80:95], v[238:241], v[144:151], v252\n"
      "v_exp_f32 v168, v168\n"
      "v_exp_f32 v169, v169\n"
      "v_exp_f32 v170, v170\n"
      "v_exp_f32 v171, v171\n"
      "v_smfmac_f32_32x32x32_bf16 v[80:95], v[242:245], v[152:159], v252\n"
      "v_exp_f32 v172, v172\n"
      "v_exp_f32 v173, v173\n"
      "v_exp_f32 v174, v174\n"
      "v_exp_f32 v175, v175\n"
      "v_mfma_f32_32x32x16_bf16 v[128:143], v[192:195], v[40:43], 0\n"
      "v_exp_f32 v176, v176\n"
      "v_exp_f32 v177, v177\n"
      "v_exp_f32 v178, v178\n"
      "v_exp_f32 v179, v179\n"
      "v_mfma_f32_32x32x8_bf16 v[128:143], v[204:205], v[56:57], v[128:143]\n"
      "v_exp_f32 v180, v180\n"
      "v_exp_f32 v181, v181\n"
      "v_exp_f32 v182, v182\n"
      "v_exp_f32 v183, v183\n"
      "v_smfmac_f32_32x32x32_bf16 v[96:111], v[238:241], v[160:167], v252\n"
      "v_exp_f32 v184, v184\n"
      "v_exp_f32 v185, v185\n"
      "v_exp_f32 v186, v186\n"
      "v_exp_f32 v187, v187\n"
      "v_smfmac_f32_32x32x32_bf16 v[96:111], v[242:245], v[168:175], v252\n"
      "v_exp_f32 v188, v188\n"
      "v_exp_f32 v189, v189\n"
      "v_exp_f32 v190, v190\n"
      "v_exp_f32 v191, v191\n"
      "s_sub_u32 s73, s73, 1\n"
      "s_cmp_lg_u32 s73, 0\n"
      "s_cbranch_scc1 1b\n"
      "ds_read2_b32 v[246:247], v16 offset0:3 offset1:4\n"
      "s_nop 0\n"
      "v_smfmac_f32_32x32x32_bf16 v[112:127], v[238:241], v[176:183], v252\n"
      "v_smfmac_f32_32x32x32_bf16 v[112:127], v[242:245], v[184:191], v252\n"
      "s_waitcnt lgkmcnt(0)\n"
      "s_setprio 3\n"
      "v_readfirstlane_b32 s90, v247\n"
      "v_readfirstlane_b32 s91, v246\n"
      "s_cmp_lg_u32 s90, 0\n"
      "s_cbranch_scc1 29f\n"
      "s_cmp_ge_u32 s91, 4\n"
      "s_cbranch_scc1 28f\n"
      "s_mov_b32 s91, 0x100000\n"
      "118:\n"
      "ds_read_b32 v248, v16 offset:12\n"
      "s_sub_u32 s91, s91, 1\n"
      "s_cmp_eq_u32 s91, 0\n"
      "s_cbranch_scc1 119f\n"
      "s_waitcnt lgkmcnt(0)\n"
      "v_readfirstlane_b32 s90, v248\n"
      "s_cmp_lt_u32 s90, 4\n"
      "s_cbranch_scc1 118b\n"
      "119:\n"
      "28:\n"
      "ds_write_b128 v32, v[76:79] offset:1024\n"
      "ds_write_b128 v32, v[72:75] offset:0\n"
      "ds_write_b128 v32, v[92:95] offset:3072\n"
      "ds_write_b128 v32, v[88:91] offset:2048\n"
      "ds_write_b128 v32, v[108:111] offset:5120\n"
      "ds_write_b128 v32, v[104:107] offset:4096\n"
      "s_nop 5\n"
      "ds_write_b128 v32, v[124:127] offset:7168\n"
      "ds_write_b128 v32, v[120:123] offset:6144\n"
      "s_waitcnt lgkmcnt(0)\n"
      "ds_write_b32 v34, v253\n"
      "s_mov_b32 s91, 0x400000\n"
      "21:\n"
      "ds_read_b32 v248, v35\n"
      "s_sub_u32 s91, s91, 1\n"
      "s_cmp_eq_u32 s91, 0\n"
      "s_cbranch_scc1 22f\n"
      "s_waitcnt lgkmcnt(0)\n"
      "v_readfirstlane_b32 s90, v248\n"
      "s_cmp_eq_u32 s90, 0\n"
      "s_cbranch_scc1 21b\n"
      "22:\n"
      "ds_read_b128 v[132:135], v33 offset:1024\n"
      "ds_read_b128 v[140:143], v33 offset:3072\n"
      "ds_read_b128 v[148:151], v33 offset:5120\n"
      "ds_read_b128 v[156:159], v33 offset:7168\n"
      "ds_read_b128 v[128:131], v33 offset:0\n"
      "ds_read_b128 v[136:139], v33 offset:2048\n"
      "ds_read_b128 v[144:147], v33 offset:4096\n"
      "ds_read_b128 v[152:155], v33 offset:6144\n"
      "s_waitcnt lgkmcnt(4)\n"
      "v_add_f32 v70, v70, v134\n"
      "v_add_f32 v86, v86, v142\n"
      "v_add_f32 v102, v102, v150\n"
      "v_add_f32 v118, v118, v158\n"
      "v_rcp_f32 v70, v70\n"
      "v_rcp_f32 v86, v86\n"
      "v_rcp_f32 v102, v102\n"
      "v_rcp_f32 v118, v118\n"
      "s_waitcnt lgkmcnt(0)\n"
      "s_mov_b64 exec, 1\n"
      "ds_add_u32 v16, v253 offset:40\n"
      "s_mov_b64 exec, -1\n"
      "v_add_f32 v64, v64, v128\n"
      "v_add_f32 v65, v65, v129\n"
      "v_add_f32 v80, v80, v136\n"
      "v_add_f32 v81, v81, v137\n"
      "v_add_f32 v96, v96, v144\n"
      "v_add_f32 v97, v97, v145\n"
      "v_add_f32 v112, v112, v152\n"
      "v_add_f32 v113, v113, v153\n"
      "v_mul_f32 v64, v64, v70\n"
      "v_mul_f32 v65, v65, v70\n"
      "v_mul_f32 v80, v80, v86\n"
      "v_mul_f32 v81, v81, v86\n"
      "v_mul_f32 v96, v96, v102\n"
      "v_mul_f32 v97, v97, v102\n"
      "v_mul_f32 v112, v112, v118\n"
      "v_mul_f32 v113, v113, v118\n"
      "v_cvt_pk_bf16_f32 v168, v64, v65\n"
      "v_cvt_pk_bf16_f32 v169, v80, v81\n"
      "v_cvt_pk_bf16_f32 v170, v96, v97\n"
      "v_cvt_pk_bf16_f32 v171, v112, v113\n"
      "global_store_dwordx4 v36, v[168:171], s[56:57] sc1\n"
      "v_add_f32 v66, v66, v130\n"
      "v_add_f32 v67, v67, v131\n"
      "v_add_f32 v82, v82, v138\n"
      "v_add_f32 v83, v83, v139\n"
      "v_add_f32 v98, v98, v146\n"
      "v_add_f32 v99, v99, v147\n"
      "v_add_f32 v114, v114, v154\n"
      "v_add_f32 v115, v115, v155\n"
      "v_mul_f32 v66, v66, v70\n"
      "v_mul_f32 v67, v67, v70\n"
      "v_mul_f32 v82, v82, v86\n"
      "v_mul_f32 v83, v83, v86\n"
      "v_mul_f32 v98, v98, v102\n"
      "v_mul_f32 v99, v99, v102\n"
      "v_mul_f32 v114, v114, v118\n"
      "v_mul_f32 v115, v115, v118\n"
      "v_cvt_pk_bf16_f32 v172, v66, v67\n"
      "v_cvt_pk_bf16_f32 v173, v82, v83\n"
      "v_cvt_pk_bf16_f32 v174, v98, v99\n"
      "v_cvt_pk_bf16_f32 v175, v114, v115\n"
      "global_store_dwordx4 v37, v[172:175], s[56:57] sc1\n"
      "s_cmp_lg_u32 s72, 0\n"
      "s_cbranch_scc1 23f\n"
      "v_add_f32 v68, v68, v132\n"
      "v_add_f32 v69, v69, v133\n"
      "v_add_f32 v84, v84, v140\n"
      "v_add_f32 v85, v85, v141\n"
      "v_add_f32 v100, v100, v148\n"
      "v_add_f32 v101, v101, v149\n"
      "v_add_f32 v116, v116, v156\n"
      "v_add_f32 v117, v117, v157\n"
      "v_mul_f32 v68, v68, v70\n"
      "v_mul_f32 v69, v69, v70\n"
      "v_mul_f32 v84, v84, v86\n"
      "v_mul_f32 v85, v85, v86\n"
      "v_mul_f32 v100, v100, v102\n"
      "v_mul_f32 v101, v101, v102\n"
      "v_mul_f32 v116, v116, v118\n"
      "v_mul_f32 v117, v117, v118\n"
      "v_cvt_pk_bf16_f32 v176, v68, v69\n"
      "v_cvt_pk_bf16_f32 v177, v84, v85\n"
      "v_cvt_pk_bf16_f32 v178, v100, v101\n"
      "v_cvt_pk_bf16_f32 v179, v116, v117\n"
      "global_store_dwordx4 v38, v[176:179], s[56:57] sc1\n"
      "23:\n"
      "s_mov_b32 s75, 1\n"
      "s_branch 30f\n"
      "29:\n"
      "s_nop 15\n"
      "30:\n"
      "s_nop 1\n"
        : "=&{v[64:79]}"(O0), "=&{v[80:95]}"(O1), "=&{v[96:111]}"(O2), "=&{v[112:127]}"(O3), "+{v[8:23]}"(P), "+{v[24:39]}"(P2),
          "+{s[68:75]}"(T), "+{v[192:223]}"(xa), "+{v[224:231]}"(xa2), [gv] "+v"(gv)
        : "{s[40:43]}"(srw), "{s[44:47]}"(srq), "{s[48:51]}"(mk_srd(ws + WS_KV, 4 * CHUNK)), "{s[52:67]}"(S),
          "{s[76:79]}"(u32x4{(u32)(u64)(loc < 12 ? wd : wo), (u32)((u64)(loc < 12 ? wd : wo) >> 32), (u32)__builtin_amdgcn_readfirstlane(loc < 17 ? 5 : 0), junk})
        : "memory", "scc", "vcc", "s90", "s91", "s92", "s93", "s94", "s95", "v40", "v41", "v42", "v43", "v44", "v45", "v46", "v47", "v48", "v49", "v50", "v51", "v52", "v53", "v54", "v55", "v56", "v57", "v58", "v59", "v60", "v61", "v62", "v63", "v128", "v129", "v130", "v131", "v132", "v133", "v134", "v135", "v136", "v137", "v138", "v139", "v140", "v141", "v142", "v143", "v144", "v145", "v146", "v147", "v148", "v149", "v150", "v151", "v152", "v153", "v154", "v155", "v156", "v157", "v158", "v159", "v160", "v161", "v162", "v163", "v164", "v165", "v166", "v167", "v168", "v169", "v170", "v171", "v172", "v173", "v174", "v175", "v176", "v177", "v178", "v179", "v180", "v181", "v182", "v183", "v184", "v185", "v186", "v187", "v188", "v189", "v190", "v191", "v232", "v233", "v234", "v235", "v236", "v237", "v238", "v239", "v240", "v241", "v242", "v243", "v244", "v245", "v246", "v247", "v248", "v249", "v250", "v251", "v252", "v253", "v254", "v255");
    {
      if (__builtin_expect(T[7] == 0, 0)) {
        LANE_DEFS;
        const u32 vof = P2[5] - (ldsb + kb * 8 * REC);
        LAS char* qp = lds + (P2[6] - ldsb);
        LAS char* qp2 = lds + (P2[7] - ldsb);
        const __amdgpu_buffer_rsrc_t rs = __builtin_amdgcn_make_buffer_rsrc(ws + WS_KV, 0, 4 * CHUNK, 0x00020000);
        const s16x4 ones = {0x3f80, 0x3f80, 0x3f80, 0};
#pragma unroll 1
        for (int i = 0; i < 4; ++i) {
          const bf16x8 q1 = *(LAS bf16x8*)(qp + i * 2048);
          u32x2 q2 = *(LAS u32x2*)(qp2 + i * 512);
          if (h) q2 = u32x2{0u, 0u};
          float mx = -3.0e38f;
#pragma unroll 1
          for (int t = 0; t < 64; ++t) {
            u32x4 k1 = __builtin_amdgcn_raw_buffer_load_b128(rs, vo, t * REC, 16);
            u32x2 k2 = __builtin_amdgcn_raw_buffer_load_b64(rs, 1024 + l31 * 8, t * REC, 16);
            f32x16 s = mfma16(__builtin_bit_cast(bf16x8, k1), q1, f32x16{});
            s = __builtin_amdgcn_mfma_f32_32x32x8bf16_1k(__builtin_bit_cast(s16x4, k2), __builtin_bit_cast(s16x4, q2), s, 0, 0, 0);
#pragma unroll
            for (int r = 0; r < 16; ++r) mx = __builtin_fmaxf(mx, s[r]);
          }
          const float m = -max32(mx);
          const float m0 = rbf(m), m1 = rbf(m - m0), m2 = rbf(m - m0 - m1);
          if (h) q2 = u32x2{pk(m0, m1), pk(m2, 0.f)};
          f32x16 o = {};
#pragma unroll 1
          for (int t = 0; t < 32; ++t) {
            const int so = ((t >> 3) * 16 + kb * 8 + (t & 7)) * REC;
            u32x4 k1 = __builtin_amdgcn_raw_buffer_load_b128(rs, vo, so, 16);
            u32x2 k2 = __builtin_amdgcn_raw_buffer_load_b64(rs, 1024 + l31 * 8, so, 16);
            u32x4 v1 = __builtin_amdgcn_raw_buffer_load_b128(rs, vof, so, 16);
            u32x4 v2 = __builtin_amdgcn_raw_buffer_load_b128(rs, vof + 672, so, 16);
            f32x16 s = mfma16(__builtin_bit_cast(bf16x8, k1), q1, f32x16{});
            s = __builtin_amdgcn_mfma_f32_32x32x8bf16_1k(h ? ones : __builtin_bit_cast(s16x4, k2), __builtin_bit_cast(s16x4, q2), s, 0, 0, 0);
            f32x8 pl, ph;
#pragma unroll
            for (int r = 0; r < 8; ++r) { pl[r] = __builtin_amdgcn_exp2f(s[r]); ph[r] = __builtin_amdgcn_exp2f(s[r + 8]); }
            o = __builtin_amdgcn_smfmac_f32_32x32x32_bf16(__builtin_bit_cast(bf16x8, v1), __builtin_bit_cast(bf16x16, pl), o, 0xdddd, 0, 0);
            o = __builtin_amdgcn_smfmac_f32_32x32x32_bf16(__builtin_bit_cast(bf16x8, v2), __builtin_bit_cast(bf16x16, ph), o, 0xdddd, 0, 0);
          }
          O0 = O1; O1 = O2; O2 = O3; O3 = o;
        }
        asm volatile(
      "v_mov_b32 v253, 1\n"
      "s_nop 15\n"
      "s_mov_b32 s91, 0x100000\n"
      "121:\n"
      "ds_read_b32 v248, v16 offset:12\n"
      "s_sub_u32 s91, s91, 1\n"
      "s_cmp_eq_u32 s91, 0\n"
      "s_cbranch_scc1 122f\n"
      "s_waitcnt lgkmcnt(0)\n"
      "v_readfirstlane_b32 s90, v248\n"
      "s_cmp_lt_u32 s90, 4\n"
      "s_cbranch_scc1 121b\n"
      "122:\n"
      "ds_write_b128 v32, v[76:79] offset:1024\n"
      "ds_write_b128 v32, v[72:75] offset:0\n"
      "ds_write_b128 v32, v[92:95] offset:3072\n"
      "ds_write_b128 v32, v[88:91] offset:2048\n"
      "ds_write_b128 v32, v[108:111] offset:5120\n"
      "ds_write_b128 v32, v[104:107] offset:4096\n"
      "s_nop 5\n"
      "ds_write_b128 v32, v[124:127] offset:7168\n"
      "ds_write_b128 v32, v[120:123] offset:6144\n"
      "s_waitcnt lgkmcnt(0)\n"
      "ds_write_b32 v34, v253\n"
      "s_mov_b32 s91, 0x400000\n"
      "21:\n"
      "ds_read_b32 v248, v35\n"
      "s_sub_u32 s91, s91, 1\n"
      "s_cmp_eq_u32 s91, 0\n"
      "s_cbranch_scc1 22f\n"
      "s_waitcnt lgkmcnt(0)\n"
      "v_readfirstlane_b32 s90, v248\n"
      "s_cmp_eq_u32 s90, 0\n"
      "s_cbranch_scc1 21b\n"
      "22:\n"
      "ds_read_b128 v[132:135], v33 offset:1024\n"
      "ds_read_b128 v[140:143], v33 offset:3072\n"
      "ds_read_b128 v[148:151], v33 offset:5120\n"
      "ds_read_b128 v[156:159], v33 offset:7168\n"
      "ds_read_b128 v[128:131], v33 offset:0\n"
      "ds_read_b128 v[136:139], v33 offset:2048\n"
      "ds_read_b128 v[144:147], v33 offset:4096\n"
      "ds_read_b128 v[152:155], v33 offset:6144\n"
      "s_waitcnt lgkmcnt(4)\n"
      "v_add_f32 v70, v70, v134\n"
      "v_add_f32 v86, v86, v142\n"
      "v_add_f32 v102, v102, v150\n"
      "v_add_f32 v118, v118, v158\n"
      "v_rcp_f32 v70, v70\n"
      "v_rcp_f32 v86, v86\n"
      "v_rcp_f32 v102, v102\n"
      "v_rcp_f32 v118, v118\n"
      "s_waitcnt lgkmcnt(0)\n"
      "s_mov_b64 exec, 1\n"
      "ds_add_u32 v16, v253 offset:40\n"
      "s_mov_b64 exec, -1\n"
      "v_add_f32 v64, v64, v128\n"
      "v_add_f32 v65, v65, v129\n"
      "v_add_f32 v80, v80, v136\n"
      "v_add_f32 v81, v81, v137\n"
      "v_add_f32 v96, v96, v144\n"
      "v_add_f32 v97, v97, v145\n"
      "v_add_f32 v112, v112, v152\n"
      "v_add_f32 v113, v113, v153\n"
      "v_mul_f32 v64, v64, v70\n"
      "v_mul_f32 v65, v65, v70\n"
      "v_mul_f32 v80, v80, v86\n"
      "v_mul_f32 v81, v81, v86\n"
      "v_mul_f32 v96, v96, v102\n"
      "v_mul_f32 v97, v97, v102\n"
      "v_mul_f32 v112, v112, v118\n"
      "v_mul_f32 v113, v113, v118\n"
      "v_cvt_pk_bf16_f32 v168, v64, v65\n"
      "v_cvt_pk_bf16_f32 v169, v80, v81\n"
      "v_cvt_pk_bf16_f32 v170, v96, v97\n"
      "v_cvt_pk_bf16_f32 v171, v112, v113\n"
      "global_store_dwordx4 v36, v[168:171], s[56:57] sc1\n"
      "v_add_f32 v66, v66, v130\n"
      "v_add_f32 v67, v67, v131\n"
      "v_add_f32 v82, v82, v138\n"
      "v_add_f32 v83, v83, v139\n"
      "v_add_f32 v98, v98, v146\n"
      "v_add_f32 v99, v99, v147\n"
      "v_add_f32 v114, v114, v154\n"
      "v_add_f32 v115, v115, v155\n"
      "v_mul_f32 v66, v66, v70\n"
      "v_mul_f32 v67, v67, v70\n"
      "v_mul_f32 v82, v82, v86\n"
      "v_mul_f32 v83, v83, v86\n"
      "v_mul_f32 v98, v98, v102\n"
      "v_mul_f32 v99, v99, v102\n"
      "v_mul_f32 v114, v114, v118\n"
      "v_mul_f32 v115, v115, v118\n"
      "v_cvt_pk_bf16_f32 v172, v66, v67\n"
      "v_cvt_pk_bf16_f32 v173, v82, v83\n"
      "v_cvt_pk_bf16_f32 v174, v98, v99\n"
      "v_cvt_pk_bf16_f32 v175, v114, v115\n"
      "global_store_dwordx4 v37, v[172:175], s[56:57] sc1\n"
      "s_cmp_lg_u32 s72, 0\n"
      "s_cbranch_scc1 23f\n"
      "v_add_f32 v68, v68, v132\n"
      "v_add_f32 v69, v69, v133\n"
      "v_add_f32 v84, v84, v140\n"
      "v_add_f32 v85, v85, v141\n"
      "v_add_f32 v100, v100, v148\n"
      "v_add_f32 v101, v101, v149\n"
      "v_add_f32 v116, v116, v156\n"
      "v_add_f32 v117, v117, v157\n"
      "v_mul_f32 v68, v68, v70\n"
      "v_mul_f32 v69, v69, v70\n"
      "v_mul_f32 v84, v84, v86\n"
      "v_mul_f32 v85, v85, v86\n"
      "v_mul_f32 v100, v100, v102\n"
      "v_mul_f32 v101, v101, v102\n"
      "v_mul_f32 v116, v116, v118\n"
      "v_mul_f32 v117, v117, v118\n"
      "v_cvt_pk_bf16_f32 v176, v68, v69\n"
      "v_cvt_pk_bf16_f32 v177, v84, v85\n"
      "v_cvt_pk_bf16_f32 v178, v100, v101\n"
      "v_cvt_pk_bf16_f32 v179, v116, v117\n"
      "global_store_dwordx4 v38, v[176:179], s[56:57] sc1\n"
      "23:\n"
            : "+{v[64:79]}"(O0), "+{v[80:95]}"(O1), "+{v[96:111]}"(O2), "+{v[112:127]}"(O3)
            : "{v[8:23]}"(P), "{v[24:39]}"(P2), "{s[52:67]}"(S), "{s[68:75]}"(T)
            : "memory", "scc", "s90", "s91", "v128", "v129", "v130", "v131", "v132", "v133", "v134", "v135", "v136", "v137", "v138", "v139", "v140", "v141", "v142", "v143", "v144", "v145", "v146", "v147", "v148", "v149", "v150", "v151", "v152", "v153", "v154", "v155", "v156", "v157", "v158", "v159", "v160", "v161", "v162", "v163", "v164", "v165", "v166", "v167", "v168", "v169", "v170", "v171", "v172", "v173", "v174", "v175", "v176", "v177", "v178", "v179", "v248", "v253", "v254");
      }
    }
    {
      LANE_DEFS;
      const u32 tag = __builtin_amdgcn_readfirstlane(P[10]), fg = ((i16 >> 2) + j3) & 3;
      u32x16 BV = {vo, 0u, 0u, 0u, 0u, 0u, 0u, 0u, ldsb + L_PAIR + vo + (w ^ 3) * 10240, ldsb + L_MISC + (C_BFLY + w) * 4,
                   ldsb + L_MISC, (2 * fg + ((lane >> 1) & 1)) * 64 + ((w >> 1) + 2 * (lane & 1)) * 4,
                   (u32)((64 * g3 + 16 * w + i16) * 1280 + j3 * 160 + gq * 40), 1u, tag, 0u};
#define OSO(s) (u32)((48 + 5 * ((j3 + (s)) & 3)) * 4096 + w * 1024)
      u32x8 BS = {0u, 0u, OSO(0), OSO(1), OSO(2), OSO(3), 0u, 0u};
      const u64 fl = (u64)(ws + WS_OFLAG + (u32)(g3 * 8 * 64));
      asm volatile(
      "ds_read_b32 v20, v34 offset:12\n"
      "s_nop 3\n"
      "global_load_dword v21, v35, s[68:69] sc1\n"
      "global_load_dwordx4 v[240:243], v36, s[74:75]\n"
      "global_load_dwordx4 v[244:247], v36, s[74:75] offset:16\n"
      "global_load_dwordx2 v[22:23], v36, s[74:75] offset:32\n"
      "s_mov_b32 s92, 0x1111\n"
      "s_mov_b32 s94, 0x40000\n"
      "s_mov_b32 s67, 1\n"
      "s_waitcnt vmcnt(4)\n"
      "global_store_dword v26, v38, s[72:73] sc1\n"
      "s_waitcnt lgkmcnt(0)\n"
      "v_readfirstlane_b32 s90, v20\n"
      "s_cmp_lt_u32 s90, 4\n"
      "s_cbranch_scc1 38f\n"
      "s_mov_b32 s67, 0\n"
      "ds_read_b128 v[140:143], v24 offset:0\n"
      "ds_read_b128 v[144:147], v24 offset:1024\n"
      "ds_read_b128 v[148:151], v24 offset:2048\n"
      "ds_read_b128 v[152:155], v24 offset:3072\n"
      "ds_read_b128 v[156:159], v24 offset:4096\n"
      "ds_read_b128 v[160:163], v24 offset:5120\n"
      "ds_read_b128 v[164:167], v24 offset:6144\n"
      "ds_read_b128 v[168:171], v24 offset:7168\n"
      "ds_read_b128 v[172:175], v24 offset:8192\n"
      "ds_read_b128 v[176:179], v24 offset:9216\n"
      "ds_read_b128 v[180:183], v24 offset:10240\n"
      "ds_read_b128 v[184:187], v24 offset:11264\n"
      "ds_read_b128 v[188:191], v24 offset:12288\n"
      "ds_read_b128 v[192:195], v24 offset:13312\n"
      "ds_read_b128 v[196:199], v24 offset:14336\n"
      "ds_read_b128 v[200:203], v24 offset:15360\n"
      "ds_read_b128 v[204:207], v24 offset:16384\n"
      "ds_read_b128 v[208:211], v24 offset:17408\n"
      "ds_read_b128 v[212:215], v24 offset:18432\n"
      "ds_read_b128 v[216:219], v24 offset:19456\n"
      "ds_read_b128 v[220:223], v24 offset:20480\n"
      "ds_read_b128 v[224:227], v24 offset:21504\n"
      "ds_read_b128 v[228:231], v24 offset:22528\n"
      "ds_read_b128 v[232:235], v24 offset:23552\n"
      "ds_read_b128 v[236:239], v24 offset:24576\n"
      "38:\n"
      "s_mov_b32 s90, 0xffff0000\n"
      "s_waitcnt vmcnt(1)\n"
      "v_lshlrev_b32 v40, 16, v240\n"
      "v_and_b32 v41, s90, v240\n"
      "v_lshlrev_b32 v42, 16, v241\n"
      "v_and_b32 v43, s90, v241\n"
      "v_lshlrev_b32 v44, 16, v242\n"
      "v_and_b32 v45, s90, v242\n"
      "v_lshlrev_b32 v46, 16, v243\n"
      "v_and_b32 v47, s90, v243\n"
      "v_lshlrev_b32 v48, 16, v244\n"
      "v_and_b32 v49, s90, v244\n"
      "v_lshlrev_b32 v50, 16, v245\n"
      "v_and_b32 v51, s90, v245\n"
      "v_lshlrev_b32 v52, 16, v246\n"
      "v_and_b32 v53, s90, v246\n"
      "v_lshlrev_b32 v54, 16, v247\n"
      "v_and_b32 v55, s90, v247\n"
      "v_lshlrev_b32 v56, 16, v22\n"
      "v_and_b32 v57, s90, v22\n"
      "v_lshlrev_b32 v58, 16, v23\n"
      "v_and_b32 v59, s90, v23\n"
      "s_branch 41f\n"
      "40:\n"
      "s_waitcnt vmcnt(0)\n"
      "41:\n"
      "s_sub_u32 s94, s94, 1\n"
      "v_cmp_eq_u32 vcc, v21, v38\n"
      "s_cmp_lt_i32 s94, 0\n"
      "s_cselect_b32 s95, -1, vcc_lo\n"
      "s_lshr_b32 s90, s95, 1\n"
      "s_and_b32 s95, s95, s90\n"
      "s_lshr_b32 s90, s95, 2\n"
      "s_and_b32 s95, s95, s90\n"
      "s_and_b32 s95, s95, s92\n"
      "s_andn2_b32 s92, s92, s95\n"
      "s_bitcmp0_b32 s95, 0\n"
      "s_cbranch_scc1 42f\n"
      "buffer_load_dwordx4 v[60:63], v24, s[44:47], s62 offen sc1\n"
      "s_add_u32 s62, s62, 4096\n"
      "buffer_load_dwordx4 v[64:67], v24, s[44:47], s62 offen sc1\n"
      "s_add_u32 s62, s62, 4096\n"
      "buffer_load_dwordx4 v[68:71], v24, s[44:47], s62 offen sc1\n"
      "s_add_u32 s62, s62, 4096\n"
      "buffer_load_dwordx4 v[72:75], v24, s[44:47], s62 offen sc1\n"
      "s_add_u32 s62, s62, 4096\n"
      "buffer_load_dwordx4 v[76:79], v24, s[44:47], s62 offen sc1\n"
      "s_add_u32 s62, s62, 4096\n"
      "42:\n"
      "s_bitcmp0_b32 s95, 4\n"
      "s_cbranch_scc1 43f\n"
      "buffer_load_dwordx4 v[80:83], v24, s[44:47], s63 offen sc1\n"
      "s_add_u32 s63, s63, 4096\n"
      "buffer_load_dwordx4 v[84:87], v24, s[44:47], s63 offen sc1\n"
      "s_add_u32 s63, s63, 4096\n"
      "buffer_load_dwordx4 v[88:91], v24, s[44:47], s63 offen sc1\n"
      "s_add_u32 s63, s63, 4096\n"
      "buffer_load_dwordx4 v[92:95], v24, s[44:47], s63 offen sc1\n"
      "s_add_u32 s63, s63, 4096\n"
      "buffer_load_dwordx4 v[96:99], v24, s[44:47], s63 offen sc1\n"
      "s_add_u32 s63, s63, 4096\n"
      "43:\n"
      "s_bitcmp0_b32 s95, 8\n"
      "s_cbranch_scc1 44f\n"
      "buffer_load_dwordx4 v[100:103], v24, s[44:47], s64 offen sc1\n"
      "s_add_u32 s64, s64, 4096\n"
      "buffer_load_dwordx4 v[104:107], v24, s[44:47], s64 offen sc1\n"
      "s_add_u32 s64, s64, 4096\n"
      "buffer_load_dwordx4 v[108:111], v24, s[44:47], s64 offen sc1\n"
      "s_add_u32 s64, s64, 4096\n"
      "buffer_load_dwordx4 v[112:115], v24, s[44:47], s64 offen sc1\n"
      "s_add_u32 s64, s64, 4096\n"
      "buffer_load_dwordx4 v[116:119], v24, s[44:47], s64 offen sc1\n"
      "s_add_u32 s64, s64, 4096\n"
      "44:\n"
      "s_bitcmp0_b32 s95, 12\n"
      "s_cbranch_scc1 45f\n"
      "buffer_load_dwordx4 v[120:123], v24, s[44:47], s65 offen sc1\n"
      "s_add_u32 s65, s65, 4096\n"
      "buffer_load_dwordx4 v[124:127], v24, s[44:47], s65 offen sc1\n"
      "s_add_u32 s65, s65, 4096\n"
      "buffer_load_dwordx4 v[128:131], v24, s[44:47], s65 offen sc1\n"
      "s_add_u32 s65, s65, 4096\n"
      "buffer_load_dwordx4 v[132:135], v24, s[44:47], s65 offen sc1\n"
      "s_add_u32 s65, s65, 4096\n"
      "buffer_load_dwordx4 v[136:139], v24, s[44:47], s65 offen sc1\n"
      "s_add_u32 s65, s65, 4096\n"
      "45:\n"
      "s_cmp_lg_u32 s95, 0\n"
      "s_cbranch_scc1 46f\n"
      "s_sleep 2\n"
      "global_load_dword v21, v35, s[68:69] sc1\n"
      "s_branch 40b\n"
      "46:\n"
      "global_load_dword v21, v35, s[68:69] sc1\n"
      "s_bcnt1_i32_b32 s66, s95\n"
      "s_ff1_i32_b32 s90, s95\n"
      "s_cmp_eq_u32 s90, s67\n"
      "s_cbranch_scc1 48f\n"
      "s_mov_b32 s67, s90\n"
      "s_mul_i32 s90, s67, 6400\n"
      "s_cmp_eq_u32 s67, 12\n"
      "s_cselect_b32 s90, 136192, s90\n"
      "v_add_u32 v39, s90, v24\n"
      "s_mov_b32 s91, 0x100000\n"
      "150:\n"
      "ds_read_b32 v20, v34 offset:12\n"
      "s_sub_u32 s91, s91, 1\n"
      "s_cmp_eq_u32 s91, 0\n"
      "s_cbranch_scc1 151f\n"
      "s_waitcnt lgkmcnt(0)\n"
      "v_readfirstlane_b32 s90, v20\n"
      "s_cmp_lt_u32 s90, 4\n"
      "s_cbranch_scc1 150b\n"
      "151:\n"
      "ds_read_b128 v[140:143], v39 offset:0\n"
      "ds_read_b128 v[144:147], v39 offset:1024\n"
      "ds_read_b128 v[148:151], v39 offset:2048\n"
      "ds_read_b128 v[152:155], v39 offset:3072\n"
      "ds_read_b128 v[156:159], v39 offset:4096\n"
      "ds_read_b128 v[160:163], v39 offset:5120\n"
      "ds_read_b128 v[164:167], v39 offset:6144\n"
      "ds_read_b128 v[168:171], v39 offset:7168\n"
      "ds_read_b128 v[172:175], v39 offset:8192\n"
      "ds_read_b128 v[176:179], v39 offset:9216\n"
      "ds_read_b128 v[180:183], v39 offset:10240\n"
      "ds_read_b128 v[184:187], v39 offset:11264\n"
      "ds_read_b128 v[188:191], v39 offset:12288\n"
      "ds_read_b128 v[192:195], v39 offset:13312\n"
      "ds_read_b128 v[196:199], v39 offset:14336\n"
      "ds_read_b128 v[200:203], v39 offset:15360\n"
      "ds_read_b128 v[204:207], v39 offset:16384\n"
      "ds_read_b128 v[208:211], v39 offset:17408\n"
      "ds_read_b128 v[212:215], v39 offset:18432\n"
      "ds_read_b128 v[216:219], v39 offset:19456\n"
      "ds_read_b128 v[220:223], v39 offset:20480\n"
      "ds_read_b128 v[224:227], v39 offset:21504\n"
      "ds_read_b128 v[228:231], v39 offset:22528\n"
      "ds_read_b128 v[232:235], v39 offset:23552\n"
      "ds_read_b128 v[236:239], v39 offset:24576\n"
      "48:\n"
      "s_bitcmp0_b32 s95, 0\n"
      "s_cbranch_scc1 50f\n"
      "s_sub_u32 s66, s66, 1\n"
      "s_waitcnt vmcnt(16)\n"
      "s_cmp_eq_u32 s66, 3\n"
      "s_cbranch_scc1 51f\n"
      "s_waitcnt vmcnt(11)\n"
      "s_cmp_eq_u32 s66, 2\n"
      "s_cbranch_scc1 51f\n"
      "s_waitcnt vmcnt(6)\n"
      "s_cmp_eq_u32 s66, 1\n"
      "s_cbranch_scc1 51f\n"
      "s_waitcnt vmcnt(1)\n"
      "51:\n"
      "s_waitcnt lgkmcnt(14)\n"
      "v_mfma_f32_16x16x32_bf16 v[40:43], v[140:143], v[60:63], v[40:43]\n"
      "s_bitset0_b32 s95, 0\n"
      "s_cmp_lg_u32 s95, 0\n"
      "v_mfma_f32_16x16x32_bf16 v[44:47], v[144:147], v[60:63], v[44:47]\n"
      "s_cselect_b32 s90, s95, s92\n"
      "s_or_b32 s90, s90, 0x10000\n"
      "v_mfma_f32_16x16x32_bf16 v[48:51], v[148:151], v[60:63], v[48:51]\n"
      "s_ff1_i32_b32 s67, s90\n"
      "s_and_b32 s67, s67, 15\n"
      "v_mfma_f32_16x16x32_bf16 v[52:55], v[152:155], v[60:63], v[52:55]\n"
      "s_mul_i32 s90, s67, 6400\n"
      "s_cmp_eq_u32 s67, 12\n"
      "v_mfma_f32_16x16x32_bf16 v[56:59], v[156:159], v[60:63], v[56:59]\n"
      "s_cselect_b32 s90, 136192, s90\n"
      "v_add_u32 v39, s90, v24\n"
      "v_mfma_f32_16x16x32_bf16 v[40:43], v[160:163], v[64:67], v[40:43]\n"
      "ds_read_b128 v[140:143], v39 offset:0\n"
      "v_mfma_f32_16x16x32_bf16 v[44:47], v[164:167], v[64:67], v[44:47]\n"
      "ds_read_b128 v[144:147], v39 offset:1024\n"
      "v_mfma_f32_16x16x32_bf16 v[48:51], v[168:171], v[64:67], v[48:51]\n"
      "ds_read_b128 v[148:151], v39 offset:2048\n"
      "v_mfma_f32_16x16x32_bf16 v[52:55], v[172:175], v[64:67], v[52:55]\n"
      "ds_read_b128 v[152:155], v39 offset:3072\n"
      "v_mfma_f32_16x16x32_bf16 v[56:59], v[176:179], v[64:67], v[56:59]\n"
      "ds_read_b128 v[156:159], v39 offset:4096\n"
      "s_waitcnt lgkmcnt(14)\n"
      "v_mfma_f32_16x16x32_bf16 v[40:43], v[180:183], v[68:71], v[40:43]\n"
      "ds_read_b128 v[160:163], v39 offset:5120\n"
      "v_mfma_f32_16x16x32_bf16 v[44:47], v[184:187], v[68:71], v[44:47]\n"
      "ds_read_b128 v[164:167], v39 offset:6144\n"
      "v_mfma_f32_16x16x32_bf16 v[48:51], v[188:191], v[68:71], v[48:51]\n"
      "ds_read_b128 v[168:171], v39 offset:7168\n"
      "v_mfma_f32_16x16x32_bf16 v[52:55], v[192:195], v[68:71], v[52:55]\n"
      "ds_read_b128 v[172:175], v39 offset:8192\n"
      "v_mfma_f32_16x16x32_bf16 v[56:59], v[196:199], v[68:71], v[56:59]\n"
      "ds_read_b128 v[176:179], v39 offset:9216\n"
      "s_waitcnt lgkmcnt(14)\n"
      "v_mfma_f32_16x16x32_bf16 v[40:43], v[200:203], v[72:75], v[40:43]\n"
      "ds_read_b128 v[180:183], v39 offset:10240\n"
      "v_mfma_f32_16x16x32_bf16 v[44:47], v[204:207], v[72:75], v[44:47]\n"
      "ds_read_b128 v[184:187], v39 offset:11264\n"
      "v_mfma_f32_16x16x32_bf16 v[48:51], v[208:211], v[72:75], v[48:51]\n"
      "ds_read_b128 v[188:191], v39 offset:12288\n"
      "v_mfma_f32_16x16x32_bf16 v[52:55], v[212:215], v[72:75], v[52:55]\n"
      "ds_read_b128 v[192:195], v39 offset:13312\n"
      "v_mfma_f32_16x16x32_bf16 v[56:59], v[216:219], v[72:75], v[56:59]\n"
      "ds_read_b128 v[196:199], v39 offset:14336\n"
      "s_waitcnt lgkmcnt(14)\n"
      "v_mfma_f32_16x16x32_bf16 v[40:43], v[220:223], v[76:79], v[40:43]\n"
      "ds_read_b128 v[200:203], v39 offset:15360\n"
      "v_mfma_f32_16x16x32_bf16 v[44:47], v[224:227], v[76:79], v[44:47]\n"
      "ds_read_b128 v[204:207], v39 offset:16384\n"
      "v_mfma_f32_16x16x32_bf16 v[48:51], v[228:231], v[76:79], v[48:51]\n"
      "ds_read_b128 v[208:211], v39 offset:17408\n"
      "v_mfma_f32_16x16x32_bf16 v[52:55], v[232:235], v[76:79], v[52:55]\n"
      "ds_read_b128 v[212:215], v39 offset:18432\n"
      "v_mfma_f32_16x16x32_bf16 v[56:59], v[236:239], v[76:79], v[56:59]\n"
      "ds_read_b128 v[216:219], v39 offset:19456\n"
      "ds_read_b128 v[220:223], v39 offset:20480\n"
      "ds_read_b128 v[224:227], v39 offset:21504\n"
      "ds_read_b128 v[228:231], v39 offset:22528\n"
      "ds_read_b128 v[232:235], v39 offset:23552\n"
      "ds_read_b128 v[236:239], v39 offset:24576\n"
      "50:\n"
      "s_bitcmp0_b32 s95, 4\n"
      "s_cbranch_scc1 55f\n"
      "s_sub_u32 s66, s66, 1\n"
      "s_waitcnt vmcnt(11)\n"
      "s_cmp_eq_u32 s66, 2\n"
      "s_cbranch_scc1 56f\n"
      "s_waitcnt vmcnt(6)\n"
      "s_cmp_eq_u32 s66, 1\n"
      "s_cbranch_scc1 56f\n"
      "s_waitcnt vmcnt(1)\n"
      "56:\n"
      "s_waitcnt lgkmcnt(14)\n"
      "v_mfma_f32_16x16x32_bf16 v[240:243], v[140:143], v[80:83], 0\n"
      "s_bitset0_b32 s95, 4\n"
      "s_cmp_lg_u32 s95, 0\n"
      "v_mfma_f32_16x16x32_bf16 v[244:247], v[144:147], v[80:83], 0\n"
      "s_cselect_b32 s90, s95, s92\n"
      "s_or_b32 s90, s90, 0x10000\n"
      "v_mfma_f32_16x16x32_bf16 v[248:251], v[148:151], v[80:83], 0\n"
      "s_ff1_i32_b32 s67, s90\n"
      "s_and_b32 s67, s67, 15\n"
      "v_mfma_f32_16x16x32_bf16 v[252:255], v[152:155], v[80:83], 0\n"
      "s_mul_i32 s90, s67, 6400\n"
      "s_cmp_eq_u32 s67, 12\n"
      "v_mfma_f32_16x16x32_bf16 v[8:11], v[156:159], v[80:83], 0\n"
      "s_cselect_b32 s90, 136192, s90\n"
      "v_add_u32 v39, s90, v24\n"
      "v_mfma_f32_16x16x32_bf16 v[240:243], v[160:163], v[84:87], v[240:243]\n"
      "ds_read_b128 v[140:143], v39 offset:0\n"
      "v_mfma_f32_16x16x32_bf16 v[244:247], v[164:167], v[84:87], v[244:247]\n"
      "ds_read_b128 v[144:147], v39 offset:1024\n"
      "v_mfma_f32_16x16x32_bf16 v[248:251], v[168:171], v[84:87], v[248:251]\n"
      "ds_read_b128 v[148:151], v39 offset:2048\n"
      "v_mfma_f32_16x16x32_bf16 v[252:255], v[172:175], v[84:87], v[252:255]\n"
      "ds_read_b128 v[152:155], v39 offset:3072\n"
      "v_mfma_f32_16x16x32_bf16 v[8:11], v[176:179], v[84:87], v[8:11]\n"
      "ds_read_b128 v[156:159], v39 offset:4096\n"
      "s_waitcnt lgkmcnt(14)\n"
      "v_mfma_f32_16x16x32_bf16 v[240:243], v[180:183], v[88:91], v[240:243]\n"
      "ds_read_b128 v[160:163], v39 offset:5120\n"
      "v_mfma_f32_16x16x32_bf16 v[244:247], v[184:187], v[88:91], v[244:247]\n"
      "ds_read_b128 v[164:167], v39 offset:6144\n"
      "v_mfma_f32_16x16x32_bf16 v[248:251], v[188:191], v[88:91], v[248:251]\n"
      "ds_read_b128 v[168:171], v39 offset:7168\n"
      "v_mfma_f32_16x16x32_bf16 v[252:255], v[192:195], v[88:91], v[252:255]\n"
      "ds_read_b128 v[172:175], v39 offset:8192\n"
      "v_mfma_f32_16x16x32_bf16 v[8:11], v[196:199], v[88:91], v[8:11]\n"
      "ds_read_b128 v[176:179], v39 offset:9216\n"
      "s_waitcnt lgkmcnt(14)\n"
      "v_mfma_f32_16x16x32_bf16 v[240:243], v[200:203], v[92:95], v[240:243]\n"
      "ds_read_b128 v[180:183], v39 offset:10240\n"
      "v_mfma_f32_16x16x32_bf16 v[244:247], v[204:207], v[92:95], v[244:247]\n"
      "ds_read_b128 v[184:187], v39 offset:11264\n"
      "v_mfma_f32_16x16x32_bf16 v[248:251], v[208:211], v[92:95], v[248:251]\n"
      "ds_read_b128 v[188:191], v39 offset:12288\n"
      "v_mfma_f32_16x16x32_bf16 v[252:255], v[212:215], v[92:95], v[252:255]\n"
      "ds_read_b128 v[192:195], v39 offset:13312\n"
      "v_mfma_f32_16x16x32_bf16 v[8:11], v[216:219], v[92:95], v[8:11]\n"
      "ds_read_b128 v[196:199], v39 offset:14336\n"
      "s_waitcnt lgkmcnt(14)\n"
      "v_mfma_f32_16x16x32_bf16 v[80:83], v[220:223], v[96:99], v[240:243]\n"
      "ds_read_b128 v[200:203], v39 offset:15360\n"
      "v_mfma_f32_16x16x32_bf16 v[84:87], v[224:227], v[96:99], v[244:247]\n"
      "ds_read_b128 v[204:207], v39 offset:16384\n"
      "v_mfma_f32_16x16x32_bf16 v[88:91], v[228:231], v[96:99], v[248:251]\n"
      "ds_read_b128 v[208:211], v39 offset:17408\n"
      "v_mfma_f32_16x16x32_bf16 v[92:95], v[232:235], v[96:99], v[252:255]\n"
      "ds_read_b128 v[212:215], v39 offset:18432\n"
      "v_mfma_f32_16x16x32_bf16 v[8:11], v[236:239], v[96:99], v[8:11]\n"
      "ds_read_b128 v[216:219], v39 offset:19456\n"
      "ds_read_b128 v[220:223], v39 offset:20480\n"
      "ds_read_b128 v[224:227], v39 offset:21504\n"
      "ds_read_b128 v[228:231], v39 offset:22528\n"
      "ds_read_b128 v[232:235], v39 offset:23552\n"
      "ds_read_b128 v[236:239], v39 offset:24576\n"
      "55:\n"
      "s_bitcmp0_b32 s95, 8\n"
      "s_cbranch_scc1 60f\n"
      "s_sub_u32 s66, s66, 1\n"
      "s_waitcnt vmcnt(6)\n"
      "s_cmp_eq_u32 s66, 1\n"
      "s_cbranch_scc1 61f\n"
      "s_waitcnt vmcnt(1)\n"
      "61:\n"
      "s_waitcnt lgkmcnt(14)\n"
      "v_mfma_f32_16x16x32_bf16 v[240:243], v[140:143], v[100:103], 0\n"
      "s_bitset0_b32 s95, 8\n"
      "s_cmp_lg_u32 s95, 0\n"
      "v_mfma_f32_16x16x32_bf16 v[244:247], v[144:147], v[100:103], 0\n"
      "s_cselect_b32 s90, s95, s92\n"
      "s_or_b32 s90, s90, 0x10000\n"
      "v_mfma_f32_16x16x32_bf16 v[248:251], v[148:151], v[100:103], 0\n"
      "s_ff1_i32_b32 s67, s90\n"
      "s_and_b32 s67, s67, 15\n"
      "v_mfma_f32_16x16x32_bf16 v[252:255], v[152:155], v[100:103], 0\n"
      "s_mul_i32 s90, s67, 6400\n"
      "s_cmp_eq_u32 s67, 12\n"
      "v_mfma_f32_16x16x32_bf16 v[12:15], v[156:159], v[100:103], 0\n"
      "s_cselect_b32 s90, 136192, s90\n"
      "v_add_u32 v39, s90, v24\n"
      "v_mfma_f32_16x16x32_bf16 v[240:243], v[160:163], v[104:107], v[240:243]\n"
      "ds_read_b128 v[140:143], v39 offset:0\n"
      "v_mfma_f32_16x16x32_bf16 v[244:247], v[164:167], v[104:107], v[244:247]\n"
      "ds_read_b128 v[144:147], v39 offset:1024\n"
      "v_mfma_f32_16x16x32_bf16 v[248:251], v[168:171], v[104:107], v[248:251]\n"
      "ds_read_b128 v[148:151], v39 offset:2048\n"
      "v_mfma_f32_16x16x32_bf16 v[252:255], v[172:175], v[104:107], v[252:255]\n"
      "ds_read_b128 v[152:155], v39 offset:3072\n"
      "v_mfma_f32_16x16x32_bf16 v[12:15], v[176:179], v[104:107], v[12:15]\n"
      "ds_read_b128 v[156:159], v39 offset:4096\n"
      "s_waitcnt lgkmcnt(14)\n"
      "v_mfma_f32_16x16x32_bf16 v[240:243], v[180:183], v[108:111], v[240:243]\n"
      "ds_read_b128 v[160:163], v39 offset:5120\n"
      "v_mfma_f32_16x16x32_bf16 v[244:247], v[184:187], v[108:111], v[244:247]\n"
      "ds_read_b128 v[164:167], v39 offset:6144\n"
      "v_mfma_f32_16x16x32_bf16 v[248:251], v[188:191], v[108:111], v[248:251]\n"
      "ds_read_b128 v[168:171], v39 offset:7168\n"
      "v_mfma_f32_16x16x32_bf16 v[252:255], v[192:195], v[108:111], v[252:255]\n"
      "ds_read_b128 v[172:175], v39 offset:8192\n"
      "v_mfma_f32_16x16x32_bf16 v[12:15], v[196:199], v[108:111], v[12:15]\n"
      "ds_read_b128 v[176:179], v39 offset:9216\n"
      "s_waitcnt lgkmcnt(14)\n"
      "v_mfma_f32_16x16x32_bf16 v[240:243], v[200:203], v[112:115], v[240:243]\n"
      "ds_read_b128 v[180:183], v39 offset:10240\n"
      "v_mfma_f32_16x16x32_bf16 v[244:247], v[204:207], v[112:115], v[244:247]\n"
      "ds_read_b128 v[184:187], v39 offset:11264\n"
      "v_mfma_f32_16x16x32_bf16 v[248:251], v[208:211], v[112:115], v[248:251]\n"
      "ds_read_b128 v[188:191], v39 offset:12288\n"
      "v_mfma_f32_16x16x32_bf16 v[252:255], v[212:215], v[112:115], v[252:255]\n"
      "ds_read_b128 v[192:195], v39 offset:13312\n"
      "v_mfma_f32_16x16x32_bf16 v[12:15], v[216:219], v[112:115], v[12:15]\n"
      "ds_read_b128 v[196:199], v39 offset:14336\n"
      "s_waitcnt lgkmcnt(14)\n"
      "v_mfma_f32_16x16x32_bf16 v[100:103], v[220:223], v[116:119], v[240:243]\n"
      "ds_read_b128 v[200:203], v39 offset:15360\n"
      "v_mfma_f32_16x16x32_bf16 v[104:107], v[224:227], v[116:119], v[244:247]\n"
      "ds_read_b128 v[204:207], v39 offset:16384\n"
      "v_mfma_f32_16x16x32_bf16 v[108:111], v[228:231], v[116:119], v[248:251]\n"
      "ds_read_b128 v[208:211], v39 offset:17408\n"
      "v_mfma_f32_16x16x32_bf16 v[112:115], v[232:235], v[116:119], v[252:255]\n"
      "ds_read_b128 v[212:215], v39 offset:18432\n"
      "v_mfma_f32_16x16x32_bf16 v[12:15], v[236:239], v[116:119], v[12:15]\n"
      "ds_read_b128 v[216:219], v39 offset:19456\n"
      "ds_read_b128 v[220:223], v39 offset:20480\n"
      "ds_read_b128 v[224:227], v39 offset:21504\n"
      "ds_read_b128 v[228:231], v39 offset:22528\n"
      "ds_read_b128 v[232:235], v39 offset:23552\n"
      "ds_read_b128 v[236:239], v39 offset:24576\n"
      "60:\n"
      "s_bitcmp0_b32 s95, 12\n"
      "s_cbranch_scc1 65f\n"
      "s_sub_u32 s66, s66, 1\n"
      "s_waitcnt vmcnt(1)\n"
      "66:\n"
      "s_waitcnt lgkmcnt(14)\n"
      "v_mfma_f32_16x16x32_bf16 v[240:243], v[140:143], v[120:123], 0\n"
      "s_bitset0_b32 s95, 12\n"
      "s_cmp_lg_u32 s95, 0\n"
      "v_mfma_f32_16x16x32_bf16 v[244:247], v[144:147], v[120:123], 0\n"
      "s_cselect_b32 s90, s95, s92\n"
      "s_or_b32 s90, s90, 0x10000\n"
      "v_mfma_f32_16x16x32_bf16 v[248:251], v[148:151], v[120:123], 0\n"
      "s_ff1_i32_b32 s67, s90\n"
      "s_and_b32 s67, s67, 15\n"
      "v_mfma_f32_16x16x32_bf16 v[252:255], v[152:155], v[120:123], 0\n"
      "s_mul_i32 s90, s67, 6400\n"
      "s_cmp_eq_u32 s67, 12\n"
      "v_mfma_f32_16x16x32_bf16 v[16:19], v[156:159], v[120:123], 0\n"
      "s_cselect_b32 s90, 136192, s90\n"
      "v_add_u32 v39, s90, v24\n"
      "v_mfma_f32_16x16x32_bf16 v[240:243], v[160:163], v[124:127], v[240:243]\n"
      "ds_read_b128 v[140:143], v39 offset:0\n"
      "v_mfma_f32_16x16x32_bf16 v[244:247], v[164:167], v[124:127], v[244:247]\n"
      "ds_read_b128 v[144:147], v39 offset:1024\n"
      "v_mfma_f32_16x16x32_bf16 v[248:251], v[168:171], v[124:127], v[248:251]\n"
      "ds_read_b128 v[148:151], v39 offset:2048\n"
      "v_mfma_f32_16x16x32_bf16 v[252:255], v[172:175], v[124:127], v[252:255]\n"
      "ds_read_b128 v[152:155], v39 offset:3072\n"
      "v_mfma_f32_16x16x32_bf16 v[16:19], v[176:179], v[124:127], v[16:19]\n"
      "ds_read_b128 v[156:159], v39 offset:4096\n"
      "s_waitcnt lgkmcnt(14)\n"
      "v_mfma_f32_16x16x32_bf16 v[240:243], v[180:183], v[128:131], v[240:243]\n"
      "ds_read_b128 v[160:163], v39 offset:5120\n"
      "v_mfma_f32_16x16x32_bf16 v[244:247], v[184:187], v[128:131], v[244:247]\n"
      "ds_read_b128 v[164:167], v39 offset:6144\n"
      "v_mfma_f32_16x16x32_bf16 v[248:251], v[188:191], v[128:131], v[248:251]\n"
      "ds_read_b128 v[168:171], v39 offset:7168\n"
      "v_mfma_f32_16x16x32_bf16 v[252:255], v[192:195], v[128:131], v[252:255]\n"
      "ds_read_b128 v[172:175], v39 offset:8192\n"
      "v_mfma_f32_16x16x32_bf16 v[16:19], v[196:199], v[128:131], v[16:19]\n"
      "ds_read_b128 v[176:179], v39 offset:9216\n"
      "s_waitcnt lgkmcnt(14)\n"
      "v_mfma_f32_16x16x32_bf16 v[240:243], v[200:203], v[132:135], v[240:243]\n"
      "ds_read_b128 v[180:183], v39 offset:10240\n"
      "v_mfma_f32_16x16x32_bf16 v[244:247], v[204:207], v[132:135], v[244:247]\n"
      "ds_read_b128 v[184:187], v39 offset:11264\n"
      "v_mfma_f32_16x16x32_bf16 v[248:251], v[208:211], v[132:135], v[248:251]\n"
      "ds_read_b128 v[188:191], v39 offset:12288\n"
      "v_mfma_f32_16x16x32_bf16 v[252:255], v[212:215], v[132:135], v[252:255]\n"
      "ds_read_b128 v[192:195], v39 offset:13312\n"
      "v_mfma_f32_16x16x32_bf16 v[16:19], v[216:219], v[132:135], v[16:19]\n"
      "ds_read_b128 v[196:199], v39 offset:14336\n"
      "s_waitcnt lgkmcnt(14)\n"
      "v_mfma_f32_16x16x32_bf16 v[120:123], v[220:223], v[136:139], v[240:243]\n"
      "ds_read_b128 v[200:203], v39 offset:15360\n"
      "v_mfma_f32_16x16x32_bf16 v[124:127], v[224:227], v[136:139], v[244:247]\n"
      "ds_read_b128 v[204:207], v39 offset:16384\n"
      "v_mfma_f32_16x16x32_bf16 v[128:131], v[228:231], v[136:139], v[248:251]\n"
      "ds_read_b128 v[208:211], v39 offset:17408\n"
      "v_mfma_f32_16x16x32_bf16 v[132:135], v[232:235], v[136:139], v[252:255]\n"
      "ds_read_b128 v[212:215], v39 offset:18432\n"
      "v_mfma_f32_16x16x32_bf16 v[16:19], v[236:239], v[136:139], v[16:19]\n"
      "ds_read_b128 v[216:219], v39 offset:19456\n"
      "ds_read_b128 v[220:223], v39 offset:20480\n"
      "ds_read_b128 v[224:227], v39 offset:21504\n"
      "ds_read_b128 v[228:231], v39 offset:22528\n"
      "ds_read_b128 v[232:235], v39 offset:23552\n"
      "ds_read_b128 v[236:239], v39 offset:24576\n"
      "65:\n"
      "s_cmp_lg_u32 s92, 0\n"
      "s_cbranch_scc1 40b\n"
      "s_mov_b32 s91, 0x100000\n"
      "153:\n"
      "ds_read_b32 v20, v33 offset:32\n"
      "s_sub_u32 s91, s91, 1\n"
      "s_cmp_eq_u32 s91, 0\n"
      "s_cbranch_scc1 154f\n"
      "s_waitcnt lgkmcnt(0)\n"
      "v_readfirstlane_b32 s90, v20\n"
      "s_cmp_lt_u32 s90, 1\n"
      "s_cbranch_scc1 153b\n"
      "154:\n"
      "ds_read_b128 v[60:63], v32 offset:0\n"
      "ds_read_b128 v[64:67], v32 offset:1024\n"
      "ds_read_b128 v[68:71], v32 offset:2048\n"
      "ds_read_b128 v[72:75], v32 offset:3072\n"
      "ds_read_b128 v[76:79], v32 offset:4096\n"
      "v_pk_add_f32 v[40:41], v[40:41], v[80:81]\n"
      "v_pk_add_f32 v[42:43], v[42:43], v[82:83]\n"
      "v_pk_add_f32 v[44:45], v[44:45], v[84:85]\n"
      "v_pk_add_f32 v[46:47], v[46:47], v[86:87]\n"
      "v_pk_add_f32 v[48:49], v[48:49], v[88:89]\n"
      "v_pk_add_f32 v[50:51], v[50:51], v[90:91]\n"
      "v_pk_add_f32 v[52:53], v[52:53], v[92:93]\n"
      "v_pk_add_f32 v[54:55], v[54:55], v[94:95]\n"
      "v_pk_add_f32 v[56:57], v[56:57], v[8:9]\n"
      "v_pk_add_f32 v[58:59], v[58:59], v[10:11]\n"
      "v_pk_add_f32 v[40:41], v[40:41], v[100:101]\n"
      "v_pk_add_f32 v[42:43], v[42:43], v[102:103]\n"
      "v_pk_add_f32 v[44:45], v[44:45], v[104:105]\n"
      "v_pk_add_f32 v[46:47], v[46:47], v[106:107]\n"
      "v_pk_add_f32 v[48:49], v[48:49], v[108:109]\n"
      "v_pk_add_f32 v[50:51], v[50:51], v[110:111]\n"
      "v_pk_add_f32 v[52:53], v[52:53], v[112:113]\n"
      "v_pk_add_f32 v[54:55], v[54:55], v[114:115]\n"
      "v_pk_add_f32 v[56:57], v[56:57], v[12:13]\n"
      "v_pk_add_f32 v[58:59], v[58:59], v[14:15]\n"
      "v_pk_add_f32 v[40:41], v[40:41], v[120:121]\n"
      "v_pk_add_f32 v[42:43], v[42:43], v[122:123]\n"
      "v_pk_add_f32 v[44:45], v[44:45], v[124:125]\n"
      "v_pk_add_f32 v[46:47], v[46:47], v[126:127]\n"
      "v_pk_add_f32 v[48:49], v[48:49], v[128:129]\n"
      "v_pk_add_f32 v[50:51], v[50:51], v[130:131]\n"
      "v_pk_add_f32 v[52:53], v[52:53], v[132:133]\n"
      "v_pk_add_f32 v[54:55], v[54:55], v[134:135]\n"
      "v_pk_add_f32 v[56:57], v[56:57], v[16:17]\n"
      "v_pk_add_f32 v[58:59], v[58:59], v[18:19]\n"
      "s_waitcnt lgkmcnt(0)\n"
      "v_pk_add_f32 v[40:41], v[40:41], v[60:61]\n"
      "v_pk_add_f32 v[42:43], v[42:43], v[62:63]\n"
      "v_pk_add_f32 v[44:45], v[44:45], v[64:65]\n"
      "v_pk_add_f32 v[46:47], v[46:47], v[66:67]\n"
      "v_pk_add_f32 v[48:49], v[48:49], v[68:69]\n"
      "v_pk_add_f32 v[50:51], v[50:51], v[70:71]\n"
      "v_pk_add_f32 v[52:53], v[52:53], v[72:73]\n"
      "v_pk_add_f32 v[54:55], v[54:55], v[74:75]\n"
      "v_pk_add_f32 v[56:57], v[56:57], v[76:77]\n"
      "v_pk_add_f32 v[58:59], v[58:59], v[78:79]\n"
      "v_cvt_pk_bf16_f32 v80, v40, v41\n"
      "v_cvt_pk_bf16_f32 v81, v42, v43\n"
      "v_cvt_pk_bf16_f32 v82, v44, v45\n"
      "v_cvt_pk_bf16_f32 v83, v46, v47\n"
      "v_cvt_pk_bf16_f32 v84, v48, v49\n"
      "v_cvt_pk_bf16_f32 v85, v50, v51\n"
      "v_cvt_pk_bf16_f32 v86, v52, v53\n"
      "v_cvt_pk_bf16_f32 v87, v54, v55\n"
      "v_cvt_pk_bf16_f32 v88, v56, v57\n"
      "v_cvt_pk_bf16_f32 v89, v58, v59\n"
      "global_store_dwordx4 v36, v[80:83], s[70:71]\n"
      "global_store_dwordx4 v36, v[84:87], s[70:71] offset:16\n"
      "global_store_dwordx2 v36, v[88:89], s[70:71] offset:32\n"
      "s_waitcnt vmcnt(3)\n"
          : "+{v[24:39]}"(BV), "+{s[60:67]}"(BS)
          : "{s[44:47]}"(mk_srd(ws + WS_ACT + (u32)(g3 * ACT_G), ACT_G)),
            "{s[68:71]}"(u32x4{(u32)fl, (u32)(fl >> 32), (u32)(u64)out, (u32)((u64)out >> 32)}),
            "{s[72:73]}"(ws + WS_OFLAG + (u32)((g1 * 8 + j1) * 64 + w * 4)), "{s[74:75]}"(x)
          : "memory", "scc", "vcc", "s90", "s91", "s92", "s93", "s94", "s95", "v8", "v9", "v10", "v11", "v12", "v13", "v14", "v15", "v16", "v17", "v18", "v19", "v20", "v21", "v22", "v23", "v40", "v41", "v42", "v43", "v44", "v45", "v46", "v47", "v48", "v49", "v50", "v51", "v52", "v53", "v54", "v55", "v56", "v57", "v58", "v59", "v60", "v61", "v62", "v63", "v64", "v65", "v66", "v67", "v68", "v69", "v70", "v71", "v72", "v73", "v74", "v75", "v76", "v77", "v78", "v79", "v80", "v81", "v82", "v83", "v84", "v85", "v86", "v87", "v88", "v89", "v90", "v91", "v92", "v93", "v94", "v95", "v96", "v97", "v98", "v99", "v100", "v101", "v102", "v103", "v104", "v105", "v106", "v107", "v108", "v109", "v110", "v111", "v112", "v113", "v114", "v115", "v116", "v117", "v118", "v119", "v120", "v121", "v122", "v123", "v124", "v125", "v126", "v127", "v128", "v129", "v130", "v131", "v132", "v133", "v134", "v135", "v136", "v137", "v138", "v139", "v140", "v141", "v142", "v143", "v144", "v145", "v146", "v147", "v148", "v149", "v150", "v151", "v152", "v153", "v154", "v155", "v156", "v157", "v158", "v159", "v160", "v161", "v162", "v163", "v164", "v165", "v166", "v167", "v168", "v169", "v170", "v171", "v172", "v173", "v174", "v175", "v176", "v177", "v178", "v179", "v180", "v181", "v182", "v183", "v184", "v185", "v186", "v187", "v188", "v189", "v190", "v191", "v192", "v193", "v194", "v195", "v196", "v197", "v198", "v199", "v200", "v201", "v202", "v203", "v204", "v205", "v206", "v207", "v208", "v209", "v210", "v211", "v212", "v213", "v214", "v215", "v216", "v217", "v218", "v219", "v220", "v221", "v222", "v223", "v224", "v225", "v226", "v227", "v228", "v229", "v230", "v231", "v232", "v233", "v234", "v235", "v236", "v237", "v238", "v239", "v240", "v241", "v242", "v243", "v244", "v245", "v246", "v247", "v248", "v249", "v250", "v251", "v252", "v253", "v254", "v255");
      if (bid == 0 && w == 0) st4_sc1(ws + WS_G, tag);
    }
  }
}

extern "C" void kernel_launch(void* const* d_in, const int* in_sizes, int n_in, void* d_out, int out_size, void* d_ws, size_t ws_size,
                              hipStream_t stream) {
  (void)hipFuncSetAttribute((const void*)fused_kernel, hipFuncAttributeMaxDynamicSharedMemorySize, LDS_BYTES);
  fused_kernel<<<256, 512, LDS_BYTES, stream>>>((const char*)d_in[0], (const char*)d_in[1], (const char*)d_in[2], (const char*)d_in[3],
                                                (const char*)d_in[4], (const char*)d_in[6], (const char*)d_in[7], (const char*)d_in[8],
                                                (char*)d_out, (char*)d_ws);
}
```
